```python
import torch

def _softmax(x, axis=-1):
    return torch.softmax(x, dim=axis)
def _gelu(x):
    return torch.nn.functional.gelu(x, approximate="tanh")
def _silu(x):
    return torch.nn.functional.silu(x)
def _sigmoid(x):
    return torch.sigmoid(x)
def _layernorm(x, axis=-1, eps=1e-5):
    m = x.mean(dim=axis, keepdim=True)
    v = x.var(dim=axis, keepdim=True, unbiased=False)
    return (x - m) / torch.sqrt(v + eps)
def _scatter_add(tgt, idx, src, axis=0):
    return tgt.clone().index_add_(axis, idx, src)
def _segment_sum(src, seg, n):
    out = torch.zeros((n,) + src.shape[1:], dtype=src.dtype)
    return out.index_add_(0, seg, src)
def _pad(x, pw):
    flat = tuple(v for pair in reversed(pw) for v in pair)
    return torch.nn.functional.pad(x, flat)

kernel_name = 'gated_mix__compressed_kv_cla__ssm_conv_h3__prompt'

def setup_inputs():

    _w = (lambda a: (lambda t: t.to(torch.bfloat16) if t.is_floating_point() else t)(torch.as_tensor(a)))
    def _A_setup():
        B, H, Sq, Sk, D, M = 1, 32, 2048, 2048, 128, 640
        Dc = 4 * D
        return {
            "x": _w((lambda *s: torch.randn(*s, dtype=torch.bfloat16) * 0.05)(B, Sq, M)),
            "c_kv": _w((lambda *s: torch.randn(*s, dtype=torch.bfloat16) * 0.4)(B, Sk, Dc)),
            "w_q": _w((lambda *s: torch.randn(*s, dtype=torch.bfloat16) * 6.4)(M, H, D) * (M ** -0.5)),
            "w_uk": _w((lambda *s: torch.randn(*s, dtype=torch.bfloat16) * 6.4)(Dc, H, D) * (Dc ** -0.5)),
            "w_uv": _w((lambda *s: torch.randn(*s, dtype=torch.bfloat16) * 0.4)(Dc, H, D) * (Dc ** -0.5)),
            "w_o": _w((lambda *s: torch.randn(*s, dtype=torch.bfloat16) * 0.4)(H, D, M) * ((H * D) ** -0.5)),
        }
    _A_in = _A_setup()

    def _B_setup():
        B, S, D = 1, 2048, 640
        ks = 4
        return {
            "q": _w((lambda *s: torch.randn(*s, dtype=torch.bfloat16) * 0.05)(B, S, D)),
            "k": _w((lambda *s: torch.randn(*s, dtype=torch.bfloat16) * 0.4)(B, S, D)),
            "v": _w((lambda *s: torch.randn(*s, dtype=torch.bfloat16) * 0.4)(B, S, D)),
            "shift_k": _w((lambda *s: torch.randn(*s, dtype=torch.bfloat16) * 0.4)(D, ks) * 0.5),
            "ssm_k": _w((lambda *s: torch.randn(*s, dtype=torch.bfloat16) * 0.8)(D, S) * (1.0 / S ** 0.5)),
            "ssm_D": _w((lambda *s: torch.randn(*s, dtype=torch.bfloat16) * 0.4)(D)),
        }
    _B_in = _B_setup()

    return {
        "A_x": _A_in["x"],
        "A_c_kv": _A_in["c_kv"],
        "A_w_q": _A_in["w_q"],
        "A_w_uk": _A_in["w_uk"],
        "A_w_uv": _A_in["w_uv"],
        "A_w_o": _A_in["w_o"],
        "B_q": _B_in["q"],
        "B_k": _B_in["k"],
        "B_v": _B_in["v"],
        "B_shift_k": _B_in["shift_k"],
        "B_ssm_k": _B_in["ssm_k"],
        "B_ssm_D": _B_in["ssm_D"],
        "gate_w": _w((lambda *s: torch.randn(*s, dtype=torch.bfloat16) * 0.05)(640) * 16.0),
    }

def reference(A_x, A_c_kv, A_w_q, A_w_uk, A_w_uv, A_w_o, B_q, B_k, B_v, B_shift_k, B_ssm_k, B_ssm_D, gate_w):
    def _A_ref(x, c_kv, w_q, w_uk, w_uv, w_o):
        B, Sq, M = x.shape[0], x.shape[1], x.shape[2]
        Sk = c_kv.shape[1]
        H, D = w_q.shape[1], w_q.shape[2]
        q = torch.einsum("bqm,mhd->bhqd", x, w_q)
        k = torch.einsum("bkc,chd->bhkd", c_kv, w_uk)
        v = torch.einsum("bkc,chd->bhkd", c_kv, w_uv)
        scores = torch.einsum("bhqd,bhkd->bhqk", q, k) * (D ** -0.5)
        off = Sk - Sq
        qi = torch.arange(Sq)[:, None]
        kj = torch.arange(Sk)[None, :]
        cmask = kj <= (qi + off)
        scores = torch.where(cmask[None, None], scores, -1e9)
        p = _softmax(scores, axis=-1)
        ctx = torch.einsum("bhqk,bhkd->bhqd", p, v)
        return torch.einsum("bhqd,hdm->bqm", ctx, w_o)

    def _B_ref(q, k, v, shift_k, ssm_k, ssm_D):
        B, S, D = q.shape
        ks = shift_k.shape[-1]
        k_pad = _pad(k, ((0, 0), (ks - 1, 0), (0, 0)))
        k_shift = torch.zeros_like(k)
        for t in range(ks):
            k_shift = k_shift + k_pad[:, t:t + S, :] * shift_k[:, ks - 1 - t][None, None, :]
        kv = k_shift * v
        L = 2 * S
        kv_pad = _pad(kv, ((0, 0), (0, S), (0, 0)))
        kf_pad = _pad(ssm_k, ((0, 0), (0, S)))
        kv_f = (lambda x, axes=(-2,-1): torch.fft.rfft2(x.float(), dim=axes))(kv_pad[:, None, :, :], axes=(1, 2))
        kf_f = (lambda x, axes=(-2,-1): torch.fft.rfft2(x.float(), dim=axes))(kf_pad[:, None, :], axes=(1, 2))
        kf_f = (lambda x, *d: x.permute(*d))(kf_f, 1, 2, 0)
        y_f = kv_f * kf_f[None, :, :, :]
        y = (lambda x, s, axes=(-2,-1): torch.fft.irfft2(x, s=s, dim=axes).to(torch.bfloat16))(y_f, (1, L), axes=(1, 2))[:, 0, :S, :]
        y = y + kv * ssm_D[None, None, :]
        return q * y

    x = A_x
    g = _sigmoid((x * gate_w).sum(-1, keepdims=True))
    a = _A_ref(x, A_c_kv, A_w_q, A_w_uk, A_w_uv, A_w_o)
    b = _B_ref(x, B_k, B_v, B_shift_k, B_ssm_k, B_ssm_D)
    return g * a + (1 - g) * b
```

```cpp
#include <hip/hip_runtime.h>
#include <hip/hip_bf16.h>
#include <stdint.h>

namespace nv {
typedef unsigned short u16;
__device__ __forceinline__ float bf2f(u16 b) { return __uint_as_float(((unsigned)b) << 16); }
__device__ __forceinline__ u16 f2bf(float f) { unsigned u = __float_as_uint(f); u += 0x7FFFu + ((u >> 16) & 1u); return (u16)(u >> 16); }
__device__ __forceinline__ float rbf(float f) { return bf2f(f2bf(f)); }

constexpr int S = 2048, H = 32, D = 128, M = 640, DC = 512, HD = H * D;

__global__ void gemm_nv(const u16* A, const u16* B, u16* C, int Mr, int N, int K) {
    __shared__ float As[16][65], Bs[16][65];
    int tx = threadIdx.x & 15, ty = threadIdx.x >> 4;
    int r0 = blockIdx.y * 64, c0 = blockIdx.x * 64;
    float acc[4][4] = {};
    for (int k0 = 0; k0 < K; k0 += 16) {
        for (int i = threadIdx.x; i < 16 * 64; i += 256) {
            int kk = i & 15, rr = i >> 4;
            As[kk][rr] = bf2f(A[(size_t)(r0 + rr) * K + k0 + kk]);
            int cc = i & 63, k2 = i >> 6;
            Bs[k2][cc] = bf2f(B[(size_t)(k0 + k2) * N + c0 + cc]);
        }
        __syncthreads();
        for (int kk = 0; kk < 16; ++kk) {
            float a[4], b[4];
            for (int i = 0; i < 4; ++i) a[i] = As[kk][ty * 4 + i];
            for (int j = 0; j < 4; ++j) b[j] = Bs[kk][tx * 4 + j];
            for (int i = 0; i < 4; ++i) for (int j = 0; j < 4; ++j) acc[i][j] += a[i] * b[j];
        }
        __syncthreads();
    }
    for (int i = 0; i < 4; ++i) for (int j = 0; j < 4; ++j)
        C[(size_t)(r0 + ty * 4 + i) * N + c0 + tx * 4 + j] = f2bf(acc[i][j]);
}

__global__ void attn_nv(const u16* Q, const u16* K, const u16* V, u16* ctx) {
    __shared__ float sc[S];
    __shared__ float qs[D];
    __shared__ float red[128];
    int qi = blockIdx.x, h = blockIdx.y, t = threadIdx.x;
    qs[t] = bf2f(Q[(size_t)qi * HD + h * D + t]);
    __syncthreads();
    float mx = -1e30f;
    for (int j = t; j <= qi; j += 128) {
        const u16* kr = K + (size_t)j * HD + h * D;
        float s = 0;
        for (int d = 0; d < D; ++d) s += qs[d] * bf2f(kr[d]);
        s = rbf(rbf(s) * rbf(0.08838834764831845f));
        sc[j] = s; mx = fmaxf(mx, s);
    }
    red[t] = mx; __syncthreads();
    for (int o = 64; o > 0; o >>= 1) { if (t < o) red[t] = fmaxf(red[t], red[t + o]); __syncthreads(); }
    mx = red[0]; __syncthreads();
    float sum = 0;
    for (int j = t; j <= qi; j += 128) { float e = __expf(sc[j] - mx); sc[j] = e; sum += e; }
    red[t] = sum; __syncthreads();
    for (int o = 64; o > 0; o >>= 1) { if (t < o) red[t] += red[t + o]; __syncthreads(); }
    sum = red[0]; __syncthreads();
    float inv = 1.f / sum;
    float acc = 0;
    for (int j = 0; j <= qi; ++j) acc += rbf(sc[j] * inv) * bf2f(V[(size_t)j * HD + h * D + t]);
    ctx[(size_t)qi * HD + h * D + t] = f2bf(acc);
}

__global__ void kv_nv(const u16* k, const u16* v, const u16* shift_k, u16* kv) {
    int i = blockIdx.x * 256 + threadIdx.x;
    if (i >= S * M) return;
    int s = i / M, d = i % M;
    float acc = 0.f;
    for (int t = 0; t < 4; ++t) {
        int ss = s + t - 3;
        float kk = ss >= 0 ? bf2f(k[(size_t)ss * M + d]) : 0.f;
        acc = rbf(acc + rbf(kk * bf2f(shift_k[d * 4 + 3 - t])));
    }
    kv[i] = f2bf(acc * bf2f(v[i]));
}
__global__ void conv_nv(const u16* kv, const u16* ssm_k, u16* y) {
    int i = blockIdx.x * 256 + threadIdx.x;
    if (i >= S * M) return;
    int s = i / M, d = i % M;
    float acc = 0.f;
    for (int j = 0; j <= s; ++j) acc += bf2f(kv[(size_t)j * M + d]) * bf2f(ssm_k[(size_t)d * S + s - j]);
    y[i] = f2bf(acc);
}
__global__ void final_nv(const u16* x, const u16* gate_w, const u16* a, const u16* kv, const u16* y, const u16* ssm_D, u16* out) {
    __shared__ float red[256];
    int s = blockIdx.x, t = threadIdx.x;
    float p = 0;
    for (int d = t; d < M; d += 256) p += rbf(bf2f(x[(size_t)s * M + d]) * bf2f(gate_w[d]));
    red[t] = p; __syncthreads();
    for (int o = 128; o > 0; o >>= 1) { if (t < o) red[t] += red[t + o]; __syncthreads(); }
    float z = rbf(red[0]);
    float g = rbf(1.f / (1.f + __expf(-z)));
    float omg = rbf(1.f - g);
    for (int d = t; d < M; d += 256) {
        size_t i = (size_t)s * M + d;
        float yy = rbf(bf2f(y[i]) + rbf(bf2f(kv[i]) * bf2f(ssm_D[d])));
        float b = rbf(bf2f(x[i]) * yy);
        out[i] = f2bf(rbf(g * bf2f(a[i])) + rbf(omg * b));
    }
}

struct Bufs { u16 *q, *k, *v, *ctx, *a, *kv, *y; };
static inline Bufs carve(void* ws) {
    Bufs b; u16* p = (u16*)ws;
    b.q = p; p += (size_t)S * HD; b.k = p; p += (size_t)S * HD; b.v = p; p += (size_t)S * HD; b.ctx = p; p += (size_t)S * HD;
    b.a = p; p += (size_t)S * M; b.kv = p; p += (size_t)S * M; b.y = p; p += (size_t)S * M;
    return b;
}
static inline void run(void* const* d_in, void* d_out, void* d_ws, hipStream_t st) {
    const u16* x = (const u16*)d_in[0]; const u16* ckv = (const u16*)d_in[1]; const u16* wq = (const u16*)d_in[2];
    const u16* wuk = (const u16*)d_in[3]; const u16* wuv = (const u16*)d_in[4]; const u16* wo = (const u16*)d_in[5];
    const u16* bk = (const u16*)d_in[7]; const u16* bv = (const u16*)d_in[8]; const u16* shk = (const u16*)d_in[9];
    const u16* ssk = (const u16*)d_in[10]; const u16* ssd = (const u16*)d_in[11]; const u16* gw = (const u16*)d_in[12];
    Bufs b = carve(d_ws);
    gemm_nv<<<dim3(HD / 64, S / 64), 256, 0, st>>>(x, wq, b.q, S, HD, M);
    gemm_nv<<<dim3(HD / 64, S / 64), 256, 0, st>>>(ckv, wuk, b.k, S, HD, DC);
    gemm_nv<<<dim3(HD / 64, S / 64), 256, 0, st>>>(ckv, wuv, b.v, S, HD, DC);
    attn_nv<<<dim3(S, H), 128, 0, st>>>(b.q, b.k, b.v, b.ctx);
    gemm_nv<<<dim3(M / 64, S / 64), 256, 0, st>>>(b.ctx, wo, b.a, S, M, HD);
    kv_nv<<<(S * M + 255) / 256, 256, 0, st>>>(bk, bv, shk, b.kv);
    conv_nv<<<(S * M + 255) / 256, 256, 0, st>>>(b.kv, ssk, b.y);
    final_nv<<<S, 256, 0, st>>>(x, gw, b.a, b.kv, b.y, ssd, (u16*)d_out);
}
}
extern "C" void kernel_launch(void* const* d_in, const int* in_sizes, int n_in, void* d_out, int out_size, void* d_ws, size_t ws_size, hipStream_t stream) {
    nv::run(d_in, d_out, d_ws, stream);
}
```

```cpp
#include <hip/hip_runtime.h>
#define WS_KF 0u
#define WS_VF 16777216u
#define WS_QF 33554432u
#define WS_CF 50331648u
#define WS_WOF 67108864u
#define WS_KVT 75497472u
#define WS_ZT 79691776u
#define WS_G 83886080u
#define WS_FLAG 84934656u
#define WS_STAMP 85983232u

#define CONV_CORE(la, lb0, lb1, lr) asm volatile( \
    "v_mov_b32 v156, %0\n" \
    "v_mov_b32 v157, %1\n" \
    "v_mov_b32 v158, %2\n" \
    "v_mov_b32 v159, %3\n" \
    "v_mov_b32 v124, 0\n" \
    "v_mov_b32 v125, 0\n" \
    "v_mov_b32 v126, 0\n" \
    "v_mov_b32 v127, 0\n" \
    "v_mov_b32 v128, 0\n" \
    "v_mov_b32 v129, 0\n" \
    "v_mov_b32 v130, 0\n" \
    "v_mov_b32 v131, 0\n" \
    "v_mov_b32 v132, 0\n" \
    "v_mov_b32 v133, 0\n" \
    "v_mov_b32 v134, 0\n" \
    "v_mov_b32 v135, 0\n" \
    "v_mov_b32 v136, 0\n" \
    "v_mov_b32 v137, 0\n" \
    "v_mov_b32 v138, 0\n" \
    "v_mov_b32 v139, 0\n" \
    "v_mov_b32 v140, 0\n" \
    "v_mov_b32 v141, 0\n" \
    "v_mov_b32 v142, 0\n" \
    "v_mov_b32 v143, 0\n" \
    "v_mov_b32 v144, 0\n" \
    "v_mov_b32 v145, 0\n" \
    "v_mov_b32 v146, 0\n" \
    "v_mov_b32 v147, 0\n" \
    "v_mov_b32 v148, 0\n" \
    "v_mov_b32 v149, 0\n" \
    "v_mov_b32 v150, 0\n" \
    "v_mov_b32 v151, 0\n" \
    "v_mov_b32 v152, 0\n" \
    "v_mov_b32 v153, 0\n" \
    "v_mov_b32 v154, 0\n" \
    "v_mov_b32 v155, 0\n" \
    "ds_read2_b32 v[28:29], v156 offset0:0 offset1:1\n" \
    "ds_read2_b32 v[30:31], v156 offset0:2 offset1:3\n" \
    "ds_read_b128 v[60:63], v158\n" \
    "ds_read_b128 v[44:47], v157\n" \
    "ds_read2_b32 v[32:33], v156 offset0:8 offset1:9\n" \
    "ds_read2_b32 v[34:35], v156 offset0:10 offset1:11\n" \
    "ds_read_b128 v[64:67], v158 offset:32\n" \
    "ds_read_b128 v[48:51], v157 offset:32\n" \
    "ds_read2_b32 v[36:37], v156 offset0:16 offset1:17\n" \
    "ds_read2_b32 v[38:39], v156 offset0:18 offset1:19\n" \
    "ds_read_b128 v[68:71], v158 offset:80\n" \
    "ds_read_b128 v[52:55], v157 offset:80\n" \
    "ds_read2_b32 v[40:41], v156 offset0:24 offset1:25\n" \
    "ds_read2_b32 v[42:43], v156 offset0:26 offset1:27\n" \
    "ds_read_b128 v[72:75], v158 offset:112\n" \
    "ds_read_b128 v[56:59], v157 offset:112\n" \
    "s_mov_b32 s56, 0\n" \
    "L_cv0_%=:\n" \
    "s_waitcnt lgkmcnt(0)\n" \
    "v_mfma_f32_32x32x16_bf16 v[140:155], v[28:31], v[60:63], v[140:155]\n" \
    "ds_read2_b32 v[76:77], v156 offset0:32 offset1:33\n" \
    "ds_read2_b32 v[78:79], v156 offset0:34 offset1:35\n" \
    "ds_read_b128 v[108:111], v158 offset:160\n" \
    "v_mfma_f32_32x32x16_bf16 v[124:139], v[28:31], v[44:47], v[124:139]\n" \
    "ds_read_b128 v[92:95], v157 offset:160\n" \
    "ds_read2_b32 v[80:81], v156 offset0:40 offset1:41\n" \
    "ds_read2_b32 v[82:83], v156 offset0:42 offset1:43\n" \
    "v_mfma_f32_32x32x16_bf16 v[140:155], v[32:35], v[64:67], v[140:155]\n" \
    "ds_read_b128 v[112:115], v158 offset:192\n" \
    "ds_read_b128 v[96:99], v157 offset:192\n" \
    "ds_read2_b32 v[84:85], v156 offset0:48 offset1:49\n" \
    "v_mfma_f32_32x32x16_bf16 v[124:139], v[32:35], v[48:51], v[124:139]\n" \
    "ds_read2_b32 v[86:87], v156 offset0:50 offset1:51\n" \
    "ds_read_b128 v[116:119], v158 offset:240\n" \
    "ds_read_b128 v[100:103], v157 offset:240\n" \
    "v_mfma_f32_32x32x16_bf16 v[140:155], v[36:39], v[68:71], v[140:155]\n" \
    "ds_read2_b32 v[88:89], v156 offset0:56 offset1:57\n" \
    "ds_read2_b32 v[90:91], v156 offset0:58 offset1:59\n" \
    "ds_read_b128 v[120:123], v158 offset:272\n" \
    "v_mfma_f32_32x32x16_bf16 v[124:139], v[36:39], v[52:55], v[124:139]\n" \
    "ds_read_b128 v[104:107], v157 offset:272\n" \
    "v_mfma_f32_32x32x16_bf16 v[140:155], v[40:43], v[72:75], v[140:155]\n" \
    "v_mfma_f32_32x32x16_bf16 v[124:139], v[40:43], v[56:59], v[124:139]\n" \
    "v_add_u32 v156, 256, v156\n" \
    "v_add_u32 v158, 320, v158\n" \
    "v_add_u32 v157, 320, v157\n" \
    "s_waitcnt lgkmcnt(0)\n" \
    "v_mfma_f32_32x32x16_bf16 v[140:155], v[76:79], v[108:111], v[140:155]\n" \
    "ds_read2_b32 v[28:29], v156 offset0:0 offset1:1\n" \
    "ds_read2_b32 v[30:31], v156 offset0:2 offset1:3\n" \
    "ds_read_b128 v[60:63], v158\n" \
    "v_mfma_f32_32x32x16_bf16 v[124:139], v[76:79], v[92:95], v[124:139]\n" \
    "ds_read_b128 v[44:47], v157\n" \
    "ds_read2_b32 v[32:33], v156 offset0:8 offset1:9\n" \
    "ds_read2_b32 v[34:35], v156 offset0:10 offset1:11\n" \
    "v_mfma_f32_32x32x16_bf16 v[140:155], v[80:83], v[112:115], v[140:155]\n" \
    "ds_read_b128 v[64:67], v158 offset:32\n" \
    "ds_read_b128 v[48:51], v157 offset:32\n" \
    "ds_read2_b32 v[36:37], v156 offset0:16 offset1:17\n" \
    "v_mfma_f32_32x32x16_bf16 v[124:139], v[80:83], v[96:99], v[124:139]\n" \
    "ds_read2_b32 v[38:39], v156 offset0:18 offset1:19\n" \
    "ds_read_b128 v[68:71], v158 offset:80\n" \
    "ds_read_b128 v[52:55], v157 offset:80\n" \
    "v_mfma_f32_32x32x16_bf16 v[140:155], v[84:87], v[116:119], v[140:155]\n" \
    "ds_read2_b32 v[40:41], v156 offset0:24 offset1:25\n" \
    "ds_read2_b32 v[42:43], v156 offset0:26 offset1:27\n" \
    "ds_read_b128 v[72:75], v158 offset:112\n" \
    "v_mfma_f32_32x32x16_bf16 v[124:139], v[84:87], v[100:103], v[124:139]\n" \
    "ds_read_b128 v[56:59], v157 offset:112\n" \
    "v_mfma_f32_32x32x16_bf16 v[140:155], v[88:91], v[120:123], v[140:155]\n" \
    "v_mfma_f32_32x32x16_bf16 v[124:139], v[88:91], v[104:107], v[124:139]\n" \
    "s_add_u32 s56, s56, 1\n" \
    "s_cmp_lt_u32 s56, 8\n" \
    "s_cbranch_scc1 L_cv0_%=\n" \
    "s_mov_b32 s56, 0\n" \
    "L_cv1_%=:\n" \
    "s_waitcnt lgkmcnt(0)\n" \
    "v_mfma_f32_32x32x16_bf16 v[140:155], v[28:31], v[60:63], v[140:155]\n" \
    "ds_read2_b32 v[76:77], v156 offset0:32 offset1:33\n" \
    "ds_read2_b32 v[78:79], v156 offset0:34 offset1:35\n" \
    "ds_read_b128 v[108:111], v158 offset:160\n" \
    "ds_read2_b32 v[80:81], v156 offset0:40 offset1:41\n" \
    "v_mfma_f32_32x32x16_bf16 v[140:155], v[32:35], v[64:67], v[140:155]\n" \
    "ds_read2_b32 v[82:83], v156 offset0:42 offset1:43\n" \
    "ds_read_b128 v[112:115], v158 offset:192\n" \
    "ds_read2_b32 v[84:85], v156 offset0:48 offset1:49\n" \
    "ds_read2_b32 v[86:87], v156 offset0:50 offset1:51\n" \
    "v_mfma_f32_32x32x16_bf16 v[140:155], v[36:39], v[68:71], v[140:155]\n" \
    "ds_read_b128 v[116:119], v158 offset:240\n" \
    "ds_read2_b32 v[88:89], v156 offset0:56 offset1:57\n" \
    "ds_read2_b32 v[90:91], v156 offset0:58 offset1:59\n" \
    "ds_read_b128 v[120:123], v158 offset:272\n" \
    "v_mfma_f32_32x32x16_bf16 v[140:155], v[40:43], v[72:75], v[140:155]\n" \
    "v_add_u32 v156, 256, v156\n" \
    "v_add_u32 v158, 320, v158\n" \
    "v_add_u32 v157, 320, v157\n" \
    "s_waitcnt lgkmcnt(0)\n" \
    "v_mfma_f32_32x32x16_bf16 v[140:155], v[76:79], v[108:111], v[140:155]\n" \
    "ds_read2_b32 v[28:29], v156 offset0:0 offset1:1\n" \
    "ds_read2_b32 v[30:31], v156 offset0:2 offset1:3\n" \
    "ds_read_b128 v[60:63], v158\n" \
    "ds_read2_b32 v[32:33], v156 offset0:8 offset1:9\n" \
    "v_mfma_f32_32x32x16_bf16 v[140:155], v[80:83], v[112:115], v[140:155]\n" \
    "ds_read2_b32 v[34:35], v156 offset0:10 offset1:11\n" \
    "ds_read_b128 v[64:67], v158 offset:32\n" \
    "ds_read2_b32 v[36:37], v156 offset0:16 offset1:17\n" \
    "ds_read2_b32 v[38:39], v156 offset0:18 offset1:19\n" \
    "v_mfma_f32_32x32x16_bf16 v[140:155], v[84:87], v[116:119], v[140:155]\n" \
    "ds_read_b128 v[68:71], v158 offset:80\n" \
    "ds_read2_b32 v[40:41], v156 offset0:24 offset1:25\n" \
    "ds_read2_b32 v[42:43], v156 offset0:26 offset1:27\n" \
    "ds_read_b128 v[72:75], v158 offset:112\n" \
    "v_mfma_f32_32x32x16_bf16 v[140:155], v[88:91], v[120:123], v[140:155]\n" \
    "s_add_u32 s56, s56, 1\n" \
    "s_cmp_lt_u32 s56, 8\n" \
    "s_cbranch_scc1 L_cv1_%=\n" \
    "s_waitcnt lgkmcnt(0)\n" \
    "s_nop 15\n" \
    "ds_write_b128 v159, v[124:127]\n" \
    "ds_write_b128 v159, v[128:131] offset:1024\n" \
    "ds_write_b128 v159, v[132:135] offset:2048\n" \
    "ds_write_b128 v159, v[136:139] offset:3072\n" \
    "ds_write_b128 v159, v[140:143] offset:4096\n" \
    "ds_write_b128 v159, v[144:147] offset:5120\n" \
    "ds_write_b128 v159, v[148:151] offset:6144\n" \
    "ds_write_b128 v159, v[152:155] offset:7168\n" \
    "s_waitcnt lgkmcnt(0)\n" \
    :: "v"(la), "v"(lb0), "v"(lb1), "v"(lr) : "v28", "v29", "v30", "v31", "v32", "v33", "v34", "v35", "v36", "v37", "v38", "v39", "v40", "v41", "v42", "v43", "v44", "v45", "v46", "v47", "v48", "v49", "v50", "v51", "v52", "v53", "v54", "v55", "v56", "v57", "v58", "v59", "v60", "v61", "v62", "v63", "v64", "v65", "v66", "v67", "v68", "v69", "v70", "v71", "v72", "v73", "v74", "v75", "v76", "v77", "v78", "v79", "v80", "v81", "v82", "v83", "v84", "v85", "v86", "v87", "v88", "v89", "v90", "v91", "v92", "v93", "v94", "v95", "v96", "v97", "v98", "v99", "v100", "v101", "v102", "v103", "v104", "v105", "v106", "v107", "v108", "v109", "v110", "v111", "v112", "v113", "v114", "v115", "v116", "v117", "v118", "v119", "v120", "v121", "v122", "v123", "v124", "v125", "v126", "v127", "v128", "v129", "v130", "v131", "v132", "v133", "v134", "v135", "v136", "v137", "v138", "v139", "v140", "v141", "v142", "v143", "v144", "v145", "v146", "v147", "v148", "v149", "v150", "v151", "v152", "v153", "v154", "v155", "v156", "v157", "v158", "v159", "s56", "scc", "memory")
typedef unsigned short u16;
typedef __bf16 bf16x8_t __attribute__((ext_vector_type(8)));
typedef float f32x4_t __attribute__((ext_vector_type(4)));
typedef float f32x16_t __attribute__((ext_vector_type(16)));
typedef float f32x2_t __attribute__((ext_vector_type(2)));
typedef __bf16 bf16x2_t __attribute__((ext_vector_type(2)));
typedef unsigned u32x4_t __attribute__((ext_vector_type(4)));
typedef unsigned u32x2_t __attribute__((ext_vector_type(2)));
typedef __attribute__((address_space(1))) unsigned gu32;
__device__ __forceinline__ float bf2f(u16 b) { return __uint_as_float(((unsigned)b) << 16); }
__device__ __forceinline__ unsigned cvtpk(float lo, float hi) { f32x2_t v = {lo, hi}; bf16x2_t b = __builtin_convertvector(v, bf16x2_t); return __builtin_bit_cast(unsigned, b); }
__device__ __forceinline__ float rbf(float f) { return __uint_as_float(cvtpk(f, 0.f) << 16); }
__device__ __forceinline__ float lo16(unsigned w) { return __uint_as_float(w << 16); }
__device__ __forceinline__ float hi16(unsigned w) { return __uint_as_float(w & 0xffff0000u); }

#define CONV_HPD 1072
#define CONV_KV (96 * 40)
#define CONV_LDS_W (4 + 2 * CONV_HPD * 4 + 12 + CONV_KV * 2)
__device__ __forceinline__ void conv_channel(int d, int l, const u16* __restrict__ ssk, const u16* __restrict__ ssd, char* __restrict__ ws, char* lds, unsigned* wg_cnt) {
  unsigned* hp0 = (unsigned*)(lds + 4); unsigned* hp1 = hp0 + CONV_HPD; u16* kvr = (u16*)(lds + 16 + 2 * CONV_HPD * 4);
  const u16* h = ssk + (size_t)d * 2048;
  const u16* kq = (const u16*)(ws + WS_KVT) + (size_t)d * 8;
  u32x4_t hv[4], kvv[4]; unsigned pv[4];
  for (int i = 0; i < 4; ++i) { const int m0 = (i * 64 + l) * 8; hv[i] = *(const u32x4_t*)(h + m0); kvv[i] = *(const u32x4_t*)(kq + (size_t)(i * 64 + l) * 5120); pv[i] = m0 ? *(const unsigned*)(h + m0 - 2) : 0u; }
  const float D = bf2f(ssd[d]);
  __builtin_amdgcn_sched_barrier(0); __builtin_amdgcn_s_setprio(0); __builtin_amdgcn_sched_barrier(0);
  if (l < 15) { hp0[l] = 0; hp1[l] = 0; }
  if (l < CONV_HPD - 1039) { hp0[1039 + l] = 0; hp1[1039 + l] = 0; }
  for (int i = l; i < 32 * 5; i += 64) *(u32x4_t*)(kvr + 64 * 40 + i * 8) = (u32x4_t){0, 0, 0, 0};
  for (int i = 0; i < 4; ++i) { const int m0 = (i * 64 + l) * 8, q0 = m0 / 2 + 15;
    *(u32x4_t*)(hp1 + q0) = hv[i];
    *(u32x4_t*)(hp0 + q0) = (u32x4_t){__builtin_amdgcn_alignbit(hv[i][0], pv[i], 16), __builtin_amdgcn_alignbit(hv[i][1], hv[i][0], 16), __builtin_amdgcn_alignbit(hv[i][2], hv[i][1], 16), __builtin_amdgcn_alignbit(hv[i][3], hv[i][2], 16)};
    *(u32x4_t*)(kvr + (m0 >> 5) * 40 + (m0 & 31)) = kvv[i]; }
  if (l == 63) hp0[1039] = hv[3][3] >> 16;
  const int r = l & 31, hh = l >> 5;
  const unsigned* ap = ((r & 1) ? hp1 : hp0) + ((8 * hh + r - (r & 1)) >> 1);
  const u16* bp1 = kvr + (63 - 32 - r) * 40 + 8 * hh;
  const u16* bp0 = kvr + (63 - r) * 40 + 8 * hh;
  { unsigned la = (unsigned)(size_t)ap, lb0 = (unsigned)(size_t)bp0, lb1 = (unsigned)(size_t)bp1, lr = (unsigned)(size_t)(lds + 16) + l * 16;
    asm volatile("s_waitcnt lgkmcnt(0)" ::: "memory");
    CONV_CORE(la, lb0, lb1, lr); }
  u32x2_t res[8];
  for (int cb = 0; cb < 2; ++cb) for (int g = 0; g < 4; ++g) {
    const int i32 = 32 * cb + r, t0 = 32 * i32 + 8 * g + 4 * hh; float o[4];
    const f32x4_t y = *(const f32x4_t*)(lds + 16 + (cb * 4 + g) * 1024 + l * 16);
    const int u = 2047 - t0 - 3;
    const u32x2_t kw = *(const u32x2_t*)(kvr + (u >> 5) * 40 + (u & 31));
    const float kvf[4] = {hi16(kw[1]), lo16(kw[1]), hi16(kw[0]), lo16(kw[0])};
    for (int e = 0; e < 4; ++e) o[e] = rbf(y[e]) + rbf(kvf[e] * D);
    res[cb * 4 + g] = (u32x2_t){cvtpk(o[0], o[1]), cvtpk(o[2], o[3])};
  }
  asm volatile("s_waitcnt lgkmcnt(0)" ::: "memory");
  u16* zs = (u16*)(lds + 16);
  for (int cb = 0; cb < 2; ++cb) for (int g = 0; g < 4; ++g) *(u32x2_t*)(zs + (32 * cb + r) * 36 + 8 * g + 4 * hh) = res[cb * 4 + g];
  __builtin_amdgcn_s_setprio(3);
  __amdgpu_buffer_rsrc_t rs = __builtin_amdgcn_make_buffer_rsrc((void*)(ws + WS_ZT + (size_t)d * 4096), 0, 4096, 0x00020000);
  for (int i = 0; i < 4; ++i) { const int t0 = (i * 64 + l) * 8; const u16* src = zs + (t0 >> 5) * 36 + (t0 & 31);
    const u32x2_t lo = *(const u32x2_t*)src, hi = *(const u32x2_t*)(src + 4);
    __builtin_amdgcn_raw_buffer_store_b128((u32x4_t){lo[0], lo[1], hi[0], hi[1]}, rs, t0 * 2, 0, 16); }
  asm volatile("s_waitcnt vmcnt(0)" ::: "memory");
  if (l == 0) { const unsigned old = __hip_atomic_fetch_add(wg_cnt, 1u, __ATOMIC_RELAXED, __HIP_MEMORY_SCOPE_WORKGROUP);
    if (old == 3u) __hip_atomic_fetch_add((gu32*)(ws + WS_FLAG) + (d / 80) * 32, 1u, __ATOMIC_RELAXED, __HIP_MEMORY_SCOPE_AGENT); }
}

extern "C" __global__ __launch_bounds__(256, 1) void k_main(const void* x, const void* ckv, const void* wq, const void* wuk, const void* wuv, void* ws, const void* wo,
    const void* bk, const void* bv, const void* shk, const void* gw) {
  const unsigned wid = __builtin_amdgcn_readfirstlane(threadIdx.x >> 6);
  const unsigned b = blockIdx.x;
  const unsigned h = (b & 7) * 4 + ((b >> 3) & 3), j = b >> 5;
  const unsigned long long px = (unsigned long long)x, pc = (unsigned long long)ckv, pq = (unsigned long long)wq, pk = (unsigned long long)wuk, pv = (unsigned long long)wuv, pw = (unsigned long long)ws, po = (unsigned long long)wo;
  const unsigned long long p1 = (unsigned long long)bk, p2 = (unsigned long long)bv, p3 = (unsigned long long)shk, p4 = (unsigned long long)gw;
  asm volatile(
    "s_mov_b32 s64, %0\n"
    "s_mov_b32 s65, %1\n"
    "s_mov_b32 s66, %2\n"
    "s_mov_b32 s67, %3\n"
    "s_mov_b32 s68, %4\n"
    "s_mov_b32 s69, %5\n"
    "s_mov_b32 s70, %6\n"
    "s_mov_b32 s71, %7\n"
    "s_mov_b32 s72, %8\n"
    "s_mov_b32 s73, %9\n"
    "s_mov_b32 s74, %10\n"
    "s_mov_b32 s75, %11\n"
    "s_mov_b32 s60, %12\n"
    "s_mov_b32 s61, %13\n"
    "s_mov_b32 s62, %14\n"
    "v_mbcnt_lo_u32_b32 v250, -1, 0\n"
    "v_mbcnt_hi_u32_b32 v250, -1, v250\n"
    "v_lshlrev_b32 v251, 4, v250\n"
    "s_getpc_b64 s[44:45]\n"
    "L_pc_%=:\n"
    "s_and_b32 s45, s45, 65535\n"
    "s_mov_b32 s46, L_codeend_%=-L_pc_%=\n"
    "s_mov_b32 s47, 131072\n"
    "s_lshr_b32 s82, %15, 3\n"
    "s_lshl_b32 s82, s82, 2\n"
    "s_add_u32 s82, s82, s60\n"
    "s_lshl_b32 s82, s82, 9\n"
    "v_and_b32 v90, 7, v250\n"
    "v_lshlrev_b32 v90, 6, v90\n"
    "s_nop 0\n"
    "buffer_load_dword v255, v90, s[44:47], s82 offen\n"
    "s_add_u32 s36, s66, 0\n"
    "s_addc_u32 s37, s67, 0\n"
    "s_and_b32 s37, s37, 65535\n"
    "s_mov_b32 s38, 2097152\n"
    "s_mov_b32 s39, 131072\n"
    "s_cmp_lt_u32 s60, 2\n"
    "s_cselect_b32 s82, s70, s72\n"
    "s_cselect_b32 s83, s71, s73\n"
    "s_sub_u32 s40, s82, 1024\n"
    "s_subb_u32 s83, s83, 0\n"
    "s_and_b32 s41, s83, 65535\n"
    "s_mov_b32 s42, 4195328\n"
    "s_mov_b32 s43, 131072\n"
    "v_lshrrev_b32 v90, 3, v250\n"
    "v_mul_u32_u24 v90, 1024, v90\n"
    "v_and_b32 v91, 7, v250\n"
    "v_lshrrev_b32 v92, 4, v250\n"
    "v_xor_b32 v91, v91, v92\n"
    "v_xor_b32 v93, 4, v91\n"
    "v_lshlrev_b32 v91, 4, v91\n"
    "v_lshlrev_b32 v93, 4, v93\n"
    "v_add_u32 v91, v91, v90\n"
    "v_add_u32 v93, v93, v90\n"
    "v_add_u32 v64, 0, v91\n"
    "v_add_u32 v65, 7168, v93\n"
    "v_add_u32 v66, 14336, v91\n"
    "v_add_u32 v67, 21504, v93\n"
    "v_add_u32 v68, 32768, v91\n"
    "v_add_u32 v69, 39936, v93\n"
    "v_add_u32 v70, 47104, v91\n"
    "v_add_u32 v71, 54272, v93\n"
    "v_bfe_u32 v90, v250, 2, 3\n"
    "v_lshlrev_b32 v90, 13, v90\n"
    "v_lshrrev_b32 v92, 5, v250\n"
    "v_lshlrev_b32 v92, 6, v92\n"
    "v_add_u32 v90, v90, v92\n"
    "v_and_b32 v91, 3, v250\n"
    "v_bfe_u32 v92, v250, 4, 1\n"
    "v_xor_b32 v91, v91, v92\n"
    "v_xor_b32 v93, 2, v91\n"
    "v_lshlrev_b32 v91, 4, v91\n"
    "v_lshlrev_b32 v93, 4, v93\n"
    "v_add_u32 v91, v91, v90\n"
    "v_add_u32 v93, v93, v90\n"
    "v_add_u32 v72, 1024, v91\n"
    "v_add_u32 v73, 128, v91\n"
    "v_add_u32 v74, 64512, v93\n"
    "v_add_u32 v75, 63616, v93\n"
    "v_add_u32 v76, 132096, v91\n"
    "v_add_u32 v77, 131200, v91\n"
    "v_add_u32 v78, 195584, v93\n"
    "v_add_u32 v79, 194688, v93\n"
    "s_lshl_b32 s82, s62, 18\n"
    "s_lshl_b32 s83, s60, 16\n"
    "s_add_u32 s77, s82, s83\n"
    "s_and_b32 s82, s60, 1\n"
    "s_lshl_b32 s82, s82, 18\n"
    "s_lshl_b32 s83, s61, 8\n"
    "s_add_u32 s78, s82, s83\n"
    "s_lshl_b32 s94, s60, 13\n"
    "s_add_u32 m0, s94, 0\n"
    "s_nop 0\n"
    "buffer_load_dwordx4 v64, s[36:39], s77 offen lds\n"
    "buffer_load_dwordx4 v65, s[36:39], s77 offen offset:1024 lds\n"
    "buffer_load_dwordx4 v66, s[36:39], s77 offen offset:2048 lds\n"
    "buffer_load_dwordx4 v67, s[36:39], s77 offen offset:3072 lds\n"
    "s_add_u32 m0, s94, 4096\n"
    "s_nop 0\n"
    "buffer_load_dwordx4 v68, s[36:39], s77 offen lds\n"
    "buffer_load_dwordx4 v69, s[36:39], s77 offen offset:1024 lds\n"
    "buffer_load_dwordx4 v70, s[36:39], s77 offen offset:2048 lds\n"
    "buffer_load_dwordx4 v71, s[36:39], s77 offen offset:3072 lds\n"
    "s_add_u32 m0, s94, 98304\n"
    "s_nop 0\n"
    "buffer_load_dwordx4 v72, s[40:43], s78 offen lds\n"
    "buffer_load_dwordx4 v73, s[40:43], s78 offen offset:1024 lds\n"
    "buffer_load_dwordx4 v74, s[40:43], s78 offen offset:2048 lds\n"
    "buffer_load_dwordx4 v75, s[40:43], s78 offen offset:3072 lds\n"
    "s_add_u32 m0, s94, 102400\n"
    "s_nop 0\n"
    "buffer_load_dwordx4 v76, s[40:43], s78 offen lds\n"
    "buffer_load_dwordx4 v77, s[40:43], s78 offen offset:1024 lds\n"
    "buffer_load_dwordx4 v78, s[40:43], s78 offen offset:2048 lds\n"
    "buffer_load_dwordx4 v79, s[40:43], s78 offen offset:3072 lds\n"
    "s_add_u32 s77, s77, 128\n"
    "s_add_u32 m0, s94, 32768\n"
    "s_nop 0\n"
    "buffer_load_dwordx4 v64, s[36:39], s77 offen lds\n"
    "buffer_load_dwordx4 v65, s[36:39], s77 offen offset:1024 lds\n"
    "buffer_load_dwordx4 v66, s[36:39], s77 offen offset:2048 lds\n"
    "buffer_load_dwordx4 v67, s[36:39], s77 offen offset:3072 lds\n"
    "s_add_u32 m0, s94, 36864\n"
    "s_nop 0\n"
    "buffer_load_dwordx4 v68, s[36:39], s77 offen lds\n"
    "buffer_load_dwordx4 v69, s[36:39], s77 offen offset:1024 lds\n"
    "buffer_load_dwordx4 v70, s[36:39], s77 offen offset:2048 lds\n"
    "buffer_load_dwordx4 v71, s[36:39], s77 offen offset:3072 lds\n"
    "s_add_u32 s77, s77, 128\n"
    "s_add_u32 s78, s78, 524288\n"
    "v_add_u32 v248, 4096, v251\n"
    "s_lshl_b32 s82, s61, 7\n"
    "s_add_u32 s56, s74, 85000192\n"
    "s_addc_u32 s57, s75, 0\n"
    "s_add_u32 s56, s56, s82\n"
    "s_addc_u32 s57, s57, 0\n"
    "s_and_b32 s57, s57, 65535\n"
    "s_mov_b32 s58, 64\n"
    "s_mov_b32 s59, 131072\n"
    "s_load_dword s63, s[56:57], 0x20\n"
    "s_cmp_lg_u32 %15, 0\n"
    "s_cbranch_scc1 L_zc_%=\n"
    "s_cmp_lg_u32 s60, 0\n"
    "s_cbranch_scc1 L_zc_%=\n"
    "s_add_u32 s44, s74, 84934656\n"
    "s_addc_u32 s45, s75, 0\n"
    "s_and_b32 s45, s45, 65535\n"
    "s_mov_b32 s46, 16384\n"
    "s_mov_b32 s47, 131072\n"
    "v_lshlrev_b32 v90, 7, v250\n"
    "v_mov_b32 v91, 0\n"
    "s_nop 1\n"
    "buffer_store_dword v91, v90, s[44:47], 0 offen\n"
    "L_zc_%=:\n"
    "s_lshl_b32 s82, %15, 2\n"
    "s_add_u32 s82, s82, s60\n"
    "s_mul_i32 s83, s82, 52429\n"
    "s_lshr_b32 s83, s83, 19\n"
    "s_mul_i32 s84, s83, 10\n"
    "s_sub_u32 s84, s82, s84\n"
    "s_cmp_lt_u32 s82, 640\n"
    "s_cselect_b32 s90, 2621440, 0\n"
    "s_cmp_eq_u32 s83, 0\n"
    "s_cselect_b32 s93, 0, 65535\n"
    "s_mov_b32 s48, %18\n"
    "s_and_b32 s49, %19, 0xffff\n"
    "s_mov_b32 s50, 2621440\n"
    "s_mov_b32 s51, 131072\n"
    "s_mov_b32 s44, %20\n"
    "s_and_b32 s45, %21, 0xffff\n"
    "s_mov_b32 s46, 2621440\n"
    "s_mov_b32 s47, 131072\n"
    "s_mov_b32 s56, %22\n"
    "s_and_b32 s57, %23, 0xffff\n"
    "s_mov_b32 s58, 5120\n"
    "s_mov_b32 s59, 131072\n"
    "v_lshlrev_b32 v240, 1, v250\n"
    "v_lshlrev_b32 v241, 12, v250\n"
    "v_lshlrev_b32 v237, 3, v250\n"
    "s_lshl_b32 s85, s84, 9\n"
    "buffer_load_dwordx2 v[212:213], v237, s[56:59], s85 offen\n"
    "s_mul_i32 s88, s83, 40960\n"
    "s_lshl_b32 s85, s84, 7\n"
    "s_add_u32 s88, s88, s85\n"
    "s_add_u32 s92, s88, 5120\n"
    "s_max_i32 s85, s88, 3840\n"
    "s_sub_u32 s85, s85, 3840\n"
    "buffer_load_ushort v192, v240, s[48:51], s85 offen\n"
    "buffer_load_ushort v193, v240, s[48:51], s85 offen offset:1280\n"
    "buffer_load_ushort v194, v240, s[48:51], s85 offen offset:2560\n"
    "v_mul_u32_u24 v238, 1280, v250\n"
    "s_nop 0\n"
    "buffer_load_dword v237, v238, s[48:51], s88 offen\n"
    "buffer_load_dword v237, v238, s[44:47], s88 offen\n"
    "buffer_load_ushort v195, v240, s[48:51], s88 offen\n"
    "buffer_load_ushort v196, v240, s[48:51], s88 offen offset:1280\n"
    "buffer_load_ushort v197, v240, s[48:51], s88 offen offset:2560\n"
    "buffer_load_ushort v198, v240, s[48:51], s88 offen offset:3840\n"
    "buffer_load_ushort v199, v240, s[48:51], s92 offen\n"
    "buffer_load_ushort v200, v240, s[48:51], s92 offen offset:1280\n"
    "buffer_load_ushort v201, v240, s[48:51], s92 offen offset:2560\n"
    "buffer_load_ushort v202, v240, s[48:51], s92 offen offset:3840\n"
    "s_lshl_b32 s85, s83, 2\n"
    "s_sub_u32 s85, 256, s85\n"
    "s_mul_i32 s89, s85, 10240\n"
    "s_lshl_b32 s85, s84, 10\n"
    "s_add_u32 s89, s89, s85\n"
    "s_add_u32 s56, s74, 75497472\n"
    "s_addc_u32 s57, s75, 0\n"
    "s_and_b32 s57, s57, 65535\n"
    "s_mov_b32 s58, 0\n"
    "s_mov_b32 s59, 131072\n"
    "s_and_b32 s82, s60, 1\n"
    "s_lshl_b32 s82, s82, 14\n"
    "v_and_b32 v90, 31, v250\n"
    "v_lshlrev_b32 v90, 7, v90\n"
    "v_add_u32 v90, s82, v90\n"
    "v_bfe_u32 v91, v250, 1, 3\n"
    "v_lshrrev_b32 v92, 5, v250\n"
    "v_add_u32 v93, 0, v92\n"
    "v_xor_b32 v93, v93, v91\n"
    "v_lshlrev_b32 v93, 4, v93\n"
    "v_add_u32 v84, v93, v90\n"
    "v_add_u32 v93, 2, v92\n"
    "v_xor_b32 v93, v93, v91\n"
    "v_lshlrev_b32 v93, 4, v93\n"
    "v_add_u32 v85, v93, v90\n"
    "v_add_u32 v93, 4, v92\n"
    "v_xor_b32 v93, v93, v91\n"
    "v_lshlrev_b32 v93, 4, v93\n"
    "v_add_u32 v86, v93, v90\n"
    "v_add_u32 v93, 6, v92\n"
    "v_xor_b32 v93, v93, v91\n"
    "v_lshlrev_b32 v93, 4, v93\n"
    "v_add_u32 v87, v93, v90\n"
    "s_lshr_b32 s82, s60, 1\n"
    "s_lshl_b32 s82, s82, 14\n"
    "s_add_u32 s82, s82, 98304\n"
    "v_lshrrev_b32 v92, 5, v250\n"
    "v_bfe_u32 v90, v250, 2, 2\n"
    "v_lshlrev_b32 v90, 6, v90\n"
    "v_lshlrev_b32 v91, 11, v92\n"
    "v_add_u32 v90, v90, v91\n"
    "v_and_b32 v91, 1, v250\n"
    "v_lshlrev_b32 v91, 3, v91\n"
    "v_add_u32 v90, v90, v91\n"
    "v_add_u32 v90, s82, v90\n"
    "v_bfe_u32 v91, v250, 4, 1\n"
    "v_lshlrev_b32 v91, 1, v91\n"
    "v_bfe_u32 v93, v250, 1, 1\n"
    "v_add_u32 v91, v91, v93\n"
    "v_lshlrev_b32 v92, 1, v92\n"
    "v_add_u32 v93, 0, v92\n"
    "v_xor_b32 v93, v93, v91\n"
    "v_lshlrev_b32 v93, 4, v93\n"
    "v_add_u32 v93, v93, v90\n"
    "v_add_u32 v88, 0, v93\n"
    "v_add_u32 v93, 1, v92\n"
    "v_xor_b32 v93, v93, v91\n"
    "v_lshlrev_b32 v93, 4, v93\n"
    "v_add_u32 v93, v93, v90\n"
    "v_add_u32 v89, 256, v93\n"
    "v_lshrrev_b32 v90, 3, v250\n"
    "v_mul_u32_u24 v90, 1280, v90\n"
    "v_and_b32 v91, 7, v250\n"
    "v_lshrrev_b32 v92, 4, v250\n"
    "v_xor_b32 v91, v91, v92\n"
    "v_xor_b32 v93, 4, v91\n"
    "v_lshlrev_b32 v91, 4, v91\n"
    "v_lshlrev_b32 v93, 4, v93\n"
    "v_add_u32 v91, v91, v90\n"
    "v_add_u32 v93, v93, v90\n"
    "v_add_u32 v112, 0, v91\n"
    "v_add_u32 v113, 9216, v93\n"
    "v_add_u32 v114, 18432, v91\n"
    "v_add_u32 v115, 27648, v93\n"
    "v_add_u32 v116, 40960, v91\n"
    "v_add_u32 v117, 50176, v93\n"
    "v_add_u32 v118, 59392, v91\n"
    "v_add_u32 v119, 68608, v93\n"
    "v_bfe_u32 v90, v250, 2, 3\n"
    "v_lshlrev_b32 v90, 13, v90\n"
    "v_lshrrev_b32 v92, 5, v250\n"
    "v_lshlrev_b32 v92, 6, v92\n"
    "v_add_u32 v90, v90, v92\n"
    "v_and_b32 v91, 3, v250\n"
    "v_bfe_u32 v92, v250, 4, 1\n"
    "v_xor_b32 v91, v91, v92\n"
    "v_xor_b32 v93, 2, v91\n"
    "v_lshlrev_b32 v91, 4, v91\n"
    "v_lshlrev_b32 v93, 4, v93\n"
    "v_add_u32 v91, v91, v90\n"
    "v_add_u32 v93, v93, v90\n"
    "v_add_u32 v120, 1024, v91\n"
    "v_add_u32 v121, 128, v91\n"
    "v_add_u32 v122, 64512, v93\n"
    "v_add_u32 v123, 63616, v93\n"
    "s_lshr_b32 s82, s60, 1\n"
    "s_lshl_b32 s82, s82, 14\n"
    "v_and_b32 v90, 31, v250\n"
    "v_lshlrev_b32 v90, 7, v90\n"
    "v_add_u32 v90, s82, v90\n"
    "v_bfe_u32 v91, v250, 1, 3\n"
    "v_lshrrev_b32 v92, 5, v250\n"
    "v_add_u32 v93, 0, v92\n"
    "v_xor_b32 v93, v93, v91\n"
    "v_lshlrev_b32 v93, 4, v93\n"
    "v_add_u32 v124, v93, v90\n"
    "v_add_u32 v93, 2, v92\n"
    "v_xor_b32 v93, v93, v91\n"
    "v_lshlrev_b32 v93, 4, v93\n"
    "v_add_u32 v125, v93, v90\n"
    "v_add_u32 v93, 4, v92\n"
    "v_xor_b32 v93, v93, v91\n"
    "v_lshlrev_b32 v93, 4, v93\n"
    "v_add_u32 v126, v93, v90\n"
    "v_add_u32 v93, 6, v92\n"
    "v_xor_b32 v93, v93, v91\n"
    "v_lshlrev_b32 v93, 4, v93\n"
    "v_add_u32 v127, v93, v90\n"
    "s_and_b32 s82, s60, 1\n"
    "s_lshl_b32 s82, s82, 10\n"
    "s_add_u32 s82, s82, 98304\n"
    "v_lshrrev_b32 v92, 5, v250\n"
    "v_bfe_u32 v90, v250, 2, 2\n"
    "v_lshlrev_b32 v90, 6, v90\n"
    "v_lshlrev_b32 v91, 11, v92\n"
    "v_add_u32 v90, v90, v91\n"
    "v_and_b32 v91, 1, v250\n"
    "v_lshlrev_b32 v91, 3, v91\n"
    "v_add_u32 v90, v90, v91\n"
    "v_add_u32 v90, s82, v90\n"
    "v_bfe_u32 v91, v250, 4, 1\n"
    "v_lshlrev_b32 v91, 1, v91\n"
    "v_bfe_u32 v93, v250, 1, 1\n"
    "v_add_u32 v91, v91, v93\n"
    "v_lshlrev_b32 v92, 1, v92\n"
    "v_add_u32 v93, 0, v92\n"
    "v_xor_b32 v93, v93, v91\n"
    "v_lshlrev_b32 v93, 4, v93\n"
    "v_add_u32 v93, v93, v90\n"
    "v_add_u32 v128, 0, v93\n"
    "v_add_u32 v93, 1, v92\n"
    "v_xor_b32 v93, v93, v91\n"
    "v_lshlrev_b32 v93, 4, v93\n"
    "v_add_u32 v93, v93, v90\n"
    "v_add_u32 v129, 256, v93\n"
    "v_lshrrev_b32 v90, 5, v250\n"
    "v_mul_u32_u24 v90, 20480, v90\n"
    "v_bfe_u32 v91, v250, 4, 1\n"
    "v_mul_u32_u24 v91, 5120, v91\n"
    "v_add_u32 v90, v90, v91\n"
    "v_and_b32 v91, 15, v250\n"
    "v_lshlrev_b32 v91, 1, v91\n"
    "v_add_u32 v130, v90, v91\n"
    "v_accvgpr_write_b32 a0, 0\n"
    "v_accvgpr_write_b32 a1, 0\n"
    "v_accvgpr_write_b32 a2, 0\n"
    "v_accvgpr_write_b32 a3, 0\n"
    "v_accvgpr_write_b32 a4, 0\n"
    "v_accvgpr_write_b32 a5, 0\n"
    "v_accvgpr_write_b32 a6, 0\n"
    "v_accvgpr_write_b32 a7, 0\n"
    "v_accvgpr_write_b32 a8, 0\n"
    "v_accvgpr_write_b32 a9, 0\n"
    "v_accvgpr_write_b32 a10, 0\n"
    "v_accvgpr_write_b32 a11, 0\n"
    "v_accvgpr_write_b32 a12, 0\n"
    "v_accvgpr_write_b32 a13, 0\n"
    "v_accvgpr_write_b32 a14, 0\n"
    "v_accvgpr_write_b32 a15, 0\n"
    "v_accvgpr_write_b32 a16, 0\n"
    "v_accvgpr_write_b32 a17, 0\n"
    "v_accvgpr_write_b32 a18, 0\n"
    "v_accvgpr_write_b32 a19, 0\n"
    "v_accvgpr_write_b32 a20, 0\n"
    "v_accvgpr_write_b32 a21, 0\n"
    "v_accvgpr_write_b32 a22, 0\n"
    "v_accvgpr_write_b32 a23, 0\n"
    "v_accvgpr_write_b32 a24, 0\n"
    "v_accvgpr_write_b32 a25, 0\n"
    "v_accvgpr_write_b32 a26, 0\n"
    "v_accvgpr_write_b32 a27, 0\n"
    "v_accvgpr_write_b32 a28, 0\n"
    "v_accvgpr_write_b32 a29, 0\n"
    "v_accvgpr_write_b32 a30, 0\n"
    "v_accvgpr_write_b32 a31, 0\n"
    "v_accvgpr_write_b32 a32, 0\n"
    "v_accvgpr_write_b32 a33, 0\n"
    "v_accvgpr_write_b32 a34, 0\n"
    "v_accvgpr_write_b32 a35, 0\n"
    "v_accvgpr_write_b32 a36, 0\n"
    "v_accvgpr_write_b32 a37, 0\n"
    "v_accvgpr_write_b32 a38, 0\n"
    "v_accvgpr_write_b32 a39, 0\n"
    "v_accvgpr_write_b32 a40, 0\n"
    "v_accvgpr_write_b32 a41, 0\n"
    "v_accvgpr_write_b32 a42, 0\n"
    "v_accvgpr_write_b32 a43, 0\n"
    "v_accvgpr_write_b32 a44, 0\n"
    "v_accvgpr_write_b32 a45, 0\n"
    "v_accvgpr_write_b32 a46, 0\n"
    "v_accvgpr_write_b32 a47, 0\n"
    "v_accvgpr_write_b32 a48, 0\n"
    "v_accvgpr_write_b32 a49, 0\n"
    "v_accvgpr_write_b32 a50, 0\n"
    "v_accvgpr_write_b32 a51, 0\n"
    "v_accvgpr_write_b32 a52, 0\n"
    "v_accvgpr_write_b32 a53, 0\n"
    "v_accvgpr_write_b32 a54, 0\n"
    "v_accvgpr_write_b32 a55, 0\n"
    "v_accvgpr_write_b32 a56, 0\n"
    "v_accvgpr_write_b32 a57, 0\n"
    "v_accvgpr_write_b32 a58, 0\n"
    "v_accvgpr_write_b32 a59, 0\n"
    "v_accvgpr_write_b32 a60, 0\n"
    "v_accvgpr_write_b32 a61, 0\n"
    "v_accvgpr_write_b32 a62, 0\n"
    "v_accvgpr_write_b32 a63, 0\n"
    "v_accvgpr_write_b32 a64, 0\n"
    "v_accvgpr_write_b32 a65, 0\n"
    "v_accvgpr_write_b32 a66, 0\n"
    "v_accvgpr_write_b32 a67, 0\n"
    "v_accvgpr_write_b32 a68, 0\n"
    "v_accvgpr_write_b32 a69, 0\n"
    "v_accvgpr_write_b32 a70, 0\n"
    "v_accvgpr_write_b32 a71, 0\n"
    "v_accvgpr_write_b32 a72, 0\n"
    "v_accvgpr_write_b32 a73, 0\n"
    "v_accvgpr_write_b32 a74, 0\n"
    "v_accvgpr_write_b32 a75, 0\n"
    "v_accvgpr_write_b32 a76, 0\n"
    "v_accvgpr_write_b32 a77, 0\n"
    "v_accvgpr_write_b32 a78, 0\n"
    "v_accvgpr_write_b32 a79, 0\n"
    "v_accvgpr_write_b32 a80, 0\n"
    "v_accvgpr_write_b32 a81, 0\n"
    "v_accvgpr_write_b32 a82, 0\n"
    "v_accvgpr_write_b32 a83, 0\n"
    "v_accvgpr_write_b32 a84, 0\n"
    "v_accvgpr_write_b32 a85, 0\n"
    "v_accvgpr_write_b32 a86, 0\n"
    "v_accvgpr_write_b32 a87, 0\n"
    "v_accvgpr_write_b32 a88, 0\n"
    "v_accvgpr_write_b32 a89, 0\n"
    "v_accvgpr_write_b32 a90, 0\n"
    "v_accvgpr_write_b32 a91, 0\n"
    "v_accvgpr_write_b32 a92, 0\n"
    "v_accvgpr_write_b32 a93, 0\n"
    "v_accvgpr_write_b32 a94, 0\n"
    "v_accvgpr_write_b32 a95, 0\n"
    "v_accvgpr_write_b32 a96, 0\n"
    "v_accvgpr_write_b32 a97, 0\n"
    "v_accvgpr_write_b32 a98, 0\n"
    "v_accvgpr_write_b32 a99, 0\n"
    "v_accvgpr_write_b32 a100, 0\n"
    "v_accvgpr_write_b32 a101, 0\n"
    "v_accvgpr_write_b32 a102, 0\n"
    "v_accvgpr_write_b32 a103, 0\n"
    "v_accvgpr_write_b32 a104, 0\n"
    "v_accvgpr_write_b32 a105, 0\n"
    "v_accvgpr_write_b32 a106, 0\n"
    "v_accvgpr_write_b32 a107, 0\n"
    "v_accvgpr_write_b32 a108, 0\n"
    "v_accvgpr_write_b32 a109, 0\n"
    "v_accvgpr_write_b32 a110, 0\n"
    "v_accvgpr_write_b32 a111, 0\n"
    "v_accvgpr_write_b32 a112, 0\n"
    "v_accvgpr_write_b32 a113, 0\n"
    "v_accvgpr_write_b32 a114, 0\n"
    "v_accvgpr_write_b32 a115, 0\n"
    "v_accvgpr_write_b32 a116, 0\n"
    "v_accvgpr_write_b32 a117, 0\n"
    "v_accvgpr_write_b32 a118, 0\n"
    "v_accvgpr_write_b32 a119, 0\n"
    "v_accvgpr_write_b32 a120, 0\n"
    "v_accvgpr_write_b32 a121, 0\n"
    "v_accvgpr_write_b32 a122, 0\n"
    "v_accvgpr_write_b32 a123, 0\n"
    "v_accvgpr_write_b32 a124, 0\n"
    "v_accvgpr_write_b32 a125, 0\n"
    "v_accvgpr_write_b32 a126, 0\n"
    "v_accvgpr_write_b32 a127, 0\n"
    "v_accvgpr_write_b32 a128, 0\n"
    "v_accvgpr_write_b32 a129, 0\n"
    "v_accvgpr_write_b32 a130, 0\n"
    "v_accvgpr_write_b32 a131, 0\n"
    "v_accvgpr_write_b32 a132, 0\n"
    "v_accvgpr_write_b32 a133, 0\n"
    "v_accvgpr_write_b32 a134, 0\n"
    "v_accvgpr_write_b32 a135, 0\n"
    "v_accvgpr_write_b32 a136, 0\n"
    "v_accvgpr_write_b32 a137, 0\n"
    "v_accvgpr_write_b32 a138, 0\n"
    "v_accvgpr_write_b32 a139, 0\n"
    "v_accvgpr_write_b32 a140, 0\n"
    "v_accvgpr_write_b32 a141, 0\n"
    "v_accvgpr_write_b32 a142, 0\n"
    "v_accvgpr_write_b32 a143, 0\n"
    "v_accvgpr_write_b32 a144, 0\n"
    "v_accvgpr_write_b32 a145, 0\n"
    "v_accvgpr_write_b32 a146, 0\n"
    "v_accvgpr_write_b32 a147, 0\n"
    "v_accvgpr_write_b32 a148, 0\n"
    "v_accvgpr_write_b32 a149, 0\n"
    "v_accvgpr_write_b32 a150, 0\n"
    "v_accvgpr_write_b32 a151, 0\n"
    "v_accvgpr_write_b32 a152, 0\n"
    "v_accvgpr_write_b32 a153, 0\n"
    "v_accvgpr_write_b32 a154, 0\n"
    "v_accvgpr_write_b32 a155, 0\n"
    "v_accvgpr_write_b32 a156, 0\n"
    "v_accvgpr_write_b32 a157, 0\n"
    "v_accvgpr_write_b32 a158, 0\n"
    "v_accvgpr_write_b32 a159, 0\n"
    "v_accvgpr_write_b32 a160, 0\n"
    "v_accvgpr_write_b32 a161, 0\n"
    "v_accvgpr_write_b32 a162, 0\n"
    "v_accvgpr_write_b32 a163, 0\n"
    "v_accvgpr_write_b32 a164, 0\n"
    "v_accvgpr_write_b32 a165, 0\n"
    "v_accvgpr_write_b32 a166, 0\n"
    "v_accvgpr_write_b32 a167, 0\n"
    "v_accvgpr_write_b32 a168, 0\n"
    "v_accvgpr_write_b32 a169, 0\n"
    "v_accvgpr_write_b32 a170, 0\n"
    "v_accvgpr_write_b32 a171, 0\n"
    "v_accvgpr_write_b32 a172, 0\n"
    "v_accvgpr_write_b32 a173, 0\n"
    "v_accvgpr_write_b32 a174, 0\n"
    "v_accvgpr_write_b32 a175, 0\n"
    "v_accvgpr_write_b32 a176, 0\n"
    "v_accvgpr_write_b32 a177, 0\n"
    "v_accvgpr_write_b32 a178, 0\n"
    "v_accvgpr_write_b32 a179, 0\n"
    "v_accvgpr_write_b32 a180, 0\n"
    "v_accvgpr_write_b32 a181, 0\n"
    "v_accvgpr_write_b32 a182, 0\n"
    "v_accvgpr_write_b32 a183, 0\n"
    "v_accvgpr_write_b32 a184, 0\n"
    "v_accvgpr_write_b32 a185, 0\n"
    "v_accvgpr_write_b32 a186, 0\n"
    "v_accvgpr_write_b32 a187, 0\n"
    "v_accvgpr_write_b32 a188, 0\n"
    "v_accvgpr_write_b32 a189, 0\n"
    "v_accvgpr_write_b32 a190, 0\n"
    "v_accvgpr_write_b32 a191, 0\n"
    "v_accvgpr_write_b32 a192, 0\n"
    "v_accvgpr_write_b32 a193, 0\n"
    "v_accvgpr_write_b32 a194, 0\n"
    "v_accvgpr_write_b32 a195, 0\n"
    "v_accvgpr_write_b32 a196, 0\n"
    "v_accvgpr_write_b32 a197, 0\n"
    "v_accvgpr_write_b32 a198, 0\n"
    "v_accvgpr_write_b32 a199, 0\n"
    "v_accvgpr_write_b32 a200, 0\n"
    "v_accvgpr_write_b32 a201, 0\n"
    "v_accvgpr_write_b32 a202, 0\n"
    "v_accvgpr_write_b32 a203, 0\n"
    "v_accvgpr_write_b32 a204, 0\n"
    "v_accvgpr_write_b32 a205, 0\n"
    "v_accvgpr_write_b32 a206, 0\n"
    "v_accvgpr_write_b32 a207, 0\n"
    "v_accvgpr_write_b32 a208, 0\n"
    "v_accvgpr_write_b32 a209, 0\n"
    "v_accvgpr_write_b32 a210, 0\n"
    "v_accvgpr_write_b32 a211, 0\n"
    "v_accvgpr_write_b32 a212, 0\n"
    "v_accvgpr_write_b32 a213, 0\n"
    "v_accvgpr_write_b32 a214, 0\n"
    "v_accvgpr_write_b32 a215, 0\n"
    "v_accvgpr_write_b32 a216, 0\n"
    "v_accvgpr_write_b32 a217, 0\n"
    "v_accvgpr_write_b32 a218, 0\n"
    "v_accvgpr_write_b32 a219, 0\n"
    "v_accvgpr_write_b32 a220, 0\n"
    "v_accvgpr_write_b32 a221, 0\n"
    "v_accvgpr_write_b32 a222, 0\n"
    "v_accvgpr_write_b32 a223, 0\n"
    "v_accvgpr_write_b32 a224, 0\n"
    "v_accvgpr_write_b32 a225, 0\n"
    "v_accvgpr_write_b32 a226, 0\n"
    "v_accvgpr_write_b32 a227, 0\n"
    "v_accvgpr_write_b32 a228, 0\n"
    "v_accvgpr_write_b32 a229, 0\n"
    "v_accvgpr_write_b32 a230, 0\n"
    "v_accvgpr_write_b32 a231, 0\n"
    "v_accvgpr_write_b32 a232, 0\n"
    "v_accvgpr_write_b32 a233, 0\n"
    "v_accvgpr_write_b32 a234, 0\n"
    "v_accvgpr_write_b32 a235, 0\n"
    "v_accvgpr_write_b32 a236, 0\n"
    "v_accvgpr_write_b32 a237, 0\n"
    "v_accvgpr_write_b32 a238, 0\n"
    "v_accvgpr_write_b32 a239, 0\n"
    "v_accvgpr_write_b32 a240, 0\n"
    "v_accvgpr_write_b32 a241, 0\n"
    "v_accvgpr_write_b32 a242, 0\n"
    "v_accvgpr_write_b32 a243, 0\n"
    "v_accvgpr_write_b32 a244, 0\n"
    "v_accvgpr_write_b32 a245, 0\n"
    "v_accvgpr_write_b32 a246, 0\n"
    "v_accvgpr_write_b32 a247, 0\n"
    "v_accvgpr_write_b32 a248, 0\n"
    "v_accvgpr_write_b32 a249, 0\n"
    "v_accvgpr_write_b32 a250, 0\n"
    "v_accvgpr_write_b32 a251, 0\n"
    "v_accvgpr_write_b32 a252, 0\n"
    "v_accvgpr_write_b32 a253, 0\n"
    "v_accvgpr_write_b32 a254, 0\n"
    "v_accvgpr_write_b32 a255, 0\n"
    "s_add_u32 s52, s64, 0\n"
    "s_addc_u32 s53, s65, 0\n"
    "s_and_b32 s53, s53, 65535\n"
    "s_mov_b32 s54, 2621440\n"
    "s_mov_b32 s55, 131072\n"
    "v_lshlrev_b32 v131, 6, v250\n"
    "v_lshrrev_b32 v108, 2, v250\n"
    "v_lshlrev_b32 v108, 13, v108\n"
    "v_and_b32 v109, 3, v250\n"
    "v_lshlrev_b32 v109, 6, v109\n"
    "v_add_u32 v108, v108, v109\n"
    "s_lshr_b32 s97, %15, 3\n"
    "s_mul_i32 s97, s97, 81920\n"
    "s_mul_i32 s82, s60, 20480\n"
    "s_add_u32 s97, s97, s82\n"
    "s_mov_b32 s76, 0\n"
    "s_mov_b32 s80, 0\n"
    "s_add_u32 s79, s94, 65536\n"
    "s_add_u32 s81, s94, 131072\n"
    "v_mov_b32 v80, v84\n"
    "v_mov_b32 v81, v85\n"
    "v_mov_b32 v82, v86\n"
    "v_mov_b32 v83, v87\n"
    "s_waitcnt lgkmcnt(0)\n"
    "s_add_u32 s63, s63, 1\n"
    "s_waitcnt vmcnt(22)\n"
    "s_barrier\n"
    "ds_read_b64_tr_b16 v[32:33], v88\n"
    "ds_read_b64_tr_b16 v[34:35], v89\n"
    "ds_read_b128 v[0:3], v80\n"
    "ds_read_b64_tr_b16 v[36:37], v88 offset:512\n"
    "ds_read_b64_tr_b16 v[38:39], v89 offset:512\n"
    "ds_read_b128 v[4:7], v80 offset:4096\n"
    "ds_read_b64_tr_b16 v[40:41], v88 offset:1024\n"
    "ds_read_b64_tr_b16 v[42:43], v89 offset:1024\n"
    "ds_read_b128 v[8:11], v80 offset:8192\n"
    "ds_read_b64_tr_b16 v[44:45], v88 offset:1536\n"
    "ds_read_b64_tr_b16 v[46:47], v89 offset:1536\n"
    "ds_read_b128 v[12:15], v80 offset:12288\n"
    "s_add_u32 m0, s81, 0\n"
    "s_nop 0\n"
    "buffer_load_dwordx4 v72, s[40:43], s78 offen lds\n"
    "buffer_load_dwordx4 v73, s[40:43], s78 offen offset:1024 lds\n"
    "buffer_load_dwordx4 v74, s[40:43], s78 offen offset:2048 lds\n"
    "buffer_load_dwordx4 v75, s[40:43], s78 offen offset:3072 lds\n"
    "s_cmp_ge_u32 s60, 2\n"
    "s_cbranch_scc1 L_kvV_%=\n"
    "L_kvloop0_%=:\n"
    "s_waitcnt lgkmcnt(0)\n"
    "v_mfma_f32_32x32x16_bf16 a[0:15], v[32:35], v[0:3], a[0:15]\n"
    "ds_read_b64_tr_b16 v[48:49], v88 offset:4096\n"
    "s_add_u32 m0, s81, 4096\n"
    "buffer_load_ushort v203, v240, s[44:47], s88 offen\n"
    "v_mfma_f32_32x32x16_bf16 a[64:79], v[32:35], v[4:7], a[64:79]\n"
    "buffer_load_dwordx4 v76, s[40:43], s78 offen lds\n"
    "ds_read_b64_tr_b16 v[50:51], v89 offset:4096\n"
    "v_mfma_f32_32x32x16_bf16 a[128:143], v[32:35], v[8:11], a[128:143]\n"
    "ds_read_b128 v[16:19], v81\n"
    "s_cmp_eq_u32 s76, 0\n"
    "s_cselect_b32 s91, s93, 65535\n"
    "buffer_load_ushort v204, v240, s[44:47], s88 offen offset:1280\n"
    "v_mfma_f32_32x32x16_bf16 a[192:207], v[32:35], v[12:15], a[192:207]\n"
    "ds_read_b64_tr_b16 v[52:53], v88 offset:4608\n"
    "s_sub_u32 s96, s76, 1\n"
    "s_cmp_lt_u32 s96, 4\n"
    "s_cselect_b32 s58, s90, 0\n"
    "buffer_load_ushort v205, v240, s[44:47], s88 offen offset:2560\n"
    "v_mfma_f32_32x32x16_bf16 a[16:31], v[36:39], v[0:3], a[16:31]\n"
    "ds_read_b64_tr_b16 v[54:55], v89 offset:4608\n"
    "v_mul_f32 v237, v218, v217\n"
    "v_fma_f32 v237, v219, v216, v237\n"
    "v_fma_f32 v237, v220, v215, v237\n"
    "buffer_load_ushort v206, v240, s[44:47], s88 offen offset:3840\n"
    "v_mfma_f32_32x32x16_bf16 a[80:95], v[36:39], v[4:7], a[80:95]\n"
    "buffer_load_dwordx4 v77, s[40:43], s78 offen offset:1024 lds\n"
    "ds_read_b128 v[20:23], v81 offset:4096\n"
    "v_fma_f32 v237, v221, v214, v237\n"
    "v_cvt_pk_bf16_f32 v237, 0, v237\n"
    "v_mfma_f32_32x32x16_bf16 a[144:159], v[36:39], v[8:11], a[144:159]\n"
    "ds_read_b64_tr_b16 v[56:57], v88 offset:5120\n"
    "v_mul_f32 v229, v237, v229\n"
    "v_mul_f32 v238, v219, v217\n"
    "v_fma_f32 v238, v220, v216, v238\n"
    "buffer_load_ushort v207, v240, s[44:47], s92 offen\n"
    "v_mfma_f32_32x32x16_bf16 a[208:223], v[36:39], v[12:15], a[208:223]\n"
    "ds_read_b64_tr_b16 v[58:59], v89 offset:5120\n"
    "v_fma_f32 v238, v221, v215, v238\n"
    "v_fma_f32 v238, v222, v214, v238\n"
    "v_cvt_pk_bf16_f32 v238, 0, v238\n"
    "buffer_load_ushort v208, v240, s[44:47], s92 offen offset:1280\n"
    "v_mfma_f32_32x32x16_bf16 a[32:47], v[40:43], v[0:3], a[32:47]\n"
    "ds_read_b128 v[24:27], v81 offset:8192\n"
    "v_mul_f32 v230, v238, v230\n"
    "v_mul_f32 v237, v220, v217\n"
    "v_fma_f32 v237, v221, v216, v237\n"
    "buffer_load_ushort v209, v240, s[44:47], s92 offen offset:2560\n"
    "v_mfma_f32_32x32x16_bf16 a[96:111], v[40:43], v[4:7], a[96:111]\n"
    "buffer_load_dwordx4 v78, s[40:43], s78 offen offset:2048 lds\n"
    "ds_read_b64_tr_b16 v[60:61], v88 offset:5632\n"
    "v_fma_f32 v237, v222, v215, v237\n"
    "v_fma_f32 v237, v223, v214, v237\n"
    "v_mfma_f32_32x32x16_bf16 a[160:175], v[40:43], v[8:11], a[160:175]\n"
    "ds_read_b64_tr_b16 v[62:63], v89 offset:5632\n"
    "v_cvt_pk_bf16_f32 v237, 0, v237\n"
    "v_mul_f32 v231, v237, v231\n"
    "v_mul_f32 v238, v221, v217\n"
    "buffer_load_ushort v210, v240, s[44:47], s92 offen offset:3840\n"
    "v_mfma_f32_32x32x16_bf16 a[224:239], v[40:43], v[12:15], a[224:239]\n"
    "ds_read_b128 v[28:31], v81 offset:12288\n"
    "v_fma_f32 v238, v222, v216, v238\n"
    "v_fma_f32 v238, v223, v215, v238\n"
    "v_fma_f32 v238, v224, v214, v238\n"
    "v_mfma_f32_32x32x16_bf16 a[48:63], v[44:47], v[0:3], a[48:63]\n"
    "v_cvt_pk_bf16_f32 v238, 0, v238\n"
    "v_mul_f32 v232, v238, v232\n"
    "v_mul_f32 v237, v222, v217\n"
    "v_fma_f32 v237, v223, v216, v237\n"
    "v_mfma_f32_32x32x16_bf16 a[112:127], v[44:47], v[4:7], a[112:127]\n"
    "buffer_load_dwordx4 v79, s[40:43], s78 offen offset:3072 lds\n"
    "v_fma_f32 v237, v224, v215, v237\n"
    "v_fma_f32 v237, v225, v214, v237\n"
    "v_cvt_pk_bf16_f32 v237, 0, v237\n"
    "v_mfma_f32_32x32x16_bf16 a[176:191], v[44:47], v[8:11], a[176:191]\n"
    "v_mul_f32 v233, v237, v233\n"
    "v_mul_f32 v238, v223, v217\n"
    "v_fma_f32 v238, v224, v216, v238\n"
    "v_fma_f32 v238, v225, v215, v238\n"
    "v_mfma_f32_32x32x16_bf16 a[240:255], v[44:47], v[12:15], a[240:255]\n"
    "v_fma_f32 v238, v226, v214, v238\n"
    "v_cvt_pk_bf16_f32 v238, 0, v238\n"
    "v_mul_f32 v234, v238, v234\n"
    "v_mul_f32 v237, v224, v217\n"
    "s_waitcnt lgkmcnt(0)\n"
    "v_mfma_f32_32x32x16_bf16 a[0:15], v[48:51], v[16:19], a[0:15]\n"
    "ds_read_b64_tr_b16 v[32:33], v88 offset:8192\n"
    "s_add_u32 m0, s79, 0\n"
    "v_fma_f32 v237, v225, v216, v237\n"
    "v_fma_f32 v237, v226, v215, v237\n"
    "s_add_u32 s88, s88, 10240\n"
    "s_add_u32 s92, s88, 5120\n"
    "v_mfma_f32_32x32x16_bf16 a[64:79], v[48:51], v[20:23], a[64:79]\n"
    "buffer_load_dwordx4 v64, s[36:39], s77 offen lds\n"
    "ds_read_b64_tr_b16 v[34:35], v89 offset:8192\n"
    "v_fma_f32 v237, v227, v214, v237\n"
    "v_cvt_pk_bf16_f32 v237, 0, v237\n"
    "v_mfma_f32_32x32x16_bf16 a[128:143], v[48:51], v[24:27], a[128:143]\n"
    "ds_read_b128 v[0:3], v82\n"
    "v_mul_f32 v235, v237, v235\n"
    "v_mul_f32 v238, v225, v217\n"
    "v_fma_f32 v238, v226, v216, v238\n"
    "v_mfma_f32_32x32x16_bf16 a[192:207], v[48:51], v[28:31], a[192:207]\n"
    "ds_read_b64_tr_b16 v[36:37], v88 offset:8704\n"
    "v_fma_f32 v238, v227, v215, v238\n"
    "v_fma_f32 v238, v228, v214, v238\n"
    "v_cvt_pk_bf16_f32 v238, 0, v238\n"
    "v_mfma_f32_32x32x16_bf16 a[16:31], v[52:55], v[16:19], a[16:31]\n"
    "ds_read_b64_tr_b16 v[38:39], v89 offset:8704\n"
    "v_mul_f32 v236, v238, v236\n"
    "v_cvt_pk_bf16_f32 v244, v236, v235\n"
    "v_cvt_pk_bf16_f32 v245, v234, v233\n"
    "v_mfma_f32_32x32x16_bf16 a[80:95], v[52:55], v[20:23], a[80:95]\n"
    "buffer_load_dwordx4 v65, s[36:39], s77 offen offset:1024 lds\n"
    "ds_read_b128 v[4:7], v82 offset:4096\n"
    "v_cvt_pk_bf16_f32 v246, v232, v231\n"
    "v_cvt_pk_bf16_f32 v247, v230, v229\n"
    "v_mfma_f32_32x32x16_bf16 a[144:159], v[52:55], v[24:27], a[144:159]\n"
    "ds_read_b64_tr_b16 v[40:41], v88 offset:9216\n"
    "v_mfma_f32_32x32x16_bf16 a[208:223], v[52:55], v[28:31], a[208:223]\n"
    "ds_read_b64_tr_b16 v[42:43], v89 offset:9216\n"
    "v_mfma_f32_32x32x16_bf16 a[32:47], v[56:59], v[16:19], a[32:47]\n"
    "ds_read_b128 v[8:11], v82 offset:8192\n"
    "v_mfma_f32_32x32x16_bf16 a[96:111], v[56:59], v[20:23], a[96:111]\n"
    "buffer_load_dwordx4 v66, s[36:39], s77 offen offset:2048 lds\n"
    "ds_read_b64_tr_b16 v[44:45], v88 offset:9728\n"
    "v_mfma_f32_32x32x16_bf16 a[160:175], v[56:59], v[24:27], a[160:175]\n"
    "ds_read_b64_tr_b16 v[46:47], v89 offset:9728\n"
    "v_mfma_f32_32x32x16_bf16 a[224:239], v[56:59], v[28:31], a[224:239]\n"
    "ds_read_b128 v[12:15], v82 offset:12288\n"
    "v_mfma_f32_32x32x16_bf16 a[48:63], v[60:63], v[16:19], a[48:63]\n"
    "v_mfma_f32_32x32x16_bf16 a[112:127], v[60:63], v[20:23], a[112:127]\n"
    "buffer_load_dwordx4 v67, s[36:39], s77 offen offset:3072 lds\n"
    "v_mfma_f32_32x32x16_bf16 a[176:191], v[60:63], v[24:27], a[176:191]\n"
    "v_mfma_f32_32x32x16_bf16 a[240:255], v[60:63], v[28:31], a[240:255]\n"
    "s_waitcnt lgkmcnt(0)\n"
    "v_mfma_f32_32x32x16_bf16 a[0:15], v[32:35], v[0:3], a[0:15]\n"
    "ds_read_b64_tr_b16 v[48:49], v88 offset:12288\n"
    "s_add_u32 m0, s79, 4096\n"
    "v_mfma_f32_32x32x16_bf16 a[64:79], v[32:35], v[4:7], a[64:79]\n"
    "buffer_load_dwordx4 v68, s[36:39], s77 offen lds\n"
    "ds_read_b64_tr_b16 v[50:51], v89 offset:12288\n"
    "v_mfma_f32_32x32x16_bf16 a[128:143], v[32:35], v[8:11], a[128:143]\n"
    "ds_read_b128 v[16:19], v83\n"
    "v_mfma_f32_32x32x16_bf16 a[192:207], v[32:35], v[12:15], a[192:207]\n"
    "ds_read_b64_tr_b16 v[52:53], v88 offset:12800\n"
    "v_mfma_f32_32x32x16_bf16 a[16:31], v[36:39], v[0:3], a[16:31]\n"
    "ds_read_b64_tr_b16 v[54:55], v89 offset:12800\n"
    "v_mfma_f32_32x32x16_bf16 a[80:95], v[36:39], v[4:7], a[80:95]\n"
    "buffer_load_dwordx4 v69, s[36:39], s77 offen offset:1024 lds\n"
    "ds_read_b128 v[20:23], v83 offset:4096\n"
    "v_mfma_f32_32x32x16_bf16 a[144:159], v[36:39], v[8:11], a[144:159]\n"
    "ds_read_b64_tr_b16 v[56:57], v88 offset:13312\n"
    "v_mfma_f32_32x32x16_bf16 a[208:223], v[36:39], v[12:15], a[208:223]\n"
    "ds_read_b64_tr_b16 v[58:59], v89 offset:13312\n"
    "v_mfma_f32_32x32x16_bf16 a[32:47], v[40:43], v[0:3], a[32:47]\n"
    "ds_read_b128 v[24:27], v83 offset:8192\n"
    "s_xor_b32 s81, s81, 229376\n"
    "s_add_u32 s78, s78, 524288\n"
    "v_mfma_f32_32x32x16_bf16 a[96:111], v[40:43], v[4:7], a[96:111]\n"
    "buffer_load_dwordx4 v70, s[36:39], s77 offen offset:2048 lds\n"
    "ds_read_b64_tr_b16 v[60:61], v88 offset:13824\n"
    "v_mfma_f32_32x32x16_bf16 a[160:175], v[40:43], v[8:11], a[160:175]\n"
    "ds_read_b64_tr_b16 v[62:63], v89 offset:13824\n"
    "buffer_store_dwordx4 v[244:247], v251, s[56:59], s89 offen\n"
    "s_sub_u32 s89, s89, 10240\n"
    "v_mfma_f32_32x32x16_bf16 a[224:239], v[40:43], v[12:15], a[224:239]\n"
    "ds_read_b128 v[28:31], v83 offset:12288\n"
    "v_mfma_f32_32x32x16_bf16 a[48:63], v[44:47], v[0:3], a[48:63]\n"
    "s_add_u32 s82, s80, s94\n"
    "s_add_u32 s80, s80, 32768\n"
    "s_cmp_eq_u32 s80, 98304\n"
    "v_mfma_f32_32x32x16_bf16 a[112:127], v[44:47], v[4:7], a[112:127]\n"
    "buffer_load_dwordx4 v71, s[36:39], s77 offen offset:3072 lds\n"
    "s_cselect_b32 s80, 0, s80\n"
    "v_add_u32 v80, s80, v84\n"
    "v_add_u32 v81, s80, v85\n"
    "v_mfma_f32_32x32x16_bf16 a[176:191], v[44:47], v[8:11], a[176:191]\n"
    "v_add_u32 v82, s80, v86\n"
    "v_add_u32 v83, s80, v87\n"
    "v_xor_b32 v88, 229376, v88\n"
    "s_add_u32 s77, s77, 128\n"
    "s_mov_b32 s79, s82\n"
    "v_mfma_f32_32x32x16_bf16 a[240:255], v[44:47], v[12:15], a[240:255]\n"
    "v_xor_b32 v89, 229376, v89\n"
    "s_waitcnt vmcnt(8) lgkmcnt(0)\n"
    "s_barrier\n"
    "v_mfma_f32_32x32x16_bf16 a[0:15], v[48:51], v[16:19], a[0:15]\n"
    "ds_read_b64_tr_b16 v[32:33], v88\n"
    "s_add_u32 m0, s81, 0\n"
    "v_mfma_f32_32x32x16_bf16 a[64:79], v[48:51], v[20:23], a[64:79]\n"
    "buffer_load_dwordx4 v72, s[40:43], s78 offen lds\n"
    "ds_read_b64_tr_b16 v[34:35], v89\n"
    "v_and_b32 v192, s91, v192\n"
    "v_and_b32 v193, s91, v193\n"
    "v_and_b32 v194, s91, v194\n"
    "v_mfma_f32_32x32x16_bf16 a[128:143], v[48:51], v[24:27], a[128:143]\n"
    "ds_read_b128 v[0:3], v80\n"
    "v_lshlrev_b32 v218, 16, v192\n"
    "v_lshlrev_b32 v219, 16, v193\n"
    "v_lshlrev_b32 v220, 16, v194\n"
    "v_lshlrev_b32 v221, 16, v195\n"
    "v_mfma_f32_32x32x16_bf16 a[192:207], v[48:51], v[28:31], a[192:207]\n"
    "ds_read_b64_tr_b16 v[36:37], v88 offset:512\n"
    "buffer_load_dword v109, v131, s[52:55], s97 offen\n"
    "v_lshlrev_b32 v222, 16, v196\n"
    "v_lshlrev_b32 v223, 16, v197\n"
    "v_lshlrev_b32 v224, 16, v198\n"
    "v_mfma_f32_32x32x16_bf16 a[16:31], v[52:55], v[16:19], a[16:31]\n"
    "ds_read_b64_tr_b16 v[38:39], v89 offset:512\n"
    "s_add_u32 s97, s97, 4096\n"
    "v_lshlrev_b32 v225, 16, v199\n"
    "v_lshlrev_b32 v226, 16, v200\n"
    "v_lshlrev_b32 v227, 16, v201\n"
    "v_mfma_f32_32x32x16_bf16 a[80:95], v[52:55], v[20:23], a[80:95]\n"
    "buffer_load_dwordx4 v73, s[40:43], s78 offen offset:1024 lds\n"
    "ds_read_b128 v[4:7], v80 offset:4096\n"
    "v_lshlrev_b32 v228, 16, v202\n"
    "v_lshlrev_b32 v229, 16, v203\n"
    "v_lshlrev_b32 v230, 16, v204\n"
    "v_mfma_f32_32x32x16_bf16 a[144:159], v[52:55], v[24:27], a[144:159]\n"
    "ds_read_b64_tr_b16 v[40:41], v88 offset:1024\n"
    "v_lshlrev_b32 v231, 16, v205\n"
    "v_lshlrev_b32 v232, 16, v206\n"
    "v_lshlrev_b32 v233, 16, v207\n"
    "v_lshlrev_b32 v234, 16, v208\n"
    "buffer_load_ushort v195, v240, s[48:51], s88 offen\n"
    "v_mfma_f32_32x32x16_bf16 a[208:223], v[52:55], v[28:31], a[208:223]\n"
    "ds_read_b64_tr_b16 v[42:43], v89 offset:1024\n"
    "v_lshlrev_b32 v235, 16, v209\n"
    "v_lshlrev_b32 v236, 16, v210\n"
    "v_mov_b32 v192, v200\n"
    "v_mov_b32 v193, v201\n"
    "buffer_load_ushort v196, v240, s[48:51], s88 offen offset:1280\n"
    "v_mfma_f32_32x32x16_bf16 a[32:47], v[56:59], v[16:19], a[32:47]\n"
    "ds_read_b128 v[8:11], v80 offset:8192\n"
    "v_mov_b32 v194, v202\n"
    "v_lshlrev_b32 v214, 16, v212\n"
    "v_and_b32 v215, 4294901760, v212\n"
    "v_lshlrev_b32 v216, 16, v213\n"
    "buffer_load_ushort v197, v240, s[48:51], s88 offen offset:2560\n"
    "v_mfma_f32_32x32x16_bf16 a[96:111], v[56:59], v[20:23], a[96:111]\n"
    "buffer_load_dwordx4 v74, s[40:43], s78 offen offset:2048 lds\n"
    "ds_read_b64_tr_b16 v[44:45], v88 offset:1536\n"
    "v_and_b32 v217, 4294901760, v213\n"
    "v_mfma_f32_32x32x16_bf16 a[160:175], v[56:59], v[24:27], a[160:175]\n"
    "ds_read_b64_tr_b16 v[46:47], v89 offset:1536\n"
    "buffer_load_ushort v198, v240, s[48:51], s88 offen offset:3840\n"
    "v_mfma_f32_32x32x16_bf16 a[224:239], v[56:59], v[28:31], a[224:239]\n"
    "ds_read_b128 v[12:15], v80 offset:12288\n"
    "buffer_load_ushort v199, v240, s[48:51], s92 offen\n"
    "v_mfma_f32_32x32x16_bf16 a[48:63], v[60:63], v[16:19], a[48:63]\n"
    "buffer_load_ushort v200, v240, s[48:51], s92 offen offset:1280\n"
    "v_mfma_f32_32x32x16_bf16 a[112:127], v[60:63], v[20:23], a[112:127]\n"
    "buffer_load_dwordx4 v75, s[40:43], s78 offen offset:3072 lds\n"
    "v_mfma_f32_32x32x16_bf16 a[176:191], v[60:63], v[24:27], a[176:191]\n"
    "buffer_load_ushort v201, v240, s[48:51], s92 offen offset:2560\n"
    "v_mfma_f32_32x32x16_bf16 a[240:255], v[60:63], v[28:31], a[240:255]\n"
    "buffer_load_ushort v202, v240, s[48:51], s92 offen offset:3840\n"
    "s_add_u32 s76, s76, 1\n"
    "s_cmp_lt_u32 s76, 6\n"
    "s_cbranch_scc1 L_kvloop0_%=\n"
    "s_waitcnt lgkmcnt(0)\n"
    "v_mfma_f32_32x32x16_bf16 a[0:15], v[32:35], v[0:3], a[0:15]\n"
    "ds_read_b64_tr_b16 v[48:49], v88 offset:4096\n"
    "s_add_u32 m0, s81, 4096\n"
    "v_mfma_f32_32x32x16_bf16 a[64:79], v[32:35], v[4:7], a[64:79]\n"
    "buffer_load_dwordx4 v76, s[40:43], s78 offen lds\n"
    "ds_read_b64_tr_b16 v[50:51], v89 offset:4096\n"
    "v_mfma_f32_32x32x16_bf16 a[128:143], v[32:35], v[8:11], a[128:143]\n"
    "ds_read_b128 v[16:19], v81\n"
    "s_add_u32 s36, s64, 0\n"
    "v_mfma_f32_32x32x16_bf16 a[192:207], v[32:35], v[12:15], a[192:207]\n"
    "ds_read_b64_tr_b16 v[52:53], v88 offset:4608\n"
    "s_addc_u32 s37, s65, 0\n"
    "v_mfma_f32_32x32x16_bf16 a[16:31], v[36:39], v[0:3], a[16:31]\n"
    "ds_read_b64_tr_b16 v[54:55], v89 offset:4608\n"
    "s_and_b32 s37, s37, 65535\n"
    "s_mov_b32 s56, %24\n"
    "s_and_b32 s57, %25, 0xffff\n"
    "s_mov_b32 s58, 1280\n"
    "s_mov_b32 s59, 131072\n"
    "v_mfma_f32_32x32x16_bf16 a[80:95], v[36:39], v[4:7], a[80:95]\n"
    "buffer_load_dwordx4 v77, s[40:43], s78 offen offset:1024 lds\n"
    "ds_read_b128 v[20:23], v81 offset:4096\n"
    "s_mov_b32 s38, 2621440\n"
    "s_lshl_b32 s93, %15, 3\n"
    "s_lshl_b32 s96, s60, 1\n"
    "s_add_u32 s93, s93, s96\n"
    "s_mul_i32 s96, s93, 1280\n"
    "v_mfma_f32_32x32x16_bf16 a[144:159], v[36:39], v[8:11], a[144:159]\n"
    "ds_read_b64_tr_b16 v[56:57], v88 offset:5120\n"
    "s_mov_b32 s39, 131072\n"
    "v_lshlrev_b32 v233, 2, v250\n"
    "v_mfma_f32_32x32x16_bf16 a[208:223], v[36:39], v[12:15], a[208:223]\n"
    "ds_read_b64_tr_b16 v[58:59], v89 offset:5120\n"
    "s_sub_u32 s82, 15, s62\n"
    "v_mfma_f32_32x32x16_bf16 a[32:47], v[40:43], v[0:3], a[32:47]\n"
    "ds_read_b128 v[24:27], v81 offset:8192\n"
    "s_cmp_lt_u32 s60, 2\n"
    "v_mfma_f32_32x32x16_bf16 a[96:111], v[40:43], v[4:7], a[96:111]\n"
    "buffer_load_dwordx4 v78, s[40:43], s78 offen offset:2048 lds\n"
    "ds_read_b64_tr_b16 v[60:61], v88 offset:5632\n"
    "s_cselect_b32 s82, s62, s82\n"
    "v_mfma_f32_32x32x16_bf16 a[160:175], v[40:43], v[8:11], a[160:175]\n"
    "ds_read_b64_tr_b16 v[62:63], v89 offset:5632\n"
    "s_lshl_b32 s82, s82, 7\n"
    "buffer_load_dword v218, v233, s[36:39], s96 offen\n"
    "buffer_load_dword v219, v233, s[36:39], s96 offen offset:256\n"
    "buffer_load_dword v220, v233, s[36:39], s96 offen offset:512\n"
    "v_mfma_f32_32x32x16_bf16 a[224:239], v[40:43], v[12:15], a[224:239]\n"
    "ds_read_b128 v[28:31], v81 offset:12288\n"
    "s_and_b32 s83, s60, 1\n"
    "buffer_load_dword v221, v233, s[36:39], s96 offen offset:768\n"
    "buffer_load_dword v222, v233, s[36:39], s96 offen offset:1024\n"
    "buffer_load_dword v223, v233, s[36:39], s96 offen offset:1280\n"
    "v_mfma_f32_32x32x16_bf16 a[48:63], v[44:47], v[0:3], a[48:63]\n"
    "s_lshl_b32 s83, s83, 6\n"
    "buffer_load_dword v224, v233, s[36:39], s96 offen offset:1536\n"
    "buffer_load_dword v225, v233, s[36:39], s96 offen offset:1792\n"
    "buffer_load_dword v226, v233, s[36:39], s96 offen offset:2048\n"
    "v_mfma_f32_32x32x16_bf16 a[112:127], v[44:47], v[4:7], a[112:127]\n"
    "buffer_load_dwordx4 v79, s[40:43], s78 offen offset:3072 lds\n"
    "s_add_u32 s82, s82, s83\n"
    "v_mfma_f32_32x32x16_bf16 a[176:191], v[44:47], v[8:11], a[176:191]\n"
    "s_mul_i32 s77, s82, 1280\n"
    "buffer_load_dword v227, v233, s[36:39], s96 offen offset:2304\n"
    "buffer_load_dword v228, v233, s[56:59], 0 offen\n"
    "buffer_load_dword v229, v233, s[56:59], 0 offen offset:256\n"
    "v_mfma_f32_32x32x16_bf16 a[240:255], v[44:47], v[12:15], a[240:255]\n"
    "s_lshl_b32 s89, %15, 2\n"
    "s_add_u32 s89, s89, s60\n"
    "s_lshr_b32 s90, s89, 3\n"
    "s_and_b32 s89, s89, 7\n"
    "buffer_load_dword v230, v233, s[56:59], 0 offen offset:512\n"
    "buffer_load_dword v231, v233, s[56:59], 0 offen offset:768\n"
    "buffer_load_dword v232, v233, s[56:59], 0 offen offset:1024\n"
    "s_waitcnt vmcnt(19) lgkmcnt(0)\n"
    "v_mfma_f32_32x32x16_bf16 a[0:15], v[48:51], v[16:19], a[0:15]\n"
    "ds_read_b64_tr_b16 v[32:33], v88 offset:8192\n"
    "s_add_u32 m0, s79, 0\n"
    "v_mfma_f32_32x32x16_bf16 a[64:79], v[48:51], v[20:23], a[64:79]\n"
    "buffer_load_dwordx4 v112, s[36:39], s77 offen lds\n"
    "ds_read_b64_tr_b16 v[34:35], v89 offset:8192\n"
    "v_mfma_f32_32x32x16_bf16 a[128:143], v[48:51], v[24:27], a[128:143]\n"
    "ds_read_b128 v[0:3], v82\n"
    "s_mul_i32 s92, s89, 5\n"
    "s_lshl_b32 s91, s92, 17\n"
    "s_lshl_b32 s88, s90, 10\n"
    "s_add_u32 s91, s91, s88\n"
    "v_mfma_f32_32x32x16_bf16 a[192:207], v[48:51], v[28:31], a[192:207]\n"
    "ds_read_b64_tr_b16 v[36:37], v88 offset:8704\n"
    "s_mul_i32 s90, s90, 40960\n"
    "s_lshl_b32 s92, s92, 5\n"
    "s_add_u32 s89, s90, s92\n"
    "s_add_u32 s90, s89, 10240\n"
    "v_mfma_f32_32x32x16_bf16 a[16:31], v[52:55], v[16:19], a[16:31]\n"
    "ds_read_b64_tr_b16 v[38:39], v89 offset:8704\n"
    "s_mov_b32 s48, %16\n"
    "s_and_b32 s49, %17, 0xffff\n"
    "s_mov_b32 s50, 5242880\n"
    "s_mov_b32 s51, 131072\n"
    "v_mfma_f32_32x32x16_bf16 a[80:95], v[52:55], v[20:23], a[80:95]\n"
    "buffer_load_dwordx4 v113, s[36:39], s77 offen offset:1024 lds\n"
    "ds_read_b128 v[4:7], v82 offset:4096\n"
    "s_mov_b32 s52, s68\n"
    "s_and_b32 s53, s69, 65535\n"
    "s_mov_b32 s54, 5242880\n"
    "v_mfma_f32_32x32x16_bf16 a[144:159], v[52:55], v[24:27], a[144:159]\n"
    "ds_read_b64_tr_b16 v[40:41], v88 offset:9216\n"
    "s_mul_i32 s97, s62, 80\n"
    "s_lshl_b32 s86, s60, 5\n"
    "s_add_u32 s97, s97, s86\n"
    "s_lshl_b32 s97, s97, 13\n"
    "buffer_load_ushort v152, v130, s[48:51], s89 offen offset:0\n"
    "buffer_load_ushort v153, v130, s[48:51], s89 offen offset:1280\n"
    "v_mfma_f32_32x32x16_bf16 a[208:223], v[52:55], v[28:31], a[208:223]\n"
    "ds_read_b64_tr_b16 v[42:43], v89 offset:9216\n"
    "s_lshl_b32 s86, s61, 8\n"
    "s_add_u32 s97, s97, s86\n"
    "buffer_load_ushort v154, v130, s[48:51], s89 offen offset:2560\n"
    "buffer_load_ushort v155, v130, s[48:51], s89 offen offset:3840\n"
    "v_mfma_f32_32x32x16_bf16 a[32:47], v[56:59], v[16:19], a[32:47]\n"
    "ds_read_b128 v[8:11], v82 offset:8192\n"
    "buffer_load_dword v109, v108, s[52:55], s97 offen\n"
    "s_add_u32 s97, s97, 131072\n"
    "v_mfma_f32_32x32x16_bf16 a[96:111], v[56:59], v[20:23], a[96:111]\n"
    "buffer_load_dwordx4 v114, s[36:39], s77 offen offset:2048 lds\n"
    "ds_read_b64_tr_b16 v[44:45], v88 offset:9728\n"
    "v_mfma_f32_32x32x16_bf16 a[160:175], v[56:59], v[24:27], a[160:175]\n"
    "ds_read_b64_tr_b16 v[46:47], v89 offset:9728\n"
    "buffer_load_ushort v160, v130, s[48:51], s89 offen offset:32\n"
    "buffer_load_ushort v161, v130, s[48:51], s89 offen offset:1312\n"
    "v_mfma_f32_32x32x16_bf16 a[224:239], v[56:59], v[28:31], a[224:239]\n"
    "ds_read_b128 v[12:15], v82 offset:12288\n"
    "buffer_load_dword v109, v108, s[52:55], s97 offen\n"
    "buffer_load_ushort v162, v130, s[48:51], s89 offen offset:2592\n"
    "buffer_load_ushort v163, v130, s[48:51], s89 offen offset:3872\n"
    "v_mfma_f32_32x32x16_bf16 a[48:63], v[60:63], v[16:19], a[48:63]\n"
    "v_mfma_f32_32x32x16_bf16 a[112:127], v[60:63], v[20:23], a[112:127]\n"
    "buffer_load_dwordx4 v115, s[36:39], s77 offen offset:3072 lds\n"
    "v_mfma_f32_32x32x16_bf16 a[176:191], v[60:63], v[24:27], a[176:191]\n"
    "s_sub_u32 s40, s68, 1024\n"
    "buffer_load_ushort v168, v130, s[48:51], s89 offen offset:64\n"
    "buffer_load_ushort v169, v130, s[48:51], s89 offen offset:1344\n"
    "v_mfma_f32_32x32x16_bf16 a[240:255], v[60:63], v[28:31], a[240:255]\n"
    "s_subb_u32 s83, s69, 0\n"
    "buffer_load_ushort v170, v130, s[48:51], s89 offen offset:2624\n"
    "buffer_load_ushort v171, v130, s[48:51], s89 offen offset:3904\n"
    "s_waitcnt lgkmcnt(0)\n"
    "v_mfma_f32_32x32x16_bf16 a[0:15], v[32:35], v[0:3], a[0:15]\n"
    "ds_read_b64_tr_b16 v[48:49], v88 offset:12288\n"
    "s_add_u32 m0, s79, 4096\n"
    "s_and_b32 s41, s83, 65535\n"
    "v_mfma_f32_32x32x16_bf16 a[64:79], v[32:35], v[4:7], a[64:79]\n"
    "buffer_load_dwordx4 v116, s[36:39], s77 offen lds\n"
    "ds_read_b64_tr_b16 v[50:51], v89 offset:12288\n"
    "s_mov_b32 s42, 5243904\n"
    "v_mfma_f32_32x32x16_bf16 a[128:143], v[32:35], v[8:11], a[128:143]\n"
    "ds_read_b128 v[16:19], v83\n"
    "s_lshl_b32 s83, s60, 17\n"
    "buffer_load_ushort v176, v130, s[48:51], s89 offen offset:96\n"
    "buffer_load_ushort v177, v130, s[48:51], s89 offen offset:1376\n"
    "v_mfma_f32_32x32x16_bf16 a[192:207], v[32:35], v[12:15], a[192:207]\n"
    "ds_read_b64_tr_b16 v[52:53], v88 offset:12800\n"
    "s_lshl_b32 s84, s61, 8\n"
    "buffer_load_ushort v178, v130, s[48:51], s89 offen offset:2656\n"
    "buffer_load_ushort v179, v130, s[48:51], s89 offen offset:3936\n"
    "v_mfma_f32_32x32x16_bf16 a[16:31], v[36:39], v[0:3], a[16:31]\n"
    "ds_read_b64_tr_b16 v[54:55], v89 offset:12800\n"
    "s_add_u32 s78, s83, s84\n"
    "v_mfma_f32_32x32x16_bf16 a[80:95], v[36:39], v[4:7], a[80:95]\n"
    "buffer_load_dwordx4 v117, s[36:39], s77 offen offset:1024 lds\n"
    "ds_read_b128 v[20:23], v83 offset:4096\n"
    "s_lshl_b32 s87, s60, 12\n"
    "v_mfma_f32_32x32x16_bf16 a[144:159], v[36:39], v[8:11], a[144:159]\n"
    "ds_read_b64_tr_b16 v[56:57], v88 offset:13312\n"
    "buffer_load_ushort v184, v130, s[48:51], s89 offen offset:128\n"
    "buffer_load_ushort v185, v130, s[48:51], s89 offen offset:1408\n"
    "v_mfma_f32_32x32x16_bf16 a[208:223], v[36:39], v[12:15], a[208:223]\n"
    "ds_read_b64_tr_b16 v[58:59], v89 offset:13312\n"
    "buffer_load_ushort v186, v130, s[48:51], s89 offen offset:2688\n"
    "buffer_load_ushort v187, v130, s[48:51], s89 offen offset:3968\n"
    "v_mfma_f32_32x32x16_bf16 a[32:47], v[40:43], v[0:3], a[32:47]\n"
    "ds_read_b128 v[24:27], v83 offset:8192\n"
    "v_mfma_f32_32x32x16_bf16 a[96:111], v[40:43], v[4:7], a[96:111]\n"
    "buffer_load_dwordx4 v118, s[36:39], s77 offen offset:2048 lds\n"
    "ds_read_b64_tr_b16 v[60:61], v88 offset:13824\n"
    "v_mfma_f32_32x32x16_bf16 a[160:175], v[40:43], v[8:11], a[160:175]\n"
    "ds_read_b64_tr_b16 v[62:63], v89 offset:13824\n"
    "buffer_load_ushort v156, v130, s[48:51], s90 offen offset:0\n"
    "buffer_load_ushort v157, v130, s[48:51], s90 offen offset:1280\n"
    "v_mfma_f32_32x32x16_bf16 a[224:239], v[40:43], v[12:15], a[224:239]\n"
    "ds_read_b128 v[28:31], v83 offset:12288\n"
    "buffer_load_ushort v158, v130, s[48:51], s90 offen offset:2560\n"
    "buffer_load_ushort v159, v130, s[48:51], s90 offen offset:3840\n"
    "v_mfma_f32_32x32x16_bf16 a[48:63], v[44:47], v[0:3], a[48:63]\n"
    "s_add_u32 s82, s80, s94\n"
    "s_add_u32 s80, s80, 32768\n"
    "s_cmp_eq_u32 s80, 98304\n"
    "v_mfma_f32_32x32x16_bf16 a[112:127], v[44:47], v[4:7], a[112:127]\n"
    "buffer_load_dwordx4 v119, s[36:39], s77 offen offset:3072 lds\n"
    "s_cselect_b32 s80, 0, s80\n"
    "v_add_u32 v80, s80, v84\n"
    "v_add_u32 v81, s80, v85\n"
    "v_mfma_f32_32x32x16_bf16 a[176:191], v[44:47], v[8:11], a[176:191]\n"
    "v_add_u32 v82, s80, v86\n"
    "v_add_u32 v83, s80, v87\n"
    "v_xor_b32 v88, 229376, v88\n"
    "s_add_u32 s77, s77, 128\n"
    "s_mov_b32 s79, s82\n"
    "v_mfma_f32_32x32x16_bf16 a[240:255], v[44:47], v[12:15], a[240:255]\n"
    "v_xor_b32 v89, 229376, v89\n"
    "s_waitcnt vmcnt(8) lgkmcnt(0)\n"
    "s_barrier\n"
    "v_mfma_f32_32x32x16_bf16 a[0:15], v[48:51], v[16:19], a[0:15]\n"
    "ds_read_b64_tr_b16 v[32:33], v88\n"
    "s_add_u32 m0, s87, 98304\n"
    "v_mfma_f32_32x32x16_bf16 a[64:79], v[48:51], v[20:23], a[64:79]\n"
    "buffer_load_dwordx4 v120, s[40:43], s78 offen lds\n"
    "ds_read_b64_tr_b16 v[34:35], v89\n"
    "v_mfma_f32_32x32x16_bf16 a[128:143], v[48:51], v[24:27], a[128:143]\n"
    "ds_read_b128 v[0:3], v80\n"
    "buffer_load_ushort v164, v130, s[48:51], s90 offen offset:32\n"
    "buffer_load_ushort v165, v130, s[48:51], s90 offen offset:1312\n"
    "v_mfma_f32_32x32x16_bf16 a[192:207], v[48:51], v[28:31], a[192:207]\n"
    "ds_read_b64_tr_b16 v[36:37], v88 offset:512\n"
    "buffer_load_ushort v166, v130, s[48:51], s90 offen offset:2592\n"
    "buffer_load_ushort v167, v130, s[48:51], s90 offen offset:3872\n"
    "v_mfma_f32_32x32x16_bf16 a[16:31], v[52:55], v[16:19], a[16:31]\n"
    "ds_read_b64_tr_b16 v[38:39], v89 offset:512\n"
    "v_mfma_f32_32x32x16_bf16 a[80:95], v[52:55], v[20:23], a[80:95]\n"
    "buffer_load_dwordx4 v121, s[40:43], s78 offen offset:1024 lds\n"
    "ds_read_b128 v[4:7], v80 offset:4096\n"
    "v_mfma_f32_32x32x16_bf16 a[144:159], v[52:55], v[24:27], a[144:159]\n"
    "ds_read_b64_tr_b16 v[40:41], v88 offset:1024\n"
    "buffer_load_ushort v172, v130, s[48:51], s90 offen offset:64\n"
    "buffer_load_ushort v173, v130, s[48:51], s90 offen offset:1344\n"
    "v_mfma_f32_32x32x16_bf16 a[208:223], v[52:55], v[28:31], a[208:223]\n"
    "ds_read_b64_tr_b16 v[42:43], v89 offset:1024\n"
    "buffer_load_ushort v174, v130, s[48:51], s90 offen offset:2624\n"
    "buffer_load_ushort v175, v130, s[48:51], s90 offen offset:3904\n"
    "v_mfma_f32_32x32x16_bf16 a[32:47], v[56:59], v[16:19], a[32:47]\n"
    "ds_read_b128 v[8:11], v80 offset:8192\n"
    "v_mfma_f32_32x32x16_bf16 a[96:111], v[56:59], v[20:23], a[96:111]\n"
    "buffer_load_dwordx4 v122, s[40:43], s78 offen offset:2048 lds\n"
    "ds_read_b64_tr_b16 v[44:45], v88 offset:1536\n"
    "v_mfma_f32_32x32x16_bf16 a[160:175], v[56:59], v[24:27], a[160:175]\n"
    "ds_read_b64_tr_b16 v[46:47], v89 offset:1536\n"
    "buffer_load_ushort v180, v130, s[48:51], s90 offen offset:96\n"
    "buffer_load_ushort v181, v130, s[48:51], s90 offen offset:1376\n"
    "v_mfma_f32_32x32x16_bf16 a[224:239], v[56:59], v[28:31], a[224:239]\n"
    "ds_read_b128 v[12:15], v80 offset:12288\n"
    "buffer_load_ushort v182, v130, s[48:51], s90 offen offset:2656\n"
    "buffer_load_ushort v183, v130, s[48:51], s90 offen offset:3936\n"
    "v_mfma_f32_32x32x16_bf16 a[48:63], v[60:63], v[16:19], a[48:63]\n"
    "v_mfma_f32_32x32x16_bf16 a[112:127], v[60:63], v[20:23], a[112:127]\n"
    "buffer_load_dwordx4 v123, s[40:43], s78 offen offset:3072 lds\n"
    "v_mfma_f32_32x32x16_bf16 a[176:191], v[60:63], v[24:27], a[176:191]\n"
    "s_add_u32 s78, s78, 524288\n"
    "buffer_load_ushort v188, v130, s[48:51], s90 offen offset:128\n"
    "buffer_load_ushort v189, v130, s[48:51], s90 offen offset:1408\n"
    "v_mfma_f32_32x32x16_bf16 a[240:255], v[60:63], v[28:31], a[240:255]\n"
    "buffer_load_ushort v190, v130, s[48:51], s90 offen offset:2688\n"
    "buffer_load_ushort v191, v130, s[48:51], s90 offen offset:3968\n"
    "s_waitcnt lgkmcnt(0)\n"
    "v_mfma_f32_32x32x16_bf16 a[0:15], v[32:35], v[0:3], a[0:15]\n"
    "ds_read_b64_tr_b16 v[48:49], v88 offset:4096\n"
    "s_add_u32 m0, s87, 114688\n"
    "v_mfma_f32_32x32x16_bf16 a[64:79], v[32:35], v[4:7], a[64:79]\n"
    "buffer_load_dwordx4 v120, s[40:43], s78 offen lds\n"
    "ds_read_b64_tr_b16 v[50:51], v89 offset:4096\n"
    "v_lshlrev_b32 v236, 16, v228\n"
    "v_and_b32 v228, 4294901760, v228\n"
    "v_lshlrev_b32 v237, 16, v218\n"
    "v_mfma_f32_32x32x16_bf16 a[128:143], v[32:35], v[8:11], a[128:143]\n"
    "ds_read_b128 v[16:19], v81\n"
    "v_and_b32 v218, 4294901760, v218\n"
    "v_mul_f32 v237, v237, v236\n"
    "v_mul_f32 v218, v218, v228\n"
    "v_cvt_pk_bf16_f32 v237, 0, v237\n"
    "v_mfma_f32_32x32x16_bf16 a[192:207], v[32:35], v[12:15], a[192:207]\n"
    "ds_read_b64_tr_b16 v[52:53], v88 offset:4608\n"
    "v_cvt_pk_bf16_f32 v218, 0, v218\n"
    "v_add_f32 v218, v218, v237\n"
    "v_mov_b32 v234, v218\n"
    "v_lshlrev_b32 v237, 16, v223\n"
    "v_mfma_f32_32x32x16_bf16 a[16:31], v[36:39], v[0:3], a[16:31]\n"
    "ds_read_b64_tr_b16 v[54:55], v89 offset:4608\n"
    "v_and_b32 v223, 4294901760, v223\n"
    "v_mul_f32 v237, v237, v236\n"
    "v_mul_f32 v223, v223, v228\n"
    "v_cvt_pk_bf16_f32 v237, 0, v237\n"
    "v_mfma_f32_32x32x16_bf16 a[80:95], v[36:39], v[4:7], a[80:95]\n"
    "buffer_load_dwordx4 v121, s[40:43], s78 offen offset:1024 lds\n"
    "ds_read_b128 v[20:23], v81 offset:4096\n"
    "v_cvt_pk_bf16_f32 v223, 0, v223\n"
    "v_add_f32 v223, v223, v237\n"
    "v_mov_b32 v235, v223\n"
    "v_mfma_f32_32x32x16_bf16 a[144:159], v[36:39], v[8:11], a[144:159]\n"
    "ds_read_b64_tr_b16 v[56:57], v88 offset:5120\n"
    "v_lshlrev_b32 v236, 16, v229\n"
    "v_and_b32 v229, 4294901760, v229\n"
    "v_lshlrev_b32 v237, 16, v219\n"
    "v_and_b32 v219, 4294901760, v219\n"
    "v_mfma_f32_32x32x16_bf16 a[208:223], v[36:39], v[12:15], a[208:223]\n"
    "ds_read_b64_tr_b16 v[58:59], v89 offset:5120\n"
    "v_mul_f32 v237, v237, v236\n"
    "v_mul_f32 v219, v219, v229\n"
    "v_cvt_pk_bf16_f32 v237, 0, v237\n"
    "v_cvt_pk_bf16_f32 v219, 0, v219\n"
    "v_mfma_f32_32x32x16_bf16 a[32:47], v[40:43], v[0:3], a[32:47]\n"
    "ds_read_b128 v[24:27], v81 offset:8192\n"
    "v_add_f32 v219, v219, v237\n"
    "v_add_f32 v234, v234, v219\n"
    "v_lshlrev_b32 v237, 16, v224\n"
    "v_and_b32 v224, 4294901760, v224\n"
    "v_mfma_f32_32x32x16_bf16 a[96:111], v[40:43], v[4:7], a[96:111]\n"
    "buffer_load_dwordx4 v122, s[40:43], s78 offen offset:2048 lds\n"
    "ds_read_b64_tr_b16 v[60:61], v88 offset:5632\n"
    "v_mul_f32 v237, v237, v236\n"
    "v_mul_f32 v224, v224, v229\n"
    "v_cvt_pk_bf16_f32 v237, 0, v237\n"
    "v_mfma_f32_32x32x16_bf16 a[160:175], v[40:43], v[8:11], a[160:175]\n"
    "ds_read_b64_tr_b16 v[62:63], v89 offset:5632\n"
    "v_cvt_pk_bf16_f32 v224, 0, v224\n"
    "v_add_f32 v224, v224, v237\n"
    "v_add_f32 v235, v235, v224\n"
    "v_lshlrev_b32 v236, 16, v230\n"
    "v_mfma_f32_32x32x16_bf16 a[224:239], v[40:43], v[12:15], a[224:239]\n"
    "ds_read_b128 v[28:31], v81 offset:12288\n"
    "v_and_b32 v230, 4294901760, v230\n"
    "v_lshlrev_b32 v237, 16, v220\n"
    "v_and_b32 v220, 4294901760, v220\n"
    "v_mul_f32 v237, v237, v236\n"
    "v_mfma_f32_32x32x16_bf16 a[48:63], v[44:47], v[0:3], a[48:63]\n"
    "v_mul_f32 v220, v220, v230\n"
    "v_cvt_pk_bf16_f32 v237, 0, v237\n"
    "v_cvt_pk_bf16_f32 v220, 0, v220\n"
    "v_add_f32 v220, v220, v237\n"
    "v_add_f32 v234, v234, v220\n"
    "v_mfma_f32_32x32x16_bf16 a[112:127], v[44:47], v[4:7], a[112:127]\n"
    "buffer_load_dwordx4 v123, s[40:43], s78 offen offset:3072 lds\n"
    "v_lshlrev_b32 v237, 16, v225\n"
    "v_and_b32 v225, 4294901760, v225\n"
    "v_mul_f32 v237, v237, v236\n"
    "v_mul_f32 v225, v225, v230\n"
    "v_mfma_f32_32x32x16_bf16 a[176:191], v[44:47], v[8:11], a[176:191]\n"
    "s_add_u32 s78, s78, 524288\n"
    "v_cvt_pk_bf16_f32 v237, 0, v237\n"
    "v_cvt_pk_bf16_f32 v225, 0, v225\n"
    "v_add_f32 v225, v225, v237\n"
    "v_add_f32 v235, v235, v225\n"
    "v_mfma_f32_32x32x16_bf16 a[240:255], v[44:47], v[12:15], a[240:255]\n"
    "v_lshlrev_b32 v236, 16, v231\n"
    "v_and_b32 v231, 4294901760, v231\n"
    "v_lshlrev_b32 v237, 16, v221\n"
    "v_and_b32 v221, 4294901760, v221\n"
    "v_mul_f32 v237, v237, v236\n"
    "s_waitcnt lgkmcnt(0)\n"
    "v_mfma_f32_32x32x16_bf16 a[0:15], v[48:51], v[16:19], a[0:15]\n"
    "ds_read_b64_tr_b16 v[32:33], v88 offset:8192\n"
    "s_add_u32 m0, s79, 0\n"
    "v_mul_f32 v221, v221, v231\n"
    "v_cvt_pk_bf16_f32 v237, 0, v237\n"
    "v_cvt_pk_bf16_f32 v221, 0, v221\n"
    "v_mfma_f32_32x32x16_bf16 a[64:79], v[48:51], v[20:23], a[64:79]\n"
    "buffer_load_dwordx4 v112, s[36:39], s77 offen lds\n"
    "ds_read_b64_tr_b16 v[34:35], v89 offset:8192\n"
    "v_add_f32 v221, v221, v237\n"
    "v_add_f32 v234, v234, v221\n"
    "v_lshlrev_b32 v237, 16, v226\n"
    "v_mfma_f32_32x32x16_bf16 a[128:143], v[48:51], v[24:27], a[128:143]\n"
    "ds_read_b128 v[0:3], v82\n"
    "v_and_b32 v226, 4294901760, v226\n"
    "v_mul_f32 v237, v237, v236\n"
    "v_mul_f32 v226, v226, v231\n"
    "v_cvt_pk_bf16_f32 v237, 0, v237\n"
    "v_mfma_f32_32x32x16_bf16 a[192:207], v[48:51], v[28:31], a[192:207]\n"
    "ds_read_b64_tr_b16 v[36:37], v88 offset:8704\n"
    "v_cvt_pk_bf16_f32 v226, 0, v226\n"
    "v_add_f32 v226, v226, v237\n"
    "v_add_f32 v235, v235, v226\n"
    "v_lshlrev_b32 v236, 16, v232\n"
    "v_mfma_f32_32x32x16_bf16 a[16:31], v[52:55], v[16:19], a[16:31]\n"
    "ds_read_b64_tr_b16 v[38:39], v89 offset:8704\n"
    "v_and_b32 v232, 4294901760, v232\n"
    "v_lshlrev_b32 v237, 16, v222\n"
    "v_and_b32 v222, 4294901760, v222\n"
    "v_mul_f32 v237, v237, v236\n"
    "v_mfma_f32_32x32x16_bf16 a[80:95], v[52:55], v[20:23], a[80:95]\n"
    "buffer_load_dwordx4 v113, s[36:39], s77 offen offset:1024 lds\n"
    "ds_read_b128 v[4:7], v82 offset:4096\n"
    "v_mul_f32 v222, v222, v232\n"
    "v_cvt_pk_bf16_f32 v237, 0, v237\n"
    "v_cvt_pk_bf16_f32 v222, 0, v222\n"
    "v_mfma_f32_32x32x16_bf16 a[144:159], v[52:55], v[24:27], a[144:159]\n"
    "ds_read_b64_tr_b16 v[40:41], v88 offset:9216\n"
    "v_add_f32 v222, v222, v237\n"
    "v_add_f32 v234, v234, v222\n"
    "v_lshlrev_b32 v237, 16, v227\n"
    "v_and_b32 v227, 4294901760, v227\n"
    "v_mfma_f32_32x32x16_bf16 a[208:223], v[52:55], v[28:31], a[208:223]\n"
    "ds_read_b64_tr_b16 v[42:43], v89 offset:9216\n"
    "v_mul_f32 v237, v237, v236\n"
    "v_mul_f32 v227, v227, v232\n"
    "v_cvt_pk_bf16_f32 v237, 0, v237\n"
    "v_cvt_pk_bf16_f32 v227, 0, v227\n"
    "v_mfma_f32_32x32x16_bf16 a[32:47], v[56:59], v[16:19], a[32:47]\n"
    "ds_read_b128 v[8:11], v82 offset:8192\n"
    "v_add_f32 v227, v227, v237\n"
    "v_add_f32 v235, v235, v227\n"
    "s_nop 1\n"
    "v_add_f32_dpp v234, v234, v234 row_shr:1 row_mask:0xf bank_mask:0xf bound_ctrl:0\n"
    "v_mfma_f32_32x32x16_bf16 a[96:111], v[56:59], v[20:23], a[96:111]\n"
    "buffer_load_dwordx4 v114, s[36:39], s77 offen offset:2048 lds\n"
    "ds_read_b64_tr_b16 v[44:45], v88 offset:9728\n"
    "v_add_f32_dpp v235, v235, v235 row_shr:1 row_mask:0xf bank_mask:0xf bound_ctrl:0\n"
    "s_nop 1\n"
    "v_add_f32_dpp v234, v234, v234 row_shr:2 row_mask:0xf bank_mask:0xf bound_ctrl:0\n"
    "v_mfma_f32_32x32x16_bf16 a[160:175], v[56:59], v[24:27], a[160:175]\n"
    "ds_read_b64_tr_b16 v[46:47], v89 offset:9728\n"
    "v_mov_b32 v64, v112\n"
    "v_add_f32_dpp v235, v235, v235 row_shr:2 row_mask:0xf bank_mask:0xf bound_ctrl:0\n"
    "s_nop 1\n"
    "v_add_f32_dpp v234, v234, v234 row_shr:4 row_mask:0xf bank_mask:0xf bound_ctrl:0\n"
    "v_mfma_f32_32x32x16_bf16 a[224:239], v[56:59], v[28:31], a[224:239]\n"
    "ds_read_b128 v[12:15], v82 offset:12288\n"
    "v_add_f32_dpp v235, v235, v235 row_shr:4 row_mask:0xf bank_mask:0xf bound_ctrl:0\n"
    "s_nop 1\n"
    "v_add_f32_dpp v234, v234, v234 row_shr:8 row_mask:0xf bank_mask:0xf bound_ctrl:0\n"
    "v_add_f32_dpp v235, v235, v235 row_shr:8 row_mask:0xf bank_mask:0xf bound_ctrl:0\n"
    "v_mfma_f32_32x32x16_bf16 a[48:63], v[60:63], v[16:19], a[48:63]\n"
    "v_mov_b32 v65, v113\n"
    "s_nop 1\n"
    "v_add_f32_dpp v234, v234, v234 row_bcast:15 row_mask:0xa bank_mask:0xf\n"
    "v_add_f32_dpp v235, v235, v235 row_bcast:15 row_mask:0xa bank_mask:0xf\n"
    "s_nop 1\n"
    "v_mfma_f32_32x32x16_bf16 a[112:127], v[60:63], v[20:23], a[112:127]\n"
    "buffer_load_dwordx4 v115, s[36:39], s77 offen offset:3072 lds\n"
    "v_add_f32_dpp v234, v234, v234 row_bcast:31 row_mask:0xc bank_mask:0xf\n"
    "v_add_f32_dpp v235, v235, v235 row_bcast:31 row_mask:0xc bank_mask:0xf\n"
    "s_nop 1\n"
    "v_readlane_b32 s88, v234, 63\n"
    "v_mfma_f32_32x32x16_bf16 a[176:191], v[60:63], v[24:27], a[176:191]\n"
    "v_mov_b32 v66, v114\n"
    "v_readlane_b32 s89, v235, 63\n"
    "s_nop 3\n"
    "v_mov_b32 v238, s88\n"
    "v_cvt_pk_bf16_f32 v238, 0, v238\n"
    "v_mfma_f32_32x32x16_bf16 a[240:255], v[60:63], v[28:31], a[240:255]\n"
    "v_mul_f32 v238, 0xbfb8aa3b, v238\n"
    "v_exp_f32 v238, v238\n"
    "s_nop 0\n"
    "v_add_f32 v238, 0x3f800000, v238\n"
    "v_rcp_f32 v238, v238\n"
    "s_waitcnt lgkmcnt(0)\n"
    "v_mfma_f32_32x32x16_bf16 a[0:15], v[32:35], v[0:3], a[0:15]\n"
    "ds_read_b64_tr_b16 v[48:49], v88 offset:12288\n"
    "s_add_u32 m0, s79, 4096\n"
    "v_mov_b32 v67, v115\n"
    "s_nop 0\n"
    "v_cvt_pk_bf16_f32 v238, 0, v238\n"
    "v_mfma_f32_32x32x16_bf16 a[64:79], v[32:35], v[4:7], a[64:79]\n"
    "buffer_load_dwordx4 v116, s[36:39], s77 offen lds\n"
    "ds_read_b64_tr_b16 v[50:51], v89 offset:12288\n"
    "v_mov_b32 v239, s89\n"
    "v_cvt_pk_bf16_f32 v239, 0, v239\n"
    "v_mul_f32 v239, 0xbfb8aa3b, v239\n"
    "v_mfma_f32_32x32x16_bf16 a[128:143], v[32:35], v[8:11], a[128:143]\n"
    "ds_read_b128 v[16:19], v83\n"
    "v_mov_b32 v68, v116\n"
    "v_exp_f32 v239, v239\n"
    "s_nop 0\n"
    "v_add_f32 v239, 0x3f800000, v239\n"
    "v_mfma_f32_32x32x16_bf16 a[192:207], v[32:35], v[12:15], a[192:207]\n"
    "ds_read_b64_tr_b16 v[52:53], v88 offset:12800\n"
    "v_rcp_f32 v239, v239\n"
    "s_nop 0\n"
    "v_cvt_pk_bf16_f32 v239, 0, v239\n"
    "v_mfma_f32_32x32x16_bf16 a[16:31], v[36:39], v[0:3], a[16:31]\n"
    "ds_read_b64_tr_b16 v[54:55], v89 offset:12800\n"
    "v_mov_b32 v69, v117\n"
    "v_mfma_f32_32x32x16_bf16 a[80:95], v[36:39], v[4:7], a[80:95]\n"
    "buffer_load_dwordx4 v117, s[36:39], s77 offen offset:1024 lds\n"
    "ds_read_b128 v[20:23], v83 offset:4096\n"
    "v_mfma_f32_32x32x16_bf16 a[144:159], v[36:39], v[8:11], a[144:159]\n"
    "ds_read_b64_tr_b16 v[56:57], v88 offset:13312\n"
    "v_mov_b32 v70, v118\n"
    "v_mfma_f32_32x32x16_bf16 a[208:223], v[36:39], v[12:15], a[208:223]\n"
    "ds_read_b64_tr_b16 v[58:59], v89 offset:13312\n"
    "v_mfma_f32_32x32x16_bf16 a[32:47], v[40:43], v[0:3], a[32:47]\n"
    "ds_read_b128 v[24:27], v83 offset:8192\n"
    "v_mov_b32 v71, v119\n"
    "s_add_u32 s44, s74, 67108864\n"
    "s_addc_u32 s45, s75, 0\n"
    "s_and_b32 s45, s45, 65535\n"
    "s_mov_b32 s46, 5242880\n"
    "s_mov_b32 s47, 131072\n"
    "v_mfma_f32_32x32x16_bf16 a[96:111], v[40:43], v[4:7], a[96:111]\n"
    "buffer_load_dwordx4 v118, s[36:39], s77 offen offset:2048 lds\n"
    "ds_read_b64_tr_b16 v[60:61], v88 offset:13824\n"
    "s_add_u32 s56, s74, 83886080\n"
    "s_addc_u32 s57, s75, 0\n"
    "s_and_b32 s57, s57, 65535\n"
    "s_mov_b32 s58, 8192\n"
    "s_mov_b32 s59, 131072\n"
    "v_mfma_f32_32x32x16_bf16 a[160:175], v[40:43], v[8:11], a[160:175]\n"
    "ds_read_b64_tr_b16 v[62:63], v89 offset:13824\n"
    "v_mov_b32 v72, v120\n"
    "s_lshl_b32 s88, s93, 2\n"
    "v_mov_b32 v240, 0\n"
    "v_mfma_f32_32x32x16_bf16 a[224:239], v[40:43], v[12:15], a[224:239]\n"
    "ds_read_b128 v[28:31], v83 offset:12288\n"
    "buffer_store_dwordx2 v[238:239], v240, s[56:59], s88 offen\n"
    "v_mfma_f32_32x32x16_bf16 a[48:63], v[44:47], v[0:3], a[48:63]\n"
    "v_add_u32 v80, 65536, v124\n"
    "v_mov_b32 v84, v124\n"
    "v_add_u32 v81, 65536, v125\n"
    "v_mov_b32 v85, v125\n"
    "v_mfma_f32_32x32x16_bf16 a[112:127], v[44:47], v[4:7], a[112:127]\n"
    "buffer_load_dwordx4 v119, s[36:39], s77 offen offset:3072 lds\n"
    "v_add_u32 v82, 65536, v126\n"
    "v_mov_b32 v86, v126\n"
    "v_add_u32 v83, 65536, v127\n"
    "v_mov_b32 v87, v127\n"
    "v_mfma_f32_32x32x16_bf16 a[176:191], v[44:47], v[8:11], a[176:191]\n"
    "v_mov_b32 v88, v128\n"
    "v_mov_b32 v89, v129\n"
    "v_mov_b32 v98, v128\n"
    "s_add_u32 s77, s77, 128\n"
    "v_mfma_f32_32x32x16_bf16 a[240:255], v[44:47], v[12:15], a[240:255]\n"
    "v_mov_b32 v99, v129\n"
    "s_waitcnt vmcnt(12) lgkmcnt(0)\n"
    "s_barrier\n"
    "v_mfma_f32_32x32x16_bf16 a[0:15], v[48:51], v[16:19], a[0:15]\n"
    "ds_read_b64_tr_b16 v[32:33], v88\n"
    "s_add_u32 m0, s87, 131072\n"
    "v_mfma_f32_32x32x16_bf16 a[64:79], v[48:51], v[20:23], a[64:79]\n"
    "buffer_load_dwordx4 v120, s[40:43], s78 offen lds\n"
    "ds_read_b64_tr_b16 v[34:35], v89\n"
    "v_lshl_or_b32 v132, v153, 16, v152\n"
    "v_lshl_or_b32 v133, v155, 16, v154\n"
    "v_lshl_or_b32 v134, v157, 16, v156\n"
    "v_lshl_or_b32 v135, v159, 16, v158\n"
    "v_mfma_f32_32x32x16_bf16 a[128:143], v[48:51], v[24:27], a[128:143]\n"
    "ds_read_b128 v[0:3], v80\n"
    "v_mov_b32 v73, v121\n"
    "s_add_u32 s88, s91, 0\n"
    "buffer_store_dwordx4 v[132:135], v251, s[44:47], s88 offen\n"
    "v_mfma_f32_32x32x16_bf16 a[192:207], v[48:51], v[28:31], a[192:207]\n"
    "ds_read_b128 v[4:7], v80 offset:4096\n"
    "v_mfma_f32_32x32x16_bf16 a[16:31], v[52:55], v[16:19], a[16:31]\n"
    "ds_read_b64_tr_b16 v[36:37], v88 offset:512\n"
    "v_mov_b32 v74, v122\n"
    "v_lshl_or_b32 v136, v161, 16, v160\n"
    "v_lshl_or_b32 v137, v163, 16, v162\n"
    "v_lshl_or_b32 v138, v165, 16, v164\n"
    "v_lshl_or_b32 v139, v167, 16, v166\n"
    "v_mfma_f32_32x32x16_bf16 a[80:95], v[52:55], v[20:23], a[80:95]\n"
    "buffer_load_dwordx4 v121, s[40:43], s78 offen offset:1024 lds\n"
    "ds_read_b64_tr_b16 v[38:39], v89 offset:512\n"
    "s_add_u32 s88, s91, 131072\n"
    "buffer_store_dwordx4 v[136:139], v251, s[44:47], s88 offen\n"
    "v_mfma_f32_32x32x16_bf16 a[144:159], v[52:55], v[24:27], a[144:159]\n"
    "ds_read_b128 v[8:11], v80 offset:8192\n"
    "v_mov_b32 v75, v123\n"
    "v_mfma_f32_32x32x16_bf16 a[208:223], v[52:55], v[28:31], a[208:223]\n"
    "ds_read_b128 v[12:15], v80 offset:12288\n"
    "v_lshl_or_b32 v140, v169, 16, v168\n"
    "v_lshl_or_b32 v141, v171, 16, v170\n"
    "v_lshl_or_b32 v142, v173, 16, v172\n"
    "v_lshl_or_b32 v143, v175, 16, v174\n"
    "v_mfma_f32_32x32x16_bf16 a[32:47], v[56:59], v[16:19], a[32:47]\n"
    "s_add_u32 s88, s91, 262144\n"
    "buffer_store_dwordx4 v[140:143], v251, s[44:47], s88 offen\n"
    "v_mfma_f32_32x32x16_bf16 a[96:111], v[56:59], v[20:23], a[96:111]\n"
    "buffer_load_dwordx4 v122, s[40:43], s78 offen offset:2048 lds\n"
    "v_mfma_f32_32x32x16_bf16 a[160:175], v[56:59], v[24:27], a[160:175]\n"
    "v_lshl_or_b32 v144, v177, 16, v176\n"
    "v_lshl_or_b32 v145, v179, 16, v178\n"
    "v_lshl_or_b32 v146, v181, 16, v180\n"
    "v_lshl_or_b32 v147, v183, 16, v182\n"
    "v_mfma_f32_32x32x16_bf16 a[224:239], v[56:59], v[28:31], a[224:239]\n"
    "s_add_u32 s88, s91, 393216\n"
    "buffer_store_dwordx4 v[144:147], v251, s[44:47], s88 offen\n"
    "v_mfma_f32_32x32x16_bf16 a[48:63], v[60:63], v[16:19], a[48:63]\n"
    "v_mfma_f32_32x32x16_bf16 a[112:127], v[60:63], v[20:23], a[112:127]\n"
    "buffer_load_dwordx4 v123, s[40:43], s78 offen offset:3072 lds\n"
    "v_lshl_or_b32 v148, v185, 16, v184\n"
    "v_lshl_or_b32 v149, v187, 16, v186\n"
    "v_lshl_or_b32 v150, v189, 16, v188\n"
    "v_lshl_or_b32 v151, v191, 16, v190\n"
    "v_mfma_f32_32x32x16_bf16 a[176:191], v[60:63], v[24:27], a[176:191]\n"
    "s_add_u32 s78, s78, 524288\n"
    "s_add_u32 s88, s91, 524288\n"
    "buffer_store_dwordx4 v[148:151], v251, s[44:47], s88 offen\n"
    "v_mfma_f32_32x32x16_bf16 a[240:255], v[60:63], v[28:31], a[240:255]\n"
    "s_lshl_b32 s82, s61, 19\n"
    "s_add_u32 s44, s74, 0\n"
    "s_addc_u32 s45, s75, 0\n"
    "s_add_u32 s44, s44, s82\n"
    "s_addc_u32 s45, s45, 0\n"
    "s_and_b32 s45, s45, 65535\n"
    "s_mov_b32 s46, 524288\n"
    "s_mov_b32 s47, 131072\n"
    "s_branch L_qbody_%=\n"
    "L_kvV_%=:\n"
    "L_kvloop1_%=:\n"
    "s_waitcnt lgkmcnt(0)\n"
    "v_mfma_f32_32x32x16_bf16 a[0:15], v[0:3], v[32:35], a[0:15]\n"
    "ds_read_b64_tr_b16 v[48:49], v88 offset:4096\n"
    "s_add_u32 m0, s81, 4096\n"
    "buffer_load_ushort v203, v240, s[44:47], s88 offen\n"
    "v_mfma_f32_32x32x16_bf16 a[64:79], v[4:7], v[32:35], a[64:79]\n"
    "buffer_load_dwordx4 v76, s[40:43], s78 offen lds\n"
    "ds_read_b64_tr_b16 v[50:51], v89 offset:4096\n"
    "v_mfma_f32_32x32x16_bf16 a[128:143], v[8:11], v[32:35], a[128:143]\n"
    "ds_read_b128 v[16:19], v81\n"
    "s_cmp_eq_u32 s76, 0\n"
    "s_cselect_b32 s91, s93, 65535\n"
    "buffer_load_ushort v204, v240, s[44:47], s88 offen offset:1280\n"
    "v_mfma_f32_32x32x16_bf16 a[192:207], v[12:15], v[32:35], a[192:207]\n"
    "ds_read_b64_tr_b16 v[52:53], v88 offset:4608\n"
    "s_sub_u32 s96, s76, 1\n"
    "s_cmp_lt_u32 s96, 4\n"
    "s_cselect_b32 s58, s90, 0\n"
    "buffer_load_ushort v205, v240, s[44:47], s88 offen offset:2560\n"
    "v_mfma_f32_32x32x16_bf16 a[16:31], v[0:3], v[36:39], a[16:31]\n"
    "ds_read_b64_tr_b16 v[54:55], v89 offset:4608\n"
    "v_mul_f32 v237, v218, v217\n"
    "v_fma_f32 v237, v219, v216, v237\n"
    "v_fma_f32 v237, v220, v215, v237\n"
    "buffer_load_ushort v206, v240, s[44:47], s88 offen offset:3840\n"
    "v_mfma_f32_32x32x16_bf16 a[80:95], v[4:7], v[36:39], a[80:95]\n"
    "buffer_load_dwordx4 v77, s[40:43], s78 offen offset:1024 lds\n"
    "ds_read_b128 v[20:23], v81 offset:4096\n"
    "v_fma_f32 v237, v221, v214, v237\n"
    "v_cvt_pk_bf16_f32 v237, 0, v237\n"
    "v_mfma_f32_32x32x16_bf16 a[144:159], v[8:11], v[36:39], a[144:159]\n"
    "ds_read_b64_tr_b16 v[56:57], v88 offset:5120\n"
    "v_mul_f32 v229, v237, v229\n"
    "v_mul_f32 v238, v219, v217\n"
    "v_fma_f32 v238, v220, v216, v238\n"
    "buffer_load_ushort v207, v240, s[44:47], s92 offen\n"
    "v_mfma_f32_32x32x16_bf16 a[208:223], v[12:15], v[36:39], a[208:223]\n"
    "ds_read_b64_tr_b16 v[58:59], v89 offset:5120\n"
    "v_fma_f32 v238, v221, v215, v238\n"
    "v_fma_f32 v238, v222, v214, v238\n"
    "v_cvt_pk_bf16_f32 v238, 0, v238\n"
    "buffer_load_ushort v208, v240, s[44:47], s92 offen offset:1280\n"
    "v_mfma_f32_32x32x16_bf16 a[32:47], v[0:3], v[40:43], a[32:47]\n"
    "ds_read_b128 v[24:27], v81 offset:8192\n"
    "v_mul_f32 v230, v238, v230\n"
    "v_mul_f32 v237, v220, v217\n"
    "v_fma_f32 v237, v221, v216, v237\n"
    "buffer_load_ushort v209, v240, s[44:47], s92 offen offset:2560\n"
    "v_mfma_f32_32x32x16_bf16 a[96:111], v[4:7], v[40:43], a[96:111]\n"
    "buffer_load_dwordx4 v78, s[40:43], s78 offen offset:2048 lds\n"
    "ds_read_b64_tr_b16 v[60:61], v88 offset:5632\n"
    "v_fma_f32 v237, v222, v215, v237\n"
    "v_fma_f32 v237, v223, v214, v237\n"
    "v_mfma_f32_32x32x16_bf16 a[160:175], v[8:11], v[40:43], a[160:175]\n"
    "ds_read_b64_tr_b16 v[62:63], v89 offset:5632\n"
    "v_cvt_pk_bf16_f32 v237, 0, v237\n"
    "v_mul_f32 v231, v237, v231\n"
    "v_mul_f32 v238, v221, v217\n"
    "buffer_load_ushort v210, v240, s[44:47], s92 offen offset:3840\n"
    "v_mfma_f32_32x32x16_bf16 a[224:239], v[12:15], v[40:43], a[224:239]\n"
    "ds_read_b128 v[28:31], v81 offset:12288\n"
    "v_fma_f32 v238, v222, v216, v238\n"
    "v_fma_f32 v238, v223, v215, v238\n"
    "v_fma_f32 v238, v224, v214, v238\n"
    "v_mfma_f32_32x32x16_bf16 a[48:63], v[0:3], v[44:47], a[48:63]\n"
    "v_cvt_pk_bf16_f32 v238, 0, v238\n"
    "v_mul_f32 v232, v238, v232\n"
    "v_mul_f32 v237, v222, v217\n"
    "v_fma_f32 v237, v223, v216, v237\n"
    "v_mfma_f32_32x32x16_bf16 a[112:127], v[4:7], v[44:47], a[112:127]\n"
    "buffer_load_dwordx4 v79, s[40:43], s78 offen offset:3072 lds\n"
    "v_fma_f32 v237, v224, v215, v237\n"
    "v_fma_f32 v237, v225, v214, v237\n"
    "v_cvt_pk_bf16_f32 v237, 0, v237\n"
    "v_mfma_f32_32x32x16_bf16 a[176:191], v[8:11], v[44:47], a[176:191]\n"
    "v_mul_f32 v233, v237, v233\n"
    "v_mul_f32 v238, v223, v217\n"
    "v_fma_f32 v238, v224, v216, v238\n"
    "v_fma_f32 v238, v225, v215, v238\n"
    "v_mfma_f32_32x32x16_bf16 a[240:255], v[12:15], v[44:47], a[240:255]\n"
    "v_fma_f32 v238, v226, v214, v238\n"
    "v_cvt_pk_bf16_f32 v238, 0, v238\n"
    "v_mul_f32 v234, v238, v234\n"
    "v_mul_f32 v237, v224, v217\n"
    "s_waitcnt lgkmcnt(0)\n"
    "v_mfma_f32_32x32x16_bf16 a[0:15], v[16:19], v[48:51], a[0:15]\n"
    "ds_read_b64_tr_b16 v[32:33], v88 offset:8192\n"
    "s_add_u32 m0, s79, 0\n"
    "v_fma_f32 v237, v225, v216, v237\n"
    "v_fma_f32 v237, v226, v215, v237\n"
    "s_add_u32 s88, s88, 10240\n"
    "s_add_u32 s92, s88, 5120\n"
    "v_mfma_f32_32x32x16_bf16 a[64:79], v[20:23], v[48:51], a[64:79]\n"
    "buffer_load_dwordx4 v64, s[36:39], s77 offen lds\n"
    "ds_read_b64_tr_b16 v[34:35], v89 offset:8192\n"
    "v_fma_f32 v237, v227, v214, v237\n"
    "v_cvt_pk_bf16_f32 v237, 0, v237\n"
    "v_mfma_f32_32x32x16_bf16 a[128:143], v[24:27], v[48:51], a[128:143]\n"
    "ds_read_b128 v[0:3], v82\n"
    "v_mul_f32 v235, v237, v235\n"
    "v_mul_f32 v238, v225, v217\n"
    "v_fma_f32 v238, v226, v216, v238\n"
    "v_mfma_f32_32x32x16_bf16 a[192:207], v[28:31], v[48:51], a[192:207]\n"
    "ds_read_b64_tr_b16 v[36:37], v88 offset:8704\n"
    "v_fma_f32 v238, v227, v215, v238\n"
    "v_fma_f32 v238, v228, v214, v238\n"
    "v_cvt_pk_bf16_f32 v238, 0, v238\n"
    "v_mfma_f32_32x32x16_bf16 a[16:31], v[16:19], v[52:55], a[16:31]\n"
    "ds_read_b64_tr_b16 v[38:39], v89 offset:8704\n"
    "v_mul_f32 v236, v238, v236\n"
    "v_cvt_pk_bf16_f32 v244, v236, v235\n"
    "v_cvt_pk_bf16_f32 v245, v234, v233\n"
    "v_mfma_f32_32x32x16_bf16 a[80:95], v[20:23], v[52:55], a[80:95]\n"
    "buffer_load_dwordx4 v65, s[36:39], s77 offen offset:1024 lds\n"
    "ds_read_b128 v[4:7], v82 offset:4096\n"
    "v_cvt_pk_bf16_f32 v246, v232, v231\n"
    "v_cvt_pk_bf16_f32 v247, v230, v229\n"
    "v_mfma_f32_32x32x16_bf16 a[144:159], v[24:27], v[52:55], a[144:159]\n"
    "ds_read_b64_tr_b16 v[40:41], v88 offset:9216\n"
    "v_mfma_f32_32x32x16_bf16 a[208:223], v[28:31], v[52:55], a[208:223]\n"
    "ds_read_b64_tr_b16 v[42:43], v89 offset:9216\n"
    "v_mfma_f32_32x32x16_bf16 a[32:47], v[16:19], v[56:59], a[32:47]\n"
    "ds_read_b128 v[8:11], v82 offset:8192\n"
    "v_mfma_f32_32x32x16_bf16 a[96:111], v[20:23], v[56:59], a[96:111]\n"
    "buffer_load_dwordx4 v66, s[36:39], s77 offen offset:2048 lds\n"
    "ds_read_b64_tr_b16 v[44:45], v88 offset:9728\n"
    "v_mfma_f32_32x32x16_bf16 a[160:175], v[24:27], v[56:59], a[160:175]\n"
    "ds_read_b64_tr_b16 v[46:47], v89 offset:9728\n"
    "v_mfma_f32_32x32x16_bf16 a[224:239], v[28:31], v[56:59], a[224:239]\n"
    "ds_read_b128 v[12:15], v82 offset:12288\n"
    "v_mfma_f32_32x32x16_bf16 a[48:63], v[16:19], v[60:63], a[48:63]\n"
    "v_mfma_f32_32x32x16_bf16 a[112:127], v[20:23], v[60:63], a[112:127]\n"
    "buffer_load_dwordx4 v67, s[36:39], s77 offen offset:3072 lds\n"
    "v_mfma_f32_32x32x16_bf16 a[176:191], v[24:27], v[60:63], a[176:191]\n"
    "v_mfma_f32_32x32x16_bf16 a[240:255], v[28:31], v[60:63], a[240:255]\n"
    "s_waitcnt lgkmcnt(0)\n"
    "v_mfma_f32_32x32x16_bf16 a[0:15], v[0:3], v[32:35], a[0:15]\n"
    "ds_read_b64_tr_b16 v[48:49], v88 offset:12288\n"
    "s_add_u32 m0, s79, 4096\n"
    "v_mfma_f32_32x32x16_bf16 a[64:79], v[4:7], v[32:35], a[64:79]\n"
    "buffer_load_dwordx4 v68, s[36:39], s77 offen lds\n"
    "ds_read_b64_tr_b16 v[50:51], v89 offset:12288\n"
    "v_mfma_f32_32x32x16_bf16 a[128:143], v[8:11], v[32:35], a[128:143]\n"
    "ds_read_b128 v[16:19], v83\n"
    "v_mfma_f32_32x32x16_bf16 a[192:207], v[12:15], v[32:35], a[192:207]\n"
    "ds_read_b64_tr_b16 v[52:53], v88 offset:12800\n"
    "v_mfma_f32_32x32x16_bf16 a[16:31], v[0:3], v[36:39], a[16:31]\n"
    "ds_read_b64_tr_b16 v[54:55], v89 offset:12800\n"
    "v_mfma_f32_32x32x16_bf16 a[80:95], v[4:7], v[36:39], a[80:95]\n"
    "buffer_load_dwordx4 v69, s[36:39], s77 offen offset:1024 lds\n"
    "ds_read_b128 v[20:23], v83 offset:4096\n"
    "v_mfma_f32_32x32x16_bf16 a[144:159], v[8:11], v[36:39], a[144:159]\n"
    "ds_read_b64_tr_b16 v[56:57], v88 offset:13312\n"
    "v_mfma_f32_32x32x16_bf16 a[208:223], v[12:15], v[36:39], a[208:223]\n"
    "ds_read_b64_tr_b16 v[58:59], v89 offset:13312\n"
    "v_mfma_f32_32x32x16_bf16 a[32:47], v[0:3], v[40:43], a[32:47]\n"
    "ds_read_b128 v[24:27], v83 offset:8192\n"
    "s_xor_b32 s81, s81, 229376\n"
    "s_add_u32 s78, s78, 524288\n"
    "v_mfma_f32_32x32x16_bf16 a[96:111], v[4:7], v[40:43], a[96:111]\n"
    "buffer_load_dwordx4 v70, s[36:39], s77 offen offset:2048 lds\n"
    "ds_read_b64_tr_b16 v[60:61], v88 offset:13824\n"
    "v_mfma_f32_32x32x16_bf16 a[160:175], v[8:11], v[40:43], a[160:175]\n"
    "ds_read_b64_tr_b16 v[62:63], v89 offset:13824\n"
    "buffer_store_dwordx4 v[244:247], v251, s[56:59], s89 offen\n"
    "s_sub_u32 s89, s89, 10240\n"
    "v_mfma_f32_32x32x16_bf16 a[224:239], v[12:15], v[40:43], a[224:239]\n"
    "ds_read_b128 v[28:31], v83 offset:12288\n"
    "v_mfma_f32_32x32x16_bf16 a[48:63], v[0:3], v[44:47], a[48:63]\n"
    "s_add_u32 s82, s80, s94\n"
    "s_add_u32 s80, s80, 32768\n"
    "s_cmp_eq_u32 s80, 98304\n"
    "v_mfma_f32_32x32x16_bf16 a[112:127], v[4:7], v[44:47], a[112:127]\n"
    "buffer_load_dwordx4 v71, s[36:39], s77 offen offset:3072 lds\n"
    "s_cselect_b32 s80, 0, s80\n"
    "v_add_u32 v80, s80, v84\n"
    "v_add_u32 v81, s80, v85\n"
    "v_mfma_f32_32x32x16_bf16 a[176:191], v[8:11], v[44:47], a[176:191]\n"
    "v_add_u32 v82, s80, v86\n"
    "v_add_u32 v83, s80, v87\n"
    "v_xor_b32 v88, 229376, v88\n"
    "s_add_u32 s77, s77, 128\n"
    "s_mov_b32 s79, s82\n"
    "v_mfma_f32_32x32x16_bf16 a[240:255], v[12:15], v[44:47], a[240:255]\n"
    "v_xor_b32 v89, 229376, v89\n"
    "s_waitcnt vmcnt(8) lgkmcnt(0)\n"
    "s_barrier\n"
    "v_mfma_f32_32x32x16_bf16 a[0:15], v[16:19], v[48:51], a[0:15]\n"
    "ds_read_b64_tr_b16 v[32:33], v88\n"
    "s_add_u32 m0, s81, 0\n"
    "v_mfma_f32_32x32x16_bf16 a[64:79], v[20:23], v[48:51], a[64:79]\n"
    "buffer_load_dwordx4 v72, s[40:43], s78 offen lds\n"
    "ds_read_b64_tr_b16 v[34:35], v89\n"
    "v_and_b32 v192, s91, v192\n"
    "v_and_b32 v193, s91, v193\n"
    "v_and_b32 v194, s91, v194\n"
    "v_mfma_f32_32x32x16_bf16 a[128:143], v[24:27], v[48:51], a[128:143]\n"
    "ds_read_b128 v[0:3], v80\n"
    "v_lshlrev_b32 v218, 16, v192\n"
    "v_lshlrev_b32 v219, 16, v193\n"
    "v_lshlrev_b32 v220, 16, v194\n"
    "v_lshlrev_b32 v221, 16, v195\n"
    "v_mfma_f32_32x32x16_bf16 a[192:207], v[28:31], v[48:51], a[192:207]\n"
    "ds_read_b64_tr_b16 v[36:37], v88 offset:512\n"
    "buffer_load_dword v109, v131, s[52:55], s97 offen\n"
    "v_lshlrev_b32 v222, 16, v196\n"
    "v_lshlrev_b32 v223, 16, v197\n"
    "v_lshlrev_b32 v224, 16, v198\n"
    "v_mfma_f32_32x32x16_bf16 a[16:31], v[16:19], v[52:55], a[16:31]\n"
    "ds_read_b64_tr_b16 v[38:39], v89 offset:512\n"
    "s_add_u32 s97, s97, 4096\n"
    "v_lshlrev_b32 v225, 16, v199\n"
    "v_lshlrev_b32 v226, 16, v200\n"
    "v_lshlrev_b32 v227, 16, v201\n"
    "v_mfma_f32_32x32x16_bf16 a[80:95], v[20:23], v[52:55], a[80:95]\n"
    "buffer_load_dwordx4 v73, s[40:43], s78 offen offset:1024 lds\n"
    "ds_read_b128 v[4:7], v80 offset:4096\n"
    "v_lshlrev_b32 v228, 16, v202\n"
    "v_lshlrev_b32 v229, 16, v203\n"
    "v_lshlrev_b32 v230, 16, v204\n"
    "v_mfma_f32_32x32x16_bf16 a[144:159], v[24:27], v[52:55], a[144:159]\n"
    "ds_read_b64_tr_b16 v[40:41], v88 offset:1024\n"
    "v_lshlrev_b32 v231, 16, v205\n"
    "v_lshlrev_b32 v232, 16, v206\n"
    "v_lshlrev_b32 v233, 16, v207\n"
    "v_lshlrev_b32 v234, 16, v208\n"
    "buffer_load_ushort v195, v240, s[48:51], s88 offen\n"
    "v_mfma_f32_32x32x16_bf16 a[208:223], v[28:31], v[52:55], a[208:223]\n"
    "ds_read_b64_tr_b16 v[42:43], v89 offset:1024\n"
    "v_lshlrev_b32 v235, 16, v209\n"
    "v_lshlrev_b32 v236, 16, v210\n"
    "v_mov_b32 v192, v200\n"
    "v_mov_b32 v193, v201\n"
    "buffer_load_ushort v196, v240, s[48:51], s88 offen offset:1280\n"
    "v_mfma_f32_32x32x16_bf16 a[32:47], v[16:19], v[56:59], a[32:47]\n"
    "ds_read_b128 v[8:11], v80 offset:8192\n"
    "v_mov_b32 v194, v202\n"
    "v_lshlrev_b32 v214, 16, v212\n"
    "v_and_b32 v215, 4294901760, v212\n"
    "v_lshlrev_b32 v216, 16, v213\n"
    "buffer_load_ushort v197, v240, s[48:51], s88 offen offset:2560\n"
    "v_mfma_f32_32x32x16_bf16 a[96:111], v[20:23], v[56:59], a[96:111]\n"
    "buffer_load_dwordx4 v74, s[40:43], s78 offen offset:2048 lds\n"
    "ds_read_b64_tr_b16 v[44:45], v88 offset:1536\n"
    "v_and_b32 v217, 4294901760, v213\n"
    "v_mfma_f32_32x32x16_bf16 a[160:175], v[24:27], v[56:59], a[160:175]\n"
    "ds_read_b64_tr_b16 v[46:47], v89 offset:1536\n"
    "buffer_load_ushort v198, v240, s[48:51], s88 offen offset:3840\n"
    "v_mfma_f32_32x32x16_bf16 a[224:239], v[28:31], v[56:59], a[224:239]\n"
    "ds_read_b128 v[12:15], v80 offset:12288\n"
    "buffer_load_ushort v199, v240, s[48:51], s92 offen\n"
    "v_mfma_f32_32x32x16_bf16 a[48:63], v[16:19], v[60:63], a[48:63]\n"
    "buffer_load_ushort v200, v240, s[48:51], s92 offen offset:1280\n"
    "v_mfma_f32_32x32x16_bf16 a[112:127], v[20:23], v[60:63], a[112:127]\n"
    "buffer_load_dwordx4 v75, s[40:43], s78 offen offset:3072 lds\n"
    "v_mfma_f32_32x32x16_bf16 a[176:191], v[24:27], v[60:63], a[176:191]\n"
    "buffer_load_ushort v201, v240, s[48:51], s92 offen offset:2560\n"
    "v_mfma_f32_32x32x16_bf16 a[240:255], v[28:31], v[60:63], a[240:255]\n"
    "buffer_load_ushort v202, v240, s[48:51], s92 offen offset:3840\n"
    "s_add_u32 s76, s76, 1\n"
    "s_cmp_lt_u32 s76, 6\n"
    "s_cbranch_scc1 L_kvloop1_%=\n"
    "s_waitcnt lgkmcnt(0)\n"
    "v_mfma_f32_32x32x16_bf16 a[0:15], v[0:3], v[32:35], a[0:15]\n"
    "ds_read_b64_tr_b16 v[48:49], v88 offset:4096\n"
    "s_add_u32 m0, s81, 4096\n"
    "v_mfma_f32_32x32x16_bf16 a[64:79], v[4:7], v[32:35], a[64:79]\n"
    "buffer_load_dwordx4 v76, s[40:43], s78 offen lds\n"
    "ds_read_b64_tr_b16 v[50:51], v89 offset:4096\n"
    "v_mfma_f32_32x32x16_bf16 a[128:143], v[8:11], v[32:35], a[128:143]\n"
    "ds_read_b128 v[16:19], v81\n"
    "s_add_u32 s36, s64, 0\n"
    "v_mfma_f32_32x32x16_bf16 a[192:207], v[12:15], v[32:35], a[192:207]\n"
    "ds_read_b64_tr_b16 v[52:53], v88 offset:4608\n"
    "s_addc_u32 s37, s65, 0\n"
    "v_mfma_f32_32x32x16_bf16 a[16:31], v[0:3], v[36:39], a[16:31]\n"
    "ds_read_b64_tr_b16 v[54:55], v89 offset:4608\n"
    "s_and_b32 s37, s37, 65535\n"
    "s_mov_b32 s56, %24\n"
    "s_and_b32 s57, %25, 0xffff\n"
    "s_mov_b32 s58, 1280\n"
    "s_mov_b32 s59, 131072\n"
    "v_mfma_f32_32x32x16_bf16 a[80:95], v[4:7], v[36:39], a[80:95]\n"
    "buffer_load_dwordx4 v77, s[40:43], s78 offen offset:1024 lds\n"
    "ds_read_b128 v[20:23], v81 offset:4096\n"
    "s_mov_b32 s38, 2621440\n"
    "s_lshl_b32 s93, %15, 3\n"
    "s_lshl_b32 s96, s60, 1\n"
    "s_add_u32 s93, s93, s96\n"
    "s_mul_i32 s96, s93, 1280\n"
    "v_mfma_f32_32x32x16_bf16 a[144:159], v[8:11], v[36:39], a[144:159]\n"
    "ds_read_b64_tr_b16 v[56:57], v88 offset:5120\n"
    "s_mov_b32 s39, 131072\n"
    "v_lshlrev_b32 v233, 2, v250\n"
    "v_mfma_f32_32x32x16_bf16 a[208:223], v[12:15], v[36:39], a[208:223]\n"
    "ds_read_b64_tr_b16 v[58:59], v89 offset:5120\n"
    "s_sub_u32 s82, 15, s62\n"
    "v_mfma_f32_32x32x16_bf16 a[32:47], v[0:3], v[40:43], a[32:47]\n"
    "ds_read_b128 v[24:27], v81 offset:8192\n"
    "s_cmp_lt_u32 s60, 2\n"
    "v_mfma_f32_32x32x16_bf16 a[96:111], v[4:7], v[40:43], a[96:111]\n"
    "buffer_load_dwordx4 v78, s[40:43], s78 offen offset:2048 lds\n"
    "ds_read_b64_tr_b16 v[60:61], v88 offset:5632\n"
    "s_cselect_b32 s82, s62, s82\n"
    "v_mfma_f32_32x32x16_bf16 a[160:175], v[8:11], v[40:43], a[160:175]\n"
    "ds_read_b64_tr_b16 v[62:63], v89 offset:5632\n"
    "s_lshl_b32 s82, s82, 7\n"
    "buffer_load_dword v218, v233, s[36:39], s96 offen\n"
    "buffer_load_dword v219, v233, s[36:39], s96 offen offset:256\n"
    "buffer_load_dword v220, v233, s[36:39], s96 offen offset:512\n"
    "v_mfma_f32_32x32x16_bf16 a[224:239], v[12:15], v[40:43], a[224:239]\n"
    "ds_read_b128 v[28:31], v81 offset:12288\n"
    "s_and_b32 s83, s60, 1\n"
    "buffer_load_dword v221, v233, s[36:39], s96 offen offset:768\n"
    "buffer_load_dword v222, v233, s[36:39], s96 offen offset:1024\n"
    "buffer_load_dword v223, v233, s[36:39], s96 offen offset:1280\n"
    "v_mfma_f32_32x32x16_bf16 a[48:63], v[0:3], v[44:47], a[48:63]\n"
    "s_lshl_b32 s83, s83, 6\n"
    "buffer_load_dword v224, v233, s[36:39], s96 offen offset:1536\n"
    "buffer_load_dword v225, v233, s[36:39], s96 offen offset:1792\n"
    "buffer_load_dword v226, v233, s[36:39], s96 offen offset:2048\n"
    "v_mfma_f32_32x32x16_bf16 a[112:127], v[4:7], v[44:47], a[112:127]\n"
    "buffer_load_dwordx4 v79, s[40:43], s78 offen offset:3072 lds\n"
    "s_add_u32 s82, s82, s83\n"
    "v_mfma_f32_32x32x16_bf16 a[176:191], v[8:11], v[44:47], a[176:191]\n"
    "s_mul_i32 s77, s82, 1280\n"
    "buffer_load_dword v227, v233, s[36:39], s96 offen offset:2304\n"
    "buffer_load_dword v228, v233, s[56:59], 0 offen\n"
    "buffer_load_dword v229, v233, s[56:59], 0 offen offset:256\n"
    "v_mfma_f32_32x32x16_bf16 a[240:255], v[12:15], v[44:47], a[240:255]\n"
    "s_lshl_b32 s89, %15, 2\n"
    "s_add_u32 s89, s89, s60\n"
    "s_lshr_b32 s90, s89, 3\n"
    "s_and_b32 s89, s89, 7\n"
    "buffer_load_dword v230, v233, s[56:59], 0 offen offset:512\n"
    "buffer_load_dword v231, v233, s[56:59], 0 offen offset:768\n"
    "buffer_load_dword v232, v233, s[56:59], 0 offen offset:1024\n"
    "s_waitcnt vmcnt(19) lgkmcnt(0)\n"
    "v_mfma_f32_32x32x16_bf16 a[0:15], v[16:19], v[48:51], a[0:15]\n"
    "ds_read_b64_tr_b16 v[32:33], v88 offset:8192\n"
    "s_add_u32 m0, s79, 0\n"
    "v_mfma_f32_32x32x16_bf16 a[64:79], v[20:23], v[48:51], a[64:79]\n"
    "buffer_load_dwordx4 v112, s[36:39], s77 offen lds\n"
    "ds_read_b64_tr_b16 v[34:35], v89 offset:8192\n"
    "v_mfma_f32_32x32x16_bf16 a[128:143], v[24:27], v[48:51], a[128:143]\n"
    "ds_read_b128 v[0:3], v82\n"
    "s_mul_i32 s92, s89, 5\n"
    "s_lshl_b32 s91, s92, 17\n"
    "s_lshl_b32 s88, s90, 10\n"
    "s_add_u32 s91, s91, s88\n"
    "v_mfma_f32_32x32x16_bf16 a[192:207], v[28:31], v[48:51], a[192:207]\n"
    "ds_read_b64_tr_b16 v[36:37], v88 offset:8704\n"
    "s_mul_i32 s90, s90, 40960\n"
    "s_lshl_b32 s92, s92, 5\n"
    "s_add_u32 s89, s90, s92\n"
    "s_add_u32 s90, s89, 10240\n"
    "v_mfma_f32_32x32x16_bf16 a[16:31], v[16:19], v[52:55], a[16:31]\n"
    "ds_read_b64_tr_b16 v[38:39], v89 offset:8704\n"
    "s_mov_b32 s48, %16\n"
    "s_and_b32 s49, %17, 0xffff\n"
    "s_mov_b32 s50, 5242880\n"
    "s_mov_b32 s51, 131072\n"
    "v_mfma_f32_32x32x16_bf16 a[80:95], v[20:23], v[52:55], a[80:95]\n"
    "buffer_load_dwordx4 v113, s[36:39], s77 offen offset:1024 lds\n"
    "ds_read_b128 v[4:7], v82 offset:4096\n"
    "s_mov_b32 s52, s68\n"
    "s_and_b32 s53, s69, 65535\n"
    "s_mov_b32 s54, 5242880\n"
    "v_mfma_f32_32x32x16_bf16 a[144:159], v[24:27], v[52:55], a[144:159]\n"
    "ds_read_b64_tr_b16 v[40:41], v88 offset:9216\n"
    "s_mul_i32 s97, s62, 80\n"
    "s_lshl_b32 s86, s60, 5\n"
    "s_add_u32 s97, s97, s86\n"
    "s_lshl_b32 s97, s97, 13\n"
    "buffer_load_ushort v152, v130, s[48:51], s89 offen offset:0\n"
    "buffer_load_ushort v153, v130, s[48:51], s89 offen offset:1280\n"
    "v_mfma_f32_32x32x16_bf16 a[208:223], v[28:31], v[52:55], a[208:223]\n"
    "ds_read_b64_tr_b16 v[42:43], v89 offset:9216\n"
    "s_lshl_b32 s86, s61, 8\n"
    "s_add_u32 s97, s97, s86\n"
    "buffer_load_ushort v154, v130, s[48:51], s89 offen offset:2560\n"
    "buffer_load_ushort v155, v130, s[48:51], s89 offen offset:3840\n"
    "v_mfma_f32_32x32x16_bf16 a[32:47], v[16:19], v[56:59], a[32:47]\n"
    "ds_read_b128 v[8:11], v82 offset:8192\n"
    "buffer_load_dword v109, v108, s[52:55], s97 offen\n"
    "s_add_u32 s97, s97, 131072\n"
    "v_mfma_f32_32x32x16_bf16 a[96:111], v[20:23], v[56:59], a[96:111]\n"
    "buffer_load_dwordx4 v114, s[36:39], s77 offen offset:2048 lds\n"
    "ds_read_b64_tr_b16 v[44:45], v88 offset:9728\n"
    "v_mfma_f32_32x32x16_bf16 a[160:175], v[24:27], v[56:59], a[160:175]\n"
    "ds_read_b64_tr_b16 v[46:47], v89 offset:9728\n"
    "buffer_load_ushort v160, v130, s[48:51], s89 offen offset:32\n"
    "buffer_load_ushort v161, v130, s[48:51], s89 offen offset:1312\n"
    "v_mfma_f32_32x32x16_bf16 a[224:239], v[28:31], v[56:59], a[224:239]\n"
    "ds_read_b128 v[12:15], v82 offset:12288\n"
    "buffer_load_dword v109, v108, s[52:55], s97 offen\n"
    "buffer_load_ushort v162, v130, s[48:51], s89 offen offset:2592\n"
    "buffer_load_ushort v163, v130, s[48:51], s89 offen offset:3872\n"
    "v_mfma_f32_32x32x16_bf16 a[48:63], v[16:19], v[60:63], a[48:63]\n"
    "v_mfma_f32_32x32x16_bf16 a[112:127], v[20:23], v[60:63], a[112:127]\n"
    "buffer_load_dwordx4 v115, s[36:39], s77 offen offset:3072 lds\n"
    "v_mfma_f32_32x32x16_bf16 a[176:191], v[24:27], v[60:63], a[176:191]\n"
    "s_sub_u32 s40, s68, 1024\n"
    "buffer_load_ushort v168, v130, s[48:51], s89 offen offset:64\n"
    "buffer_load_ushort v169, v130, s[48:51], s89 offen offset:1344\n"
    "v_mfma_f32_32x32x16_bf16 a[240:255], v[28:31], v[60:63], a[240:255]\n"
    "s_subb_u32 s83, s69, 0\n"
    "buffer_load_ushort v170, v130, s[48:51], s89 offen offset:2624\n"
    "buffer_load_ushort v171, v130, s[48:51], s89 offen offset:3904\n"
    "s_waitcnt lgkmcnt(0)\n"
    "v_mfma_f32_32x32x16_bf16 a[0:15], v[0:3], v[32:35], a[0:15]\n"
    "ds_read_b64_tr_b16 v[48:49], v88 offset:12288\n"
    "s_add_u32 m0, s79, 4096\n"
    "s_and_b32 s41, s83, 65535\n"
    "v_mfma_f32_32x32x16_bf16 a[64:79], v[4:7], v[32:35], a[64:79]\n"
    "buffer_load_dwordx4 v116, s[36:39], s77 offen lds\n"
    "ds_read_b64_tr_b16 v[50:51], v89 offset:12288\n"
    "s_mov_b32 s42, 5243904\n"
    "v_mfma_f32_32x32x16_bf16 a[128:143], v[8:11], v[32:35], a[128:143]\n"
    "ds_read_b128 v[16:19], v83\n"
    "s_lshl_b32 s83, s60, 17\n"
    "buffer_load_ushort v176, v130, s[48:51], s89 offen offset:96\n"
    "buffer_load_ushort v177, v130, s[48:51], s89 offen offset:1376\n"
    "v_mfma_f32_32x32x16_bf16 a[192:207], v[12:15], v[32:35], a[192:207]\n"
    "ds_read_b64_tr_b16 v[52:53], v88 offset:12800\n"
    "s_lshl_b32 s84, s61, 8\n"
    "buffer_load_ushort v178, v130, s[48:51], s89 offen offset:2656\n"
    "buffer_load_ushort v179, v130, s[48:51], s89 offen offset:3936\n"
    "v_mfma_f32_32x32x16_bf16 a[16:31], v[0:3], v[36:39], a[16:31]\n"
    "ds_read_b64_tr_b16 v[54:55], v89 offset:12800\n"
    "s_add_u32 s78, s83, s84\n"
    "v_mfma_f32_32x32x16_bf16 a[80:95], v[4:7], v[36:39], a[80:95]\n"
    "buffer_load_dwordx4 v117, s[36:39], s77 offen offset:1024 lds\n"
    "ds_read_b128 v[20:23], v83 offset:4096\n"
    "s_lshl_b32 s87, s60, 12\n"
    "v_mfma_f32_32x32x16_bf16 a[144:159], v[8:11], v[36:39], a[144:159]\n"
    "ds_read_b64_tr_b16 v[56:57], v88 offset:13312\n"
    "buffer_load_ushort v184, v130, s[48:51], s89 offen offset:128\n"
    "buffer_load_ushort v185, v130, s[48:51], s89 offen offset:1408\n"
    "v_mfma_f32_32x32x16_bf16 a[208:223], v[12:15], v[36:39], a[208:223]\n"
    "ds_read_b64_tr_b16 v[58:59], v89 offset:13312\n"
    "buffer_load_ushort v186, v130, s[48:51], s89 offen offset:2688\n"
    "buffer_load_ushort v187, v130, s[48:51], s89 offen offset:3968\n"
    "v_mfma_f32_32x32x16_bf16 a[32:47], v[0:3], v[40:43], a[32:47]\n"
    "ds_read_b128 v[24:27], v83 offset:8192\n"
    "v_mfma_f32_32x32x16_bf16 a[96:111], v[4:7], v[40:43], a[96:111]\n"
    "buffer_load_dwordx4 v118, s[36:39], s77 offen offset:2048 lds\n"
    "ds_read_b64_tr_b16 v[60:61], v88 offset:13824\n"
    "v_mfma_f32_32x32x16_bf16 a[160:175], v[8:11], v[40:43], a[160:175]\n"
    "ds_read_b64_tr_b16 v[62:63], v89 offset:13824\n"
    "buffer_load_ushort v156, v130, s[48:51], s90 offen offset:0\n"
    "buffer_load_ushort v157, v130, s[48:51], s90 offen offset:1280\n"
    "v_mfma_f32_32x32x16_bf16 a[224:239], v[12:15], v[40:43], a[224:239]\n"
    "ds_read_b128 v[28:31], v83 offset:12288\n"
    "buffer_load_ushort v158, v130, s[48:51], s90 offen offset:2560\n"
    "buffer_load_ushort v159, v130, s[48:51], s90 offen offset:3840\n"
    "v_mfma_f32_32x32x16_bf16 a[48:63], v[0:3], v[44:47], a[48:63]\n"
    "s_add_u32 s82, s80, s94\n"
    "s_add_u32 s80, s80, 32768\n"
    "s_cmp_eq_u32 s80, 98304\n"
    "v_mfma_f32_32x32x16_bf16 a[112:127], v[4:7], v[44:47], a[112:127]\n"
    "buffer_load_dwordx4 v119, s[36:39], s77 offen offset:3072 lds\n"
    "s_cselect_b32 s80, 0, s80\n"
    "v_add_u32 v80, s80, v84\n"
    "v_add_u32 v81, s80, v85\n"
    "v_mfma_f32_32x32x16_bf16 a[176:191], v[8:11], v[44:47], a[176:191]\n"
    "v_add_u32 v82, s80, v86\n"
    "v_add_u32 v83, s80, v87\n"
    "v_xor_b32 v88, 229376, v88\n"
    "s_add_u32 s77, s77, 128\n"
    "s_mov_b32 s79, s82\n"
    "v_mfma_f32_32x32x16_bf16 a[240:255], v[12:15], v[44:47], a[240:255]\n"
    "v_xor_b32 v89, 229376, v89\n"
    "s_waitcnt vmcnt(8) lgkmcnt(0)\n"
    "s_barrier\n"
    "v_mfma_f32_32x32x16_bf16 a[0:15], v[16:19], v[48:51], a[0:15]\n"
    "ds_read_b64_tr_b16 v[32:33], v88\n"
    "s_add_u32 m0, s87, 98304\n"
    "v_mfma_f32_32x32x16_bf16 a[64:79], v[20:23], v[48:51], a[64:79]\n"
    "buffer_load_dwordx4 v120, s[40:43], s78 offen lds\n"
    "ds_read_b64_tr_b16 v[34:35], v89\n"
    "v_mfma_f32_32x32x16_bf16 a[128:143], v[24:27], v[48:51], a[128:143]\n"
    "ds_read_b128 v[0:3], v80\n"
    "buffer_load_ushort v164, v130, s[48:51], s90 offen offset:32\n"
    "buffer_load_ushort v165, v130, s[48:51], s90 offen offset:1312\n"
    "v_mfma_f32_32x32x16_bf16 a[192:207], v[28:31], v[48:51], a[192:207]\n"
    "ds_read_b64_tr_b16 v[36:37], v88 offset:512\n"
    "buffer_load_ushort v166, v130, s[48:51], s90 offen offset:2592\n"
    "buffer_load_ushort v167, v130, s[48:51], s90 offen offset:3872\n"
    "v_mfma_f32_32x32x16_bf16 a[16:31], v[16:19], v[52:55], a[16:31]\n"
    "ds_read_b64_tr_b16 v[38:39], v89 offset:512\n"
    "v_mfma_f32_32x32x16_bf16 a[80:95], v[20:23], v[52:55], a[80:95]\n"
    "buffer_load_dwordx4 v121, s[40:43], s78 offen offset:1024 lds\n"
    "ds_read_b128 v[4:7], v80 offset:4096\n"
    "v_mfma_f32_32x32x16_bf16 a[144:159], v[24:27], v[52:55], a[144:159]\n"
    "ds_read_b64_tr_b16 v[40:41], v88 offset:1024\n"
    "buffer_load_ushort v172, v130, s[48:51], s90 offen offset:64\n"
    "buffer_load_ushort v173, v130, s[48:51], s90 offen offset:1344\n"
    "v_mfma_f32_32x32x16_bf16 a[208:223], v[28:31], v[52:55], a[208:223]\n"
    "ds_read_b64_tr_b16 v[42:43], v89 offset:1024\n"
    "buffer_load_ushort v174, v130, s[48:51], s90 offen offset:2624\n"
    "buffer_load_ushort v175, v130, s[48:51], s90 offen offset:3904\n"
    "v_mfma_f32_32x32x16_bf16 a[32:47], v[16:19], v[56:59], a[32:47]\n"
    "ds_read_b128 v[8:11], v80 offset:8192\n"
    "v_mfma_f32_32x32x16_bf16 a[96:111], v[20:23], v[56:59], a[96:111]\n"
    "buffer_load_dwordx4 v122, s[40:43], s78 offen offset:2048 lds\n"
    "ds_read_b64_tr_b16 v[44:45], v88 offset:1536\n"
    "v_mfma_f32_32x32x16_bf16 a[160:175], v[24:27], v[56:59], a[160:175]\n"
    "ds_read_b64_tr_b16 v[46:47], v89 offset:1536\n"
    "buffer_load_ushort v180, v130, s[48:51], s90 offen offset:96\n"
    "buffer_load_ushort v181, v130, s[48:51], s90 offen offset:1376\n"
    "v_mfma_f32_32x32x16_bf16 a[224:239], v[28:31], v[56:59], a[224:239]\n"
    "ds_read_b128 v[12:15], v80 offset:12288\n"
    "buffer_load_ushort v182, v130, s[48:51], s90 offen offset:2656\n"
    "buffer_load_ushort v183, v130, s[48:51], s90 offen offset:3936\n"
    "v_mfma_f32_32x32x16_bf16 a[48:63], v[16:19], v[60:63], a[48:63]\n"
    "v_mfma_f32_32x32x16_bf16 a[112:127], v[20:23], v[60:63], a[112:127]\n"
    "buffer_load_dwordx4 v123, s[40:43], s78 offen offset:3072 lds\n"
    "v_mfma_f32_32x32x16_bf16 a[176:191], v[24:27], v[60:63], a[176:191]\n"
    "s_add_u32 s78, s78, 524288\n"
    "buffer_load_ushort v188, v130, s[48:51], s90 offen offset:128\n"
    "buffer_load_ushort v189, v130, s[48:51], s90 offen offset:1408\n"
    "v_mfma_f32_32x32x16_bf16 a[240:255], v[28:31], v[60:63], a[240:255]\n"
    "buffer_load_ushort v190, v130, s[48:51], s90 offen offset:2688\n"
    "buffer_load_ushort v191, v130, s[48:51], s90 offen offset:3968\n"
    "s_waitcnt lgkmcnt(0)\n"
    "v_mfma_f32_32x32x16_bf16 a[0:15], v[0:3], v[32:35], a[0:15]\n"
    "ds_read_b64_tr_b16 v[48:49], v88 offset:4096\n"
    "s_add_u32 m0, s87, 114688\n"
    "v_mfma_f32_32x32x16_bf16 a[64:79], v[4:7], v[32:35], a[64:79]\n"
    "buffer_load_dwordx4 v120, s[40:43], s78 offen lds\n"
    "ds_read_b64_tr_b16 v[50:51], v89 offset:4096\n"
    "v_lshlrev_b32 v236, 16, v228\n"
    "v_and_b32 v228, 4294901760, v228\n"
    "v_lshlrev_b32 v237, 16, v218\n"
    "v_mfma_f32_32x32x16_bf16 a[128:143], v[8:11], v[32:35], a[128:143]\n"
    "ds_read_b128 v[16:19], v81\n"
    "v_and_b32 v218, 4294901760, v218\n"
    "v_mul_f32 v237, v237, v236\n"
    "v_mul_f32 v218, v218, v228\n"
    "v_cvt_pk_bf16_f32 v237, 0, v237\n"
    "v_mfma_f32_32x32x16_bf16 a[192:207], v[12:15], v[32:35], a[192:207]\n"
    "ds_read_b64_tr_b16 v[52:53], v88 offset:4608\n"
    "v_cvt_pk_bf16_f32 v218, 0, v218\n"
    "v_add_f32 v218, v218, v237\n"
    "v_mov_b32 v234, v218\n"
    "v_lshlrev_b32 v237, 16, v223\n"
    "v_mfma_f32_32x32x16_bf16 a[16:31], v[0:3], v[36:39], a[16:31]\n"
    "ds_read_b64_tr_b16 v[54:55], v89 offset:4608\n"
    "v_and_b32 v223, 4294901760, v223\n"
    "v_mul_f32 v237, v237, v236\n"
    "v_mul_f32 v223, v223, v228\n"
    "v_cvt_pk_bf16_f32 v237, 0, v237\n"
    "v_mfma_f32_32x32x16_bf16 a[80:95], v[4:7], v[36:39], a[80:95]\n"
    "buffer_load_dwordx4 v121, s[40:43], s78 offen offset:1024 lds\n"
    "ds_read_b128 v[20:23], v81 offset:4096\n"
    "v_cvt_pk_bf16_f32 v223, 0, v223\n"
    "v_add_f32 v223, v223, v237\n"
    "v_mov_b32 v235, v223\n"
    "v_mfma_f32_32x32x16_bf16 a[144:159], v[8:11], v[36:39], a[144:159]\n"
    "ds_read_b64_tr_b16 v[56:57], v88 offset:5120\n"
    "v_lshlrev_b32 v236, 16, v229\n"
    "v_and_b32 v229, 4294901760, v229\n"
    "v_lshlrev_b32 v237, 16, v219\n"
    "v_and_b32 v219, 4294901760, v219\n"
    "v_mfma_f32_32x32x16_bf16 a[208:223], v[12:15], v[36:39], a[208:223]\n"
    "ds_read_b64_tr_b16 v[58:59], v89 offset:5120\n"
    "v_mul_f32 v237, v237, v236\n"
    "v_mul_f32 v219, v219, v229\n"
    "v_cvt_pk_bf16_f32 v237, 0, v237\n"
    "v_cvt_pk_bf16_f32 v219, 0, v219\n"
    "v_mfma_f32_32x32x16_bf16 a[32:47], v[0:3], v[40:43], a[32:47]\n"
    "ds_read_b128 v[24:27], v81 offset:8192\n"
    "v_add_f32 v219, v219, v237\n"
    "v_add_f32 v234, v234, v219\n"
    "v_lshlrev_b32 v237, 16, v224\n"
    "v_and_b32 v224, 4294901760, v224\n"
    "v_mfma_f32_32x32x16_bf16 a[96:111], v[4:7], v[40:43], a[96:111]\n"
    "buffer_load_dwordx4 v122, s[40:43], s78 offen offset:2048 lds\n"
    "ds_read_b64_tr_b16 v[60:61], v88 offset:5632\n"
    "v_mul_f32 v237, v237, v236\n"
    "v_mul_f32 v224, v224, v229\n"
    "v_cvt_pk_bf16_f32 v237, 0, v237\n"
    "v_mfma_f32_32x32x16_bf16 a[160:175], v[8:11], v[40:43], a[160:175]\n"
    "ds_read_b64_tr_b16 v[62:63], v89 offset:5632\n"
    "v_cvt_pk_bf16_f32 v224, 0, v224\n"
    "v_add_f32 v224, v224, v237\n"
    "v_add_f32 v235, v235, v224\n"
    "v_lshlrev_b32 v236, 16, v230\n"
    "v_mfma_f32_32x32x16_bf16 a[224:239], v[12:15], v[40:43], a[224:239]\n"
    "ds_read_b128 v[28:31], v81 offset:12288\n"
    "v_and_b32 v230, 4294901760, v230\n"
    "v_lshlrev_b32 v237, 16, v220\n"
    "v_and_b32 v220, 4294901760, v220\n"
    "v_mul_f32 v237, v237, v236\n"
    "v_mfma_f32_32x32x16_bf16 a[48:63], v[0:3], v[44:47], a[48:63]\n"
    "v_mul_f32 v220, v220, v230\n"
    "v_cvt_pk_bf16_f32 v237, 0, v237\n"
    "v_cvt_pk_bf16_f32 v220, 0, v220\n"
    "v_add_f32 v220, v220, v237\n"
    "v_add_f32 v234, v234, v220\n"
    "v_mfma_f32_32x32x16_bf16 a[112:127], v[4:7], v[44:47], a[112:127]\n"
    "buffer_load_dwordx4 v123, s[40:43], s78 offen offset:3072 lds\n"
    "v_lshlrev_b32 v237, 16, v225\n"
    "v_and_b32 v225, 4294901760, v225\n"
    "v_mul_f32 v237, v237, v236\n"
    "v_mul_f32 v225, v225, v230\n"
    "v_mfma_f32_32x32x16_bf16 a[176:191], v[8:11], v[44:47], a[176:191]\n"
    "s_add_u32 s78, s78, 524288\n"
    "v_cvt_pk_bf16_f32 v237, 0, v237\n"
    "v_cvt_pk_bf16_f32 v225, 0, v225\n"
    "v_add_f32 v225, v225, v237\n"
    "v_add_f32 v235, v235, v225\n"
    "v_mfma_f32_32x32x16_bf16 a[240:255], v[12:15], v[44:47], a[240:255]\n"
    "v_lshlrev_b32 v236, 16, v231\n"
    "v_and_b32 v231, 4294901760, v231\n"
    "v_lshlrev_b32 v237, 16, v221\n"
    "v_and_b32 v221, 4294901760, v221\n"
    "v_mul_f32 v237, v237, v236\n"
    "s_waitcnt lgkmcnt(0)\n"
    "v_mfma_f32_32x32x16_bf16 a[0:15], v[16:19], v[48:51], a[0:15]\n"
    "ds_read_b64_tr_b16 v[32:33], v88 offset:8192\n"
    "s_add_u32 m0, s79, 0\n"
    "v_mul_f32 v221, v221, v231\n"
    "v_cvt_pk_bf16_f32 v237, 0, v237\n"
    "v_cvt_pk_bf16_f32 v221, 0, v221\n"
    "v_mfma_f32_32x32x16_bf16 a[64:79], v[20:23], v[48:51], a[64:79]\n"
    "buffer_load_dwordx4 v112, s[36:39], s77 offen lds\n"
    "ds_read_b64_tr_b16 v[34:35], v89 offset:8192\n"
    "v_add_f32 v221, v221, v237\n"
    "v_add_f32 v234, v234, v221\n"
    "v_lshlrev_b32 v237, 16, v226\n"
    "v_mfma_f32_32x32x16_bf16 a[128:143], v[24:27], v[48:51], a[128:143]\n"
    "ds_read_b128 v[0:3], v82\n"
    "v_and_b32 v226, 4294901760, v226\n"
    "v_mul_f32 v237, v237, v236\n"
    "v_mul_f32 v226, v226, v231\n"
    "v_cvt_pk_bf16_f32 v237, 0, v237\n"
    "v_mfma_f32_32x32x16_bf16 a[192:207], v[28:31], v[48:51], a[192:207]\n"
    "ds_read_b64_tr_b16 v[36:37], v88 offset:8704\n"
    "v_cvt_pk_bf16_f32 v226, 0, v226\n"
    "v_add_f32 v226, v226, v237\n"
    "v_add_f32 v235, v235, v226\n"
    "v_lshlrev_b32 v236, 16, v232\n"
    "v_mfma_f32_32x32x16_bf16 a[16:31], v[16:19], v[52:55], a[16:31]\n"
    "ds_read_b64_tr_b16 v[38:39], v89 offset:8704\n"
    "v_and_b32 v232, 4294901760, v232\n"
    "v_lshlrev_b32 v237, 16, v222\n"
    "v_and_b32 v222, 4294901760, v222\n"
    "v_mul_f32 v237, v237, v236\n"
    "v_mfma_f32_32x32x16_bf16 a[80:95], v[20:23], v[52:55], a[80:95]\n"
    "buffer_load_dwordx4 v113, s[36:39], s77 offen offset:1024 lds\n"
    "ds_read_b128 v[4:7], v82 offset:4096\n"
    "v_mul_f32 v222, v222, v232\n"
    "v_cvt_pk_bf16_f32 v237, 0, v237\n"
    "v_cvt_pk_bf16_f32 v222, 0, v222\n"
    "v_mfma_f32_32x32x16_bf16 a[144:159], v[24:27], v[52:55], a[144:159]\n"
    "ds_read_b64_tr_b16 v[40:41], v88 offset:9216\n"
    "v_add_f32 v222, v222, v237\n"
    "v_add_f32 v234, v234, v222\n"
    "v_lshlrev_b32 v237, 16, v227\n"
    "v_and_b32 v227, 4294901760, v227\n"
    "v_mfma_f32_32x32x16_bf16 a[208:223], v[28:31], v[52:55], a[208:223]\n"
    "ds_read_b64_tr_b16 v[42:43], v89 offset:9216\n"
    "v_mul_f32 v237, v237, v236\n"
    "v_mul_f32 v227, v227, v232\n"
    "v_cvt_pk_bf16_f32 v237, 0, v237\n"
    "v_cvt_pk_bf16_f32 v227, 0, v227\n"
    "v_mfma_f32_32x32x16_bf16 a[32:47], v[16:19], v[56:59], a[32:47]\n"
    "ds_read_b128 v[8:11], v82 offset:8192\n"
    "v_add_f32 v227, v227, v237\n"
    "v_add_f32 v235, v235, v227\n"
    "s_nop 1\n"
    "v_add_f32_dpp v234, v234, v234 row_shr:1 row_mask:0xf bank_mask:0xf bound_ctrl:0\n"
    "v_mfma_f32_32x32x16_bf16 a[96:111], v[20:23], v[56:59], a[96:111]\n"
    "buffer_load_dwordx4 v114, s[36:39], s77 offen offset:2048 lds\n"
    "ds_read_b64_tr_b16 v[44:45], v88 offset:9728\n"
    "v_add_f32_dpp v235, v235, v235 row_shr:1 row_mask:0xf bank_mask:0xf bound_ctrl:0\n"
    "s_nop 1\n"
    "v_add_f32_dpp v234, v234, v234 row_shr:2 row_mask:0xf bank_mask:0xf bound_ctrl:0\n"
    "v_mfma_f32_32x32x16_bf16 a[160:175], v[24:27], v[56:59], a[160:175]\n"
    "ds_read_b64_tr_b16 v[46:47], v89 offset:9728\n"
    "v_mov_b32 v64, v112\n"
    "v_add_f32_dpp v235, v235, v235 row_shr:2 row_mask:0xf bank_mask:0xf bound_ctrl:0\n"
    "s_nop 1\n"
    "v_add_f32_dpp v234, v234, v234 row_shr:4 row_mask:0xf bank_mask:0xf bound_ctrl:0\n"
    "v_mfma_f32_32x32x16_bf16 a[224:239], v[28:31], v[56:59], a[224:239]\n"
    "ds_read_b128 v[12:15], v82 offset:12288\n"
    "v_add_f32_dpp v235, v235, v235 row_shr:4 row_mask:0xf bank_mask:0xf bound_ctrl:0\n"
    "s_nop 1\n"
    "v_add_f32_dpp v234, v234, v234 row_shr:8 row_mask:0xf bank_mask:0xf bound_ctrl:0\n"
    "v_add_f32_dpp v235, v235, v235 row_shr:8 row_mask:0xf bank_mask:0xf bound_ctrl:0\n"
    "v_mfma_f32_32x32x16_bf16 a[48:63], v[16:19], v[60:63], a[48:63]\n"
    "v_mov_b32 v65, v113\n"
    "s_nop 1\n"
    "v_add_f32_dpp v234, v234, v234 row_bcast:15 row_mask:0xa bank_mask:0xf\n"
    "v_add_f32_dpp v235, v235, v235 row_bcast:15 row_mask:0xa bank_mask:0xf\n"
    "s_nop 1\n"
    "v_mfma_f32_32x32x16_bf16 a[112:127], v[20:23], v[60:63], a[112:127]\n"
    "buffer_load_dwordx4 v115, s[36:39], s77 offen offset:3072 lds\n"
    "v_add_f32_dpp v234, v234, v234 row_bcast:31 row_mask:0xc bank_mask:0xf\n"
    "v_add_f32_dpp v235, v235, v235 row_bcast:31 row_mask:0xc bank_mask:0xf\n"
    "s_nop 1\n"
    "v_readlane_b32 s88, v234, 63\n"
    "v_mfma_f32_32x32x16_bf16 a[176:191], v[24:27], v[60:63], a[176:191]\n"
    "v_mov_b32 v66, v114\n"
    "v_readlane_b32 s89, v235, 63\n"
    "s_nop 3\n"
    "v_mov_b32 v238, s88\n"
    "v_cvt_pk_bf16_f32 v238, 0, v238\n"
    "v_mfma_f32_32x32x16_bf16 a[240:255], v[28:31], v[60:63], a[240:255]\n"
    "v_mul_f32 v238, 0xbfb8aa3b, v238\n"
    "v_exp_f32 v238, v238\n"
    "s_nop 0\n"
    "v_add_f32 v238, 0x3f800000, v238\n"
    "v_rcp_f32 v238, v238\n"
    "s_waitcnt lgkmcnt(0)\n"
    "v_mfma_f32_32x32x16_bf16 a[0:15], v[0:3], v[32:35], a[0:15]\n"
    "ds_read_b64_tr_b16 v[48:49], v88 offset:12288\n"
    "s_add_u32 m0, s79, 4096\n"
    "v_mov_b32 v67, v115\n"
    "s_nop 0\n"
    "v_cvt_pk_bf16_f32 v238, 0, v238\n"
    "v_mfma_f32_32x32x16_bf16 a[64:79], v[4:7], v[32:35], a[64:79]\n"
    "buffer_load_dwordx4 v116, s[36:39], s77 offen lds\n"
    "ds_read_b64_tr_b16 v[50:51], v89 offset:12288\n"
    "v_mov_b32 v239, s89\n"
    "v_cvt_pk_bf16_f32 v239, 0, v239\n"
    "v_mul_f32 v239, 0xbfb8aa3b, v239\n"
    "v_mfma_f32_32x32x16_bf16 a[128:143], v[8:11], v[32:35], a[128:143]\n"
    "ds_read_b128 v[16:19], v83\n"
    "v_mov_b32 v68, v116\n"
    "v_exp_f32 v239, v239\n"
    "s_nop 0\n"
    "v_add_f32 v239, 0x3f800000, v239\n"
    "v_mfma_f32_32x32x16_bf16 a[192:207], v[12:15], v[32:35], a[192:207]\n"
    "ds_read_b64_tr_b16 v[52:53], v88 offset:12800\n"
    "v_rcp_f32 v239, v239\n"
    "s_nop 0\n"
    "v_cvt_pk_bf16_f32 v239, 0, v239\n"
    "v_mfma_f32_32x32x16_bf16 a[16:31], v[0:3], v[36:39], a[16:31]\n"
    "ds_read_b64_tr_b16 v[54:55], v89 offset:12800\n"
    "v_mov_b32 v69, v117\n"
    "v_mfma_f32_32x32x16_bf16 a[80:95], v[4:7], v[36:39], a[80:95]\n"
    "buffer_load_dwordx4 v117, s[36:39], s77 offen offset:1024 lds\n"
    "ds_read_b128 v[20:23], v83 offset:4096\n"
    "v_mfma_f32_32x32x16_bf16 a[144:159], v[8:11], v[36:39], a[144:159]\n"
    "ds_read_b64_tr_b16 v[56:57], v88 offset:13312\n"
    "v_mov_b32 v70, v118\n"
    "v_mfma_f32_32x32x16_bf16 a[208:223], v[12:15], v[36:39], a[208:223]\n"
    "ds_read_b64_tr_b16 v[58:59], v89 offset:13312\n"
    "v_mfma_f32_32x32x16_bf16 a[32:47], v[0:3], v[40:43], a[32:47]\n"
    "ds_read_b128 v[24:27], v83 offset:8192\n"
    "v_mov_b32 v71, v119\n"
    "s_add_u32 s44, s74, 67108864\n"
    "s_addc_u32 s45, s75, 0\n"
    "s_and_b32 s45, s45, 65535\n"
    "s_mov_b32 s46, 5242880\n"
    "s_mov_b32 s47, 131072\n"
    "v_mfma_f32_32x32x16_bf16 a[96:111], v[4:7], v[40:43], a[96:111]\n"
    "buffer_load_dwordx4 v118, s[36:39], s77 offen offset:2048 lds\n"
    "ds_read_b64_tr_b16 v[60:61], v88 offset:13824\n"
    "s_add_u32 s56, s74, 83886080\n"
    "s_addc_u32 s57, s75, 0\n"
    "s_and_b32 s57, s57, 65535\n"
    "s_mov_b32 s58, 8192\n"
    "s_mov_b32 s59, 131072\n"
    "v_mfma_f32_32x32x16_bf16 a[160:175], v[8:11], v[40:43], a[160:175]\n"
    "ds_read_b64_tr_b16 v[62:63], v89 offset:13824\n"
    "v_mov_b32 v72, v120\n"
    "s_lshl_b32 s88, s93, 2\n"
    "v_mov_b32 v240, 0\n"
    "v_mfma_f32_32x32x16_bf16 a[224:239], v[12:15], v[40:43], a[224:239]\n"
    "ds_read_b128 v[28:31], v83 offset:12288\n"
    "buffer_store_dwordx2 v[238:239], v240, s[56:59], s88 offen\n"
    "v_mfma_f32_32x32x16_bf16 a[48:63], v[0:3], v[44:47], a[48:63]\n"
    "v_add_u32 v80, 65536, v124\n"
    "v_mov_b32 v84, v124\n"
    "v_add_u32 v81, 65536, v125\n"
    "v_mov_b32 v85, v125\n"
    "v_mfma_f32_32x32x16_bf16 a[112:127], v[4:7], v[44:47], a[112:127]\n"
    "buffer_load_dwordx4 v119, s[36:39], s77 offen offset:3072 lds\n"
    "v_add_u32 v82, 65536, v126\n"
    "v_mov_b32 v86, v126\n"
    "v_add_u32 v83, 65536, v127\n"
    "v_mov_b32 v87, v127\n"
    "v_mfma_f32_32x32x16_bf16 a[176:191], v[8:11], v[44:47], a[176:191]\n"
    "v_mov_b32 v88, v128\n"
    "v_mov_b32 v89, v129\n"
    "v_mov_b32 v98, v128\n"
    "s_add_u32 s77, s77, 128\n"
    "v_mfma_f32_32x32x16_bf16 a[240:255], v[12:15], v[44:47], a[240:255]\n"
    "v_mov_b32 v99, v129\n"
    "s_waitcnt vmcnt(12) lgkmcnt(0)\n"
    "s_barrier\n"
    "v_mfma_f32_32x32x16_bf16 a[0:15], v[16:19], v[48:51], a[0:15]\n"
    "ds_read_b64_tr_b16 v[32:33], v88\n"
    "s_add_u32 m0, s87, 131072\n"
    "v_mfma_f32_32x32x16_bf16 a[64:79], v[20:23], v[48:51], a[64:79]\n"
    "buffer_load_dwordx4 v120, s[40:43], s78 offen lds\n"
    "ds_read_b64_tr_b16 v[34:35], v89\n"
    "v_lshl_or_b32 v132, v153, 16, v152\n"
    "v_lshl_or_b32 v133, v155, 16, v154\n"
    "v_lshl_or_b32 v134, v157, 16, v156\n"
    "v_lshl_or_b32 v135, v159, 16, v158\n"
    "v_mfma_f32_32x32x16_bf16 a[128:143], v[24:27], v[48:51], a[128:143]\n"
    "ds_read_b128 v[0:3], v80\n"
    "v_mov_b32 v73, v121\n"
    "s_add_u32 s88, s91, 0\n"
    "buffer_store_dwordx4 v[132:135], v251, s[44:47], s88 offen\n"
    "v_mfma_f32_32x32x16_bf16 a[192:207], v[28:31], v[48:51], a[192:207]\n"
    "ds_read_b128 v[4:7], v80 offset:4096\n"
    "v_mfma_f32_32x32x16_bf16 a[16:31], v[16:19], v[52:55], a[16:31]\n"
    "ds_read_b64_tr_b16 v[36:37], v88 offset:512\n"
    "v_mov_b32 v74, v122\n"
    "v_lshl_or_b32 v136, v161, 16, v160\n"
    "v_lshl_or_b32 v137, v163, 16, v162\n"
    "v_lshl_or_b32 v138, v165, 16, v164\n"
    "v_lshl_or_b32 v139, v167, 16, v166\n"
    "v_mfma_f32_32x32x16_bf16 a[80:95], v[20:23], v[52:55], a[80:95]\n"
    "buffer_load_dwordx4 v121, s[40:43], s78 offen offset:1024 lds\n"
    "ds_read_b64_tr_b16 v[38:39], v89 offset:512\n"
    "s_add_u32 s88, s91, 131072\n"
    "buffer_store_dwordx4 v[136:139], v251, s[44:47], s88 offen\n"
    "v_mfma_f32_32x32x16_bf16 a[144:159], v[24:27], v[52:55], a[144:159]\n"
    "ds_read_b128 v[8:11], v80 offset:8192\n"
    "v_mov_b32 v75, v123\n"
    "v_mfma_f32_32x32x16_bf16 a[208:223], v[28:31], v[52:55], a[208:223]\n"
    "ds_read_b128 v[12:15], v80 offset:12288\n"
    "v_lshl_or_b32 v140, v169, 16, v168\n"
    "v_lshl_or_b32 v141, v171, 16, v170\n"
    "v_lshl_or_b32 v142, v173, 16, v172\n"
    "v_lshl_or_b32 v143, v175, 16, v174\n"
    "v_mfma_f32_32x32x16_bf16 a[32:47], v[16:19], v[56:59], a[32:47]\n"
    "s_add_u32 s88, s91, 262144\n"
    "buffer_store_dwordx4 v[140:143], v251, s[44:47], s88 offen\n"
    "v_mfma_f32_32x32x16_bf16 a[96:111], v[20:23], v[56:59], a[96:111]\n"
    "buffer_load_dwordx4 v122, s[40:43], s78 offen offset:2048 lds\n"
    "v_mfma_f32_32x32x16_bf16 a[160:175], v[24:27], v[56:59], a[160:175]\n"
    "v_lshl_or_b32 v144, v177, 16, v176\n"
    "v_lshl_or_b32 v145, v179, 16, v178\n"
    "v_lshl_or_b32 v146, v181, 16, v180\n"
    "v_lshl_or_b32 v147, v183, 16, v182\n"
    "v_mfma_f32_32x32x16_bf16 a[224:239], v[28:31], v[56:59], a[224:239]\n"
    "s_add_u32 s88, s91, 393216\n"
    "buffer_store_dwordx4 v[144:147], v251, s[44:47], s88 offen\n"
    "v_mfma_f32_32x32x16_bf16 a[48:63], v[16:19], v[60:63], a[48:63]\n"
    "v_mfma_f32_32x32x16_bf16 a[112:127], v[20:23], v[60:63], a[112:127]\n"
    "buffer_load_dwordx4 v123, s[40:43], s78 offen offset:3072 lds\n"
    "v_lshl_or_b32 v148, v185, 16, v184\n"
    "v_lshl_or_b32 v149, v187, 16, v186\n"
    "v_lshl_or_b32 v150, v189, 16, v188\n"
    "v_lshl_or_b32 v151, v191, 16, v190\n"
    "v_mfma_f32_32x32x16_bf16 a[176:191], v[24:27], v[60:63], a[176:191]\n"
    "s_add_u32 s78, s78, 524288\n"
    "s_add_u32 s88, s91, 524288\n"
    "buffer_store_dwordx4 v[148:151], v251, s[44:47], s88 offen\n"
    "v_mfma_f32_32x32x16_bf16 a[240:255], v[28:31], v[60:63], a[240:255]\n"
    "s_lshl_b32 s82, s61, 19\n"
    "s_add_u32 s44, s74, 16777216\n"
    "s_addc_u32 s45, s75, 0\n"
    "s_add_u32 s44, s44, s82\n"
    "s_addc_u32 s45, s45, 0\n"
    "s_and_b32 s45, s45, 65535\n"
    "s_mov_b32 s46, 524288\n"
    "s_mov_b32 s47, 131072\n"
    "L_qbody_%=:\n"
    "s_lshl_b32 s82, s62, 3\n"
    "s_and_b32 s83, s60, 1\n"
    "s_lshl_b32 s83, s83, 2\n"
    "s_add_u32 s82, s82, s83\n"
    "s_lshl_b32 s95, s82, 13\n"
    "s_waitcnt lgkmcnt(0)\n"
    "v_mfma_f32_32x32x16_bf16 v[112:127], v[32:35], v[0:3], 0\n"
    "ds_read_b64_tr_b16 v[48:49], v88 offset:4096\n"
    "s_add_u32 m0, s94, 32768\n"
    "v_accvgpr_read_b32 v90, a0\n"
    "v_accvgpr_read_b32 v91, a1\n"
    "v_accvgpr_read_b32 v92, a2\n"
    "v_mfma_f32_32x32x16_bf16 v[128:143], v[36:39], v[0:3], 0\n"
    "buffer_load_dwordx4 v64, s[36:39], s77 offen lds\n"
    "ds_read_b64_tr_b16 v[50:51], v89 offset:4096\n"
    "v_accvgpr_read_b32 v93, a3\n"
    "v_accvgpr_read_b32 v94, a4\n"
    "v_accvgpr_read_b32 v95, a5\n"
    "v_mfma_f32_32x32x16_bf16 v[144:159], v[32:35], v[4:7], 0\n"
    "ds_read_b128 v[16:19], v81\n"
    "v_accvgpr_read_b32 v96, a6\n"
    "v_accvgpr_read_b32 v97, a7\n"
    "v_cvt_pk_bf16_f32 v100, v90, v91\n"
    "v_cvt_pk_bf16_f32 v101, v92, v93\n"
    "v_mfma_f32_32x32x16_bf16 v[160:175], v[36:39], v[4:7], 0\n"
    "buffer_load_dwordx4 v65, s[36:39], s77 offen offset:1024 lds\n"
    "ds_read_b128 v[20:23], v81 offset:4096\n"
    "v_cvt_pk_bf16_f32 v102, v94, v95\n"
    "v_cvt_pk_bf16_f32 v103, v96, v97\n"
    "v_accvgpr_read_b32 v90, a8\n"
    "v_mfma_f32_32x32x16_bf16 v[176:191], v[32:35], v[8:11], 0\n"
    "ds_read_b64_tr_b16 v[52:53], v88 offset:4608\n"
    "v_accvgpr_read_b32 v91, a9\n"
    "v_accvgpr_read_b32 v92, a10\n"
    "v_accvgpr_read_b32 v93, a11\n"
    "v_accvgpr_read_b32 v94, a12\n"
    "v_mfma_f32_32x32x16_bf16 v[192:207], v[36:39], v[8:11], 0\n"
    "buffer_load_dwordx4 v66, s[36:39], s77 offen offset:2048 lds\n"
    "ds_read_b64_tr_b16 v[54:55], v89 offset:4608\n"
    "v_accvgpr_read_b32 v95, a13\n"
    "v_accvgpr_read_b32 v96, a14\n"
    "v_accvgpr_read_b32 v97, a15\n"
    "v_mfma_f32_32x32x16_bf16 v[208:223], v[32:35], v[12:15], 0\n"
    "ds_read_b128 v[24:27], v81 offset:8192\n"
    "v_cvt_pk_bf16_f32 v104, v90, v91\n"
    "v_cvt_pk_bf16_f32 v105, v92, v93\n"
    "v_cvt_pk_bf16_f32 v106, v94, v95\n"
    "v_cvt_pk_bf16_f32 v107, v96, v97\n"
    "v_mfma_f32_32x32x16_bf16 v[224:239], v[36:39], v[12:15], 0\n"
    "buffer_load_dwordx4 v67, s[36:39], s77 offen offset:3072 lds\n"
    "ds_read_b128 v[28:31], v81 offset:12288\n"
    "v_accvgpr_read_b32 v90, a16\n"
    "v_accvgpr_read_b32 v91, a17\n"
    "v_accvgpr_read_b32 v92, a18\n"
    "s_waitcnt lgkmcnt(0)\n"
    "v_mfma_f32_32x32x16_bf16 v[112:127], v[48:51], v[16:19], v[112:127]\n"
    "ds_read_b64_tr_b16 v[32:33], v88 offset:8192\n"
    "s_add_u32 m0, s94, 36864\n"
    "v_accvgpr_read_b32 v93, a19\n"
    "v_accvgpr_read_b32 v94, a20\n"
    "v_accvgpr_read_b32 v95, a21\n"
    "v_mfma_f32_32x32x16_bf16 v[128:143], v[52:55], v[16:19], v[128:143]\n"
    "buffer_load_dwordx4 v68, s[36:39], s77 offen lds\n"
    "ds_read_b64_tr_b16 v[34:35], v89 offset:8192\n"
    "v_accvgpr_read_b32 v96, a22\n"
    "v_accvgpr_read_b32 v97, a23\n"
    "v_cvt_pk_bf16_f32 v108, v90, v91\n"
    "v_mfma_f32_32x32x16_bf16 v[144:159], v[48:51], v[20:23], v[144:159]\n"
    "ds_read_b128 v[0:3], v82\n"
    "v_cvt_pk_bf16_f32 v109, v92, v93\n"
    "v_cvt_pk_bf16_f32 v110, v94, v95\n"
    "v_cvt_pk_bf16_f32 v111, v96, v97\n"
    "v_accvgpr_read_b32 v90, a24\n"
    "v_mfma_f32_32x32x16_bf16 v[160:175], v[52:55], v[20:23], v[160:175]\n"
    "buffer_load_dwordx4 v69, s[36:39], s77 offen offset:1024 lds\n"
    "ds_read_b128 v[4:7], v82 offset:4096\n"
    "v_accvgpr_read_b32 v91, a25\n"
    "v_accvgpr_read_b32 v92, a26\n"
    "v_accvgpr_read_b32 v93, a27\n"
    "v_mfma_f32_32x32x16_bf16 v[176:191], v[48:51], v[24:27], v[176:191]\n"
    "ds_read_b64_tr_b16 v[36:37], v88 offset:8704\n"
    "v_accvgpr_read_b32 v94, a28\n"
    "v_accvgpr_read_b32 v95, a29\n"
    "v_accvgpr_read_b32 v96, a30\n"
    "v_accvgpr_read_b32 v97, a31\n"
    "v_mfma_f32_32x32x16_bf16 v[192:207], v[52:55], v[24:27], v[192:207]\n"
    "buffer_load_dwordx4 v70, s[36:39], s77 offen offset:2048 lds\n"
    "ds_read_b64_tr_b16 v[38:39], v89 offset:8704\n"
    "v_cvt_pk_bf16_f32 v252, v90, v91\n"
    "v_cvt_pk_bf16_f32 v253, v92, v93\n"
    "v_cvt_pk_bf16_f32 v254, v94, v95\n"
    "v_mfma_f32_32x32x16_bf16 v[208:223], v[48:51], v[28:31], v[208:223]\n"
    "ds_read_b128 v[8:11], v82 offset:8192\n"
    "v_cvt_pk_bf16_f32 v255, v96, v97\n"
    "v_accvgpr_read_b32 v90, a32\n"
    "v_accvgpr_read_b32 v91, a33\n"
    "v_accvgpr_read_b32 v92, a34\n"
    "v_mfma_f32_32x32x16_bf16 v[224:239], v[52:55], v[28:31], v[224:239]\n"
    "buffer_load_dwordx4 v71, s[36:39], s77 offen offset:3072 lds\n"
    "ds_read_b128 v[12:15], v82 offset:12288\n"
    "v_accvgpr_read_b32 v93, a35\n"
    "v_accvgpr_read_b32 v94, a36\n"
    "v_accvgpr_read_b32 v95, a37\n"
    "s_waitcnt lgkmcnt(0)\n"
    "v_mfma_f32_32x32x16_bf16 v[112:127], v[32:35], v[0:3], v[112:127]\n"
    "ds_read_b64_tr_b16 v[48:49], v88 offset:12288\n"
    "s_add_u32 s77, s77, 128\n"
    "s_add_u32 s85, s95, 0\n"
    "v_accvgpr_read_b32 v96, a38\n"
    "v_accvgpr_read_b32 v97, a39\n"
    "v_mfma_f32_32x32x16_bf16 v[128:143], v[36:39], v[0:3], v[128:143]\n"
    "buffer_store_dwordx4 v[100:103], v251, s[44:47], s85 offen sc1\n"
    "ds_read_b64_tr_b16 v[50:51], v89 offset:12288\n"
    "v_cvt_pk_bf16_f32 v240, v90, v91\n"
    "v_cvt_pk_bf16_f32 v241, v92, v93\n"
    "v_cvt_pk_bf16_f32 v242, v94, v95\n"
    "v_mfma_f32_32x32x16_bf16 v[144:159], v[32:35], v[4:7], v[144:159]\n"
    "ds_read_b128 v[16:19], v83\n"
    "s_add_u32 s85, s95, 0\n"
    "v_cvt_pk_bf16_f32 v243, v96, v97\n"
    "v_accvgpr_read_b32 v90, a40\n"
    "v_accvgpr_read_b32 v91, a41\n"
    "v_mfma_f32_32x32x16_bf16 v[160:175], v[36:39], v[4:7], v[160:175]\n"
    "buffer_store_dwordx4 v[104:107], v251, s[44:47], s85 offen offset:1024 sc1\n"
    "ds_read_b128 v[20:23], v83 offset:4096\n"
    "v_accvgpr_read_b32 v92, a42\n"
    "v_accvgpr_read_b32 v93, a43\n"
    "v_accvgpr_read_b32 v94, a44\n"
    "v_mfma_f32_32x32x16_bf16 v[176:191], v[32:35], v[8:11], v[176:191]\n"
    "ds_read_b64_tr_b16 v[52:53], v88 offset:12800\n"
    "v_add_u32 v80, 0, v84\n"
    "v_add_u32 v81, 0, v85\n"
    "s_add_u32 s85, s95, 0\n"
    "v_accvgpr_read_b32 v95, a45\n"
    "v_mfma_f32_32x32x16_bf16 v[192:207], v[36:39], v[8:11], v[192:207]\n"
    "buffer_store_dwordx4 v[108:111], v251, s[44:47], s85 offen offset:2048 sc1\n"
    "ds_read_b64_tr_b16 v[54:55], v89 offset:12800\n"
    "v_add_u32 v82, 0, v86\n"
    "v_accvgpr_read_b32 v96, a46\n"
    "v_accvgpr_read_b32 v97, a47\n"
    "v_mfma_f32_32x32x16_bf16 v[208:223], v[32:35], v[12:15], v[208:223]\n"
    "ds_read_b128 v[24:27], v83 offset:8192\n"
    "v_add_u32 v88, 16384, v98\n"
    "v_add_u32 v89, 16384, v99\n"
    "s_add_u32 s85, s95, 0\n"
    "v_cvt_pk_bf16_f32 v244, v90, v91\n"
    "v_mfma_f32_32x32x16_bf16 v[224:239], v[36:39], v[12:15], v[224:239]\n"
    "buffer_store_dwordx4 v[252:255], v251, s[44:47], s85 offen offset:3072 sc1\n"
    "ds_read_b128 v[28:31], v83 offset:12288\n"
    "v_add_u32 v83, 0, v87\n"
    "v_cvt_pk_bf16_f32 v245, v92, v93\n"
    "v_cvt_pk_bf16_f32 v246, v94, v95\n"
    "s_waitcnt vmcnt(21) lgkmcnt(0)\n"
    "s_barrier\n"
    "v_mfma_f32_32x32x16_bf16 v[112:127], v[48:51], v[16:19], v[112:127]\n"
    "ds_read_b64_tr_b16 v[32:33], v88\n"
    "s_add_u32 m0, s87, 98304\n"
    "s_add_u32 s85, s95, 4096\n"
    "v_cvt_pk_bf16_f32 v247, v96, v97\n"
    "v_mfma_f32_32x32x16_bf16 v[128:143], v[52:55], v[16:19], v[128:143]\n"
    "buffer_store_dwordx4 v[240:243], v251, s[44:47], s85 offen sc1\n"
    "buffer_load_dwordx4 v72, s[40:43], s78 offen lds\n"
    "ds_read_b64_tr_b16 v[34:35], v89\n"
    "v_mfma_f32_32x32x16_bf16 v[144:159], v[48:51], v[20:23], v[144:159]\n"
    "ds_read_b128 v[0:3], v80\n"
    "s_add_u32 s85, s95, 4096\n"
    "v_mfma_f32_32x32x16_bf16 v[160:175], v[52:55], v[20:23], v[160:175]\n"
    "buffer_store_dwordx4 v[244:247], v251, s[44:47], s85 offen offset:1024 sc1\n"
    "buffer_load_dwordx4 v73, s[40:43], s78 offen offset:1024 lds\n"
    "ds_read_b128 v[4:7], v80 offset:4096\n"
    "v_mfma_f32_32x32x16_bf16 v[176:191], v[48:51], v[24:27], v[176:191]\n"
    "ds_read_b64_tr_b16 v[36:37], v88 offset:512\n"
    "v_mfma_f32_32x32x16_bf16 v[192:207], v[52:55], v[24:27], v[192:207]\n"
    "buffer_load_dwordx4 v74, s[40:43], s78 offen offset:2048 lds\n"
    "ds_read_b64_tr_b16 v[38:39], v89 offset:512\n"
    "v_mfma_f32_32x32x16_bf16 v[208:223], v[48:51], v[28:31], v[208:223]\n"
    "buffer_load_dwordx4 v75, s[40:43], s78 offen offset:3072 lds\n"
    "ds_read_b128 v[8:11], v80 offset:8192\n"
    "v_mfma_f32_32x32x16_bf16 v[224:239], v[52:55], v[28:31], v[224:239]\n"
    "ds_read_b128 v[12:15], v80 offset:12288\n"
    "s_add_u32 s78, s78, 524288\n"
    "s_waitcnt lgkmcnt(0)\n"
    "v_mfma_f32_32x32x16_bf16 v[112:127], v[32:35], v[0:3], v[112:127]\n"
    "ds_read_b64_tr_b16 v[48:49], v88 offset:4096\n"
    "s_add_u32 m0, s94, 65536\n"
    "v_accvgpr_read_b32 v90, a48\n"
    "v_accvgpr_read_b32 v91, a49\n"
    "v_accvgpr_read_b32 v92, a50\n"
    "v_mfma_f32_32x32x16_bf16 v[128:143], v[36:39], v[0:3], v[128:143]\n"
    "buffer_load_dwordx4 v64, s[36:39], s77 offen lds\n"
    "ds_read_b64_tr_b16 v[50:51], v89 offset:4096\n"
    "v_accvgpr_read_b32 v93, a51\n"
    "v_accvgpr_read_b32 v94, a52\n"
    "v_accvgpr_read_b32 v95, a53\n"
    "v_mfma_f32_32x32x16_bf16 v[144:159], v[32:35], v[4:7], v[144:159]\n"
    "ds_read_b128 v[16:19], v81\n"
    "v_accvgpr_read_b32 v96, a54\n"
    "v_accvgpr_read_b32 v97, a55\n"
    "v_cvt_pk_bf16_f32 v100, v90, v91\n"
    "v_cvt_pk_bf16_f32 v101, v92, v93\n"
    "v_mfma_f32_32x32x16_bf16 v[160:175], v[36:39], v[4:7], v[160:175]\n"
    "buffer_load_dwordx4 v65, s[36:39], s77 offen offset:1024 lds\n"
    "ds_read_b128 v[20:23], v81 offset:4096\n"
    "v_cvt_pk_bf16_f32 v102, v94, v95\n"
    "v_cvt_pk_bf16_f32 v103, v96, v97\n"
    "v_accvgpr_read_b32 v90, a56\n"
    "v_mfma_f32_32x32x16_bf16 v[176:191], v[32:35], v[8:11], v[176:191]\n"
    "ds_read_b64_tr_b16 v[52:53], v88 offset:4608\n"
    "v_accvgpr_read_b32 v91, a57\n"
    "v_accvgpr_read_b32 v92, a58\n"
    "v_accvgpr_read_b32 v93, a59\n"
    "v_accvgpr_read_b32 v94, a60\n"
    "v_mfma_f32_32x32x16_bf16 v[192:207], v[36:39], v[8:11], v[192:207]\n"
    "buffer_load_dwordx4 v66, s[36:39], s77 offen offset:2048 lds\n"
    "ds_read_b64_tr_b16 v[54:55], v89 offset:4608\n"
    "v_accvgpr_read_b32 v95, a61\n"
    "v_accvgpr_read_b32 v96, a62\n"
    "v_accvgpr_read_b32 v97, a63\n"
    "v_mfma_f32_32x32x16_bf16 v[208:223], v[32:35], v[12:15], v[208:223]\n"
    "ds_read_b128 v[24:27], v81 offset:8192\n"
    "v_cvt_pk_bf16_f32 v104, v90, v91\n"
    "v_cvt_pk_bf16_f32 v105, v92, v93\n"
    "v_cvt_pk_bf16_f32 v106, v94, v95\n"
    "v_cvt_pk_bf16_f32 v107, v96, v97\n"
    "v_mfma_f32_32x32x16_bf16 v[224:239], v[36:39], v[12:15], v[224:239]\n"
    "buffer_load_dwordx4 v67, s[36:39], s77 offen offset:3072 lds\n"
    "ds_read_b128 v[28:31], v81 offset:12288\n"
    "v_accvgpr_read_b32 v90, a64\n"
    "v_accvgpr_read_b32 v91, a65\n"
    "v_accvgpr_read_b32 v92, a66\n"
    "s_waitcnt lgkmcnt(0)\n"
    "v_mfma_f32_32x32x16_bf16 v[112:127], v[48:51], v[16:19], v[112:127]\n"
    "ds_read_b64_tr_b16 v[32:33], v88 offset:8192\n"
    "s_add_u32 m0, s94, 69632\n"
    "v_accvgpr_read_b32 v93, a67\n"
    "v_accvgpr_read_b32 v94, a68\n"
    "v_accvgpr_read_b32 v95, a69\n"
    "v_mfma_f32_32x32x16_bf16 v[128:143], v[52:55], v[16:19], v[128:143]\n"
    "buffer_load_dwordx4 v68, s[36:39], s77 offen lds\n"
    "ds_read_b64_tr_b16 v[34:35], v89 offset:8192\n"
    "v_accvgpr_read_b32 v96, a70\n"
    "v_accvgpr_read_b32 v97, a71\n"
    "v_cvt_pk_bf16_f32 v108, v90, v91\n"
    "v_mfma_f32_32x32x16_bf16 v[144:159], v[48:51], v[20:23], v[144:159]\n"
    "ds_read_b128 v[0:3], v82\n"
    "v_cvt_pk_bf16_f32 v109, v92, v93\n"
    "v_cvt_pk_bf16_f32 v110, v94, v95\n"
    "v_cvt_pk_bf16_f32 v111, v96, v97\n"
    "v_accvgpr_read_b32 v90, a72\n"
    "v_mfma_f32_32x32x16_bf16 v[160:175], v[52:55], v[20:23], v[160:175]\n"
    "buffer_load_dwordx4 v69, s[36:39], s77 offen offset:1024 lds\n"
    "ds_read_b128 v[4:7], v82 offset:4096\n"
    "v_accvgpr_read_b32 v91, a73\n"
    "v_accvgpr_read_b32 v92, a74\n"
    "v_accvgpr_read_b32 v93, a75\n"
    "v_mfma_f32_32x32x16_bf16 v[176:191], v[48:51], v[24:27], v[176:191]\n"
    "ds_read_b64_tr_b16 v[36:37], v88 offset:8704\n"
    "v_accvgpr_read_b32 v94, a76\n"
    "v_accvgpr_read_b32 v95, a77\n"
    "v_accvgpr_read_b32 v96, a78\n"
    "v_accvgpr_read_b32 v97, a79\n"
    "v_mfma_f32_32x32x16_bf16 v[192:207], v[52:55], v[24:27], v[192:207]\n"
    "buffer_load_dwordx4 v70, s[36:39], s77 offen offset:2048 lds\n"
    "ds_read_b64_tr_b16 v[38:39], v89 offset:8704\n"
    "v_cvt_pk_bf16_f32 v252, v90, v91\n"
    "v_cvt_pk_bf16_f32 v253, v92, v93\n"
    "v_cvt_pk_bf16_f32 v254, v94, v95\n"
    "v_mfma_f32_32x32x16_bf16 v[208:223], v[48:51], v[28:31], v[208:223]\n"
    "ds_read_b128 v[8:11], v82 offset:8192\n"
    "v_cvt_pk_bf16_f32 v255, v96, v97\n"
    "v_accvgpr_read_b32 v90, a80\n"
    "v_accvgpr_read_b32 v91, a81\n"
    "v_accvgpr_read_b32 v92, a82\n"
    "v_mfma_f32_32x32x16_bf16 v[224:239], v[52:55], v[28:31], v[224:239]\n"
    "buffer_load_dwordx4 v71, s[36:39], s77 offen offset:3072 lds\n"
    "ds_read_b128 v[12:15], v82 offset:12288\n"
    "v_accvgpr_read_b32 v93, a83\n"
    "v_accvgpr_read_b32 v94, a84\n"
    "v_accvgpr_read_b32 v95, a85\n"
    "s_waitcnt lgkmcnt(0)\n"
    "v_mfma_f32_32x32x16_bf16 v[112:127], v[32:35], v[0:3], v[112:127]\n"
    "ds_read_b64_tr_b16 v[48:49], v88 offset:12288\n"
    "s_add_u32 s77, s77, 128\n"
    "s_add_u32 s85, s95, 4096\n"
    "v_accvgpr_read_b32 v96, a86\n"
    "v_accvgpr_read_b32 v97, a87\n"
    "v_mfma_f32_32x32x16_bf16 v[128:143], v[36:39], v[0:3], v[128:143]\n"
    "buffer_store_dwordx4 v[100:103], v251, s[44:47], s85 offen offset:2048 sc1\n"
    "ds_read_b64_tr_b16 v[50:51], v89 offset:12288\n"
    "v_cvt_pk_bf16_f32 v240, v90, v91\n"
    "v_cvt_pk_bf16_f32 v241, v92, v93\n"
    "v_cvt_pk_bf16_f32 v242, v94, v95\n"
    "v_mfma_f32_32x32x16_bf16 v[144:159], v[32:35], v[4:7], v[144:159]\n"
    "ds_read_b128 v[16:19], v83\n"
    "s_add_u32 s85, s95, 4096\n"
    "v_cvt_pk_bf16_f32 v243, v96, v97\n"
    "v_accvgpr_read_b32 v90, a88\n"
    "v_accvgpr_read_b32 v91, a89\n"
    "v_mfma_f32_32x32x16_bf16 v[160:175], v[36:39], v[4:7], v[160:175]\n"
    "buffer_store_dwordx4 v[104:107], v251, s[44:47], s85 offen offset:3072 sc1\n"
    "ds_read_b128 v[20:23], v83 offset:4096\n"
    "v_accvgpr_read_b32 v92, a90\n"
    "v_accvgpr_read_b32 v93, a91\n"
    "v_accvgpr_read_b32 v94, a92\n"
    "v_mfma_f32_32x32x16_bf16 v[176:191], v[32:35], v[8:11], v[176:191]\n"
    "ds_read_b64_tr_b16 v[52:53], v88 offset:12800\n"
    "v_add_u32 v80, 32768, v84\n"
    "v_add_u32 v81, 32768, v85\n"
    "s_add_u32 s85, s95, 8192\n"
    "v_accvgpr_read_b32 v95, a93\n"
    "v_mfma_f32_32x32x16_bf16 v[192:207], v[36:39], v[8:11], v[192:207]\n"
    "buffer_store_dwordx4 v[108:111], v251, s[44:47], s85 offen sc1\n"
    "ds_read_b64_tr_b16 v[54:55], v89 offset:12800\n"
    "v_add_u32 v82, 32768, v86\n"
    "v_accvgpr_read_b32 v96, a94\n"
    "v_accvgpr_read_b32 v97, a95\n"
    "v_mfma_f32_32x32x16_bf16 v[208:223], v[32:35], v[12:15], v[208:223]\n"
    "ds_read_b128 v[24:27], v83 offset:8192\n"
    "v_add_u32 v88, 32768, v98\n"
    "v_add_u32 v89, 32768, v99\n"
    "s_add_u32 s85, s95, 8192\n"
    "v_cvt_pk_bf16_f32 v244, v90, v91\n"
    "v_mfma_f32_32x32x16_bf16 v[224:239], v[36:39], v[12:15], v[224:239]\n"
    "buffer_store_dwordx4 v[252:255], v251, s[44:47], s85 offen offset:1024 sc1\n"
    "ds_read_b128 v[28:31], v83 offset:12288\n"
    "v_add_u32 v83, 32768, v87\n"
    "v_cvt_pk_bf16_f32 v245, v92, v93\n"
    "v_cvt_pk_bf16_f32 v246, v94, v95\n"
    "s_waitcnt vmcnt(22) lgkmcnt(0)\n"
    "s_barrier\n"
    "v_mfma_f32_32x32x16_bf16 v[112:127], v[48:51], v[16:19], v[112:127]\n"
    "ds_read_b64_tr_b16 v[32:33], v88\n"
    "s_add_u32 m0, s87, 114688\n"
    "s_add_u32 s85, s95, 8192\n"
    "v_cvt_pk_bf16_f32 v247, v96, v97\n"
    "v_mfma_f32_32x32x16_bf16 v[128:143], v[52:55], v[16:19], v[128:143]\n"
    "buffer_store_dwordx4 v[240:243], v251, s[44:47], s85 offen offset:2048 sc1\n"
    "buffer_load_dwordx4 v72, s[40:43], s78 offen lds\n"
    "ds_read_b64_tr_b16 v[34:35], v89\n"
    "v_mfma_f32_32x32x16_bf16 v[144:159], v[48:51], v[20:23], v[144:159]\n"
    "ds_read_b128 v[0:3], v80\n"
    "s_add_u32 s85, s95, 8192\n"
    "v_mfma_f32_32x32x16_bf16 v[160:175], v[52:55], v[20:23], v[160:175]\n"
    "buffer_store_dwordx4 v[244:247], v251, s[44:47], s85 offen offset:3072 sc1\n"
    "buffer_load_dwordx4 v73, s[40:43], s78 offen offset:1024 lds\n"
    "ds_read_b128 v[4:7], v80 offset:4096\n"
    "v_mfma_f32_32x32x16_bf16 v[176:191], v[48:51], v[24:27], v[176:191]\n"
    "ds_read_b64_tr_b16 v[36:37], v88 offset:512\n"
    "v_mfma_f32_32x32x16_bf16 v[192:207], v[52:55], v[24:27], v[192:207]\n"
    "buffer_load_dwordx4 v74, s[40:43], s78 offen offset:2048 lds\n"
    "ds_read_b64_tr_b16 v[38:39], v89 offset:512\n"
    "v_mfma_f32_32x32x16_bf16 v[208:223], v[48:51], v[28:31], v[208:223]\n"
    "buffer_load_dwordx4 v75, s[40:43], s78 offen offset:3072 lds\n"
    "ds_read_b128 v[8:11], v80 offset:8192\n"
    "v_mfma_f32_32x32x16_bf16 v[224:239], v[52:55], v[28:31], v[224:239]\n"
    "ds_read_b128 v[12:15], v80 offset:12288\n"
    "s_add_u32 s78, s78, 524288\n"
    "s_waitcnt lgkmcnt(0)\n"
    "v_mfma_f32_32x32x16_bf16 v[112:127], v[32:35], v[0:3], v[112:127]\n"
    "ds_read_b64_tr_b16 v[48:49], v88 offset:4096\n"
    "s_add_u32 m0, s94, 0\n"
    "v_accvgpr_read_b32 v90, a96\n"
    "v_accvgpr_read_b32 v91, a97\n"
    "v_accvgpr_read_b32 v92, a98\n"
    "v_mfma_f32_32x32x16_bf16 v[128:143], v[36:39], v[0:3], v[128:143]\n"
    "buffer_load_dwordx4 v64, s[36:39], s77 offen lds\n"
    "ds_read_b64_tr_b16 v[50:51], v89 offset:4096\n"
    "v_accvgpr_read_b32 v93, a99\n"
    "v_accvgpr_read_b32 v94, a100\n"
    "v_accvgpr_read_b32 v95, a101\n"
    "v_mfma_f32_32x32x16_bf16 v[144:159], v[32:35], v[4:7], v[144:159]\n"
    "ds_read_b128 v[16:19], v81\n"
    "v_accvgpr_read_b32 v96, a102\n"
    "v_accvgpr_read_b32 v97, a103\n"
    "v_cvt_pk_bf16_f32 v100, v90, v91\n"
    "v_cvt_pk_bf16_f32 v101, v92, v93\n"
    "v_mfma_f32_32x32x16_bf16 v[160:175], v[36:39], v[4:7], v[160:175]\n"
    "buffer_load_dwordx4 v65, s[36:39], s77 offen offset:1024 lds\n"
    "ds_read_b128 v[20:23], v81 offset:4096\n"
    "v_cvt_pk_bf16_f32 v102, v94, v95\n"
    "v_cvt_pk_bf16_f32 v103, v96, v97\n"
    "v_accvgpr_read_b32 v90, a104\n"
    "v_mfma_f32_32x32x16_bf16 v[176:191], v[32:35], v[8:11], v[176:191]\n"
    "ds_read_b64_tr_b16 v[52:53], v88 offset:4608\n"
    "v_accvgpr_read_b32 v91, a105\n"
    "v_accvgpr_read_b32 v92, a106\n"
    "v_accvgpr_read_b32 v93, a107\n"
    "v_accvgpr_read_b32 v94, a108\n"
    "v_mfma_f32_32x32x16_bf16 v[192:207], v[36:39], v[8:11], v[192:207]\n"
    "buffer_load_dwordx4 v66, s[36:39], s77 offen offset:2048 lds\n"
    "ds_read_b64_tr_b16 v[54:55], v89 offset:4608\n"
    "v_accvgpr_read_b32 v95, a109\n"
    "v_accvgpr_read_b32 v96, a110\n"
    "v_accvgpr_read_b32 v97, a111\n"
    "v_mfma_f32_32x32x16_bf16 v[208:223], v[32:35], v[12:15], v[208:223]\n"
    "ds_read_b128 v[24:27], v81 offset:8192\n"
    "v_cvt_pk_bf16_f32 v104, v90, v91\n"
    "v_cvt_pk_bf16_f32 v105, v92, v93\n"
    "v_cvt_pk_bf16_f32 v106, v94, v95\n"
    "v_cvt_pk_bf16_f32 v107, v96, v97\n"
    "v_mfma_f32_32x32x16_bf16 v[224:239], v[36:39], v[12:15], v[224:239]\n"
    "buffer_load_dwordx4 v67, s[36:39], s77 offen offset:3072 lds\n"
    "ds_read_b128 v[28:31], v81 offset:12288\n"
    "v_accvgpr_read_b32 v90, a112\n"
    "v_accvgpr_read_b32 v91, a113\n"
    "v_accvgpr_read_b32 v92, a114\n"
    "s_waitcnt lgkmcnt(0)\n"
    "v_mfma_f32_32x32x16_bf16 v[112:127], v[48:51], v[16:19], v[112:127]\n"
    "ds_read_b64_tr_b16 v[32:33], v88 offset:8192\n"
    "s_add_u32 m0, s94, 4096\n"
    "v_accvgpr_read_b32 v93, a115\n"
    "v_accvgpr_read_b32 v94, a116\n"
    "v_accvgpr_read_b32 v95, a117\n"
    "v_mfma_f32_32x32x16_bf16 v[128:143], v[52:55], v[16:19], v[128:143]\n"
    "buffer_load_dwordx4 v68, s[36:39], s77 offen lds\n"
    "ds_read_b64_tr_b16 v[34:35], v89 offset:8192\n"
    "v_accvgpr_read_b32 v96, a118\n"
    "v_accvgpr_read_b32 v97, a119\n"
    "v_cvt_pk_bf16_f32 v108, v90, v91\n"
    "v_mfma_f32_32x32x16_bf16 v[144:159], v[48:51], v[20:23], v[144:159]\n"
    "ds_read_b128 v[0:3], v82\n"
    "v_cvt_pk_bf16_f32 v109, v92, v93\n"
    "v_cvt_pk_bf16_f32 v110, v94, v95\n"
    "v_cvt_pk_bf16_f32 v111, v96, v97\n"
    "v_accvgpr_read_b32 v90, a120\n"
    "v_mfma_f32_32x32x16_bf16 v[160:175], v[52:55], v[20:23], v[160:175]\n"
    "buffer_load_dwordx4 v69, s[36:39], s77 offen offset:1024 lds\n"
    "ds_read_b128 v[4:7], v82 offset:4096\n"
    "v_accvgpr_read_b32 v91, a121\n"
    "v_accvgpr_read_b32 v92, a122\n"
    "v_accvgpr_read_b32 v93, a123\n"
    "v_mfma_f32_32x32x16_bf16 v[176:191], v[48:51], v[24:27], v[176:191]\n"
    "ds_read_b64_tr_b16 v[36:37], v88 offset:8704\n"
    "v_accvgpr_read_b32 v94, a124\n"
    "v_accvgpr_read_b32 v95, a125\n"
    "v_accvgpr_read_b32 v96, a126\n"
    "v_accvgpr_read_b32 v97, a127\n"
    "v_mfma_f32_32x32x16_bf16 v[192:207], v[52:55], v[24:27], v[192:207]\n"
    "buffer_load_dwordx4 v70, s[36:39], s77 offen offset:2048 lds\n"
    "ds_read_b64_tr_b16 v[38:39], v89 offset:8704\n"
    "v_cvt_pk_bf16_f32 v252, v90, v91\n"
    "v_cvt_pk_bf16_f32 v253, v92, v93\n"
    "v_cvt_pk_bf16_f32 v254, v94, v95\n"
    "v_mfma_f32_32x32x16_bf16 v[208:223], v[48:51], v[28:31], v[208:223]\n"
    "ds_read_b128 v[8:11], v82 offset:8192\n"
    "v_cvt_pk_bf16_f32 v255, v96, v97\n"
    "v_accvgpr_read_b32 v90, a128\n"
    "v_accvgpr_read_b32 v91, a129\n"
    "v_accvgpr_read_b32 v92, a130\n"
    "v_mfma_f32_32x32x16_bf16 v[224:239], v[52:55], v[28:31], v[224:239]\n"
    "buffer_load_dwordx4 v71, s[36:39], s77 offen offset:3072 lds\n"
    "ds_read_b128 v[12:15], v82 offset:12288\n"
    "v_accvgpr_read_b32 v93, a131\n"
    "v_accvgpr_read_b32 v94, a132\n"
    "v_accvgpr_read_b32 v95, a133\n"
    "s_waitcnt lgkmcnt(0)\n"
    "v_mfma_f32_32x32x16_bf16 v[112:127], v[32:35], v[0:3], v[112:127]\n"
    "ds_read_b64_tr_b16 v[48:49], v88 offset:12288\n"
    "s_add_u32 s77, s77, 128\n"
    "s_add_u32 s85, s95, 12288\n"
    "v_accvgpr_read_b32 v96, a134\n"
    "v_accvgpr_read_b32 v97, a135\n"
    "v_mfma_f32_32x32x16_bf16 v[128:143], v[36:39], v[0:3], v[128:143]\n"
    "buffer_store_dwordx4 v[100:103], v251, s[44:47], s85 offen sc1\n"
    "ds_read_b64_tr_b16 v[50:51], v89 offset:12288\n"
    "v_cvt_pk_bf16_f32 v240, v90, v91\n"
    "v_cvt_pk_bf16_f32 v241, v92, v93\n"
    "v_cvt_pk_bf16_f32 v242, v94, v95\n"
    "v_mfma_f32_32x32x16_bf16 v[144:159], v[32:35], v[4:7], v[144:159]\n"
    "ds_read_b128 v[16:19], v83\n"
    "s_add_u32 s85, s95, 12288\n"
    "v_cvt_pk_bf16_f32 v243, v96, v97\n"
    "v_accvgpr_read_b32 v90, a136\n"
    "v_accvgpr_read_b32 v91, a137\n"
    "v_mfma_f32_32x32x16_bf16 v[160:175], v[36:39], v[4:7], v[160:175]\n"
    "buffer_store_dwordx4 v[104:107], v251, s[44:47], s85 offen offset:1024 sc1\n"
    "ds_read_b128 v[20:23], v83 offset:4096\n"
    "v_accvgpr_read_b32 v92, a138\n"
    "v_accvgpr_read_b32 v93, a139\n"
    "v_accvgpr_read_b32 v94, a140\n"
    "v_mfma_f32_32x32x16_bf16 v[176:191], v[32:35], v[8:11], v[176:191]\n"
    "ds_read_b64_tr_b16 v[52:53], v88 offset:12800\n"
    "v_add_u32 v80, 65536, v84\n"
    "v_add_u32 v81, 65536, v85\n"
    "s_add_u32 s85, s95, 12288\n"
    "v_accvgpr_read_b32 v95, a141\n"
    "v_mfma_f32_32x32x16_bf16 v[192:207], v[36:39], v[8:11], v[192:207]\n"
    "buffer_store_dwordx4 v[108:111], v251, s[44:47], s85 offen offset:2048 sc1\n"
    "ds_read_b64_tr_b16 v[54:55], v89 offset:12800\n"
    "v_add_u32 v82, 65536, v86\n"
    "v_accvgpr_read_b32 v96, a142\n"
    "v_accvgpr_read_b32 v97, a143\n"
    "v_mfma_f32_32x32x16_bf16 v[208:223], v[32:35], v[12:15], v[208:223]\n"
    "ds_read_b128 v[24:27], v83 offset:8192\n"
    "v_add_u32 v88, 0, v98\n"
    "v_add_u32 v89, 0, v99\n"
    "s_add_u32 s85, s95, 12288\n"
    "v_cvt_pk_bf16_f32 v244, v90, v91\n"
    "v_mfma_f32_32x32x16_bf16 v[224:239], v[36:39], v[12:15], v[224:239]\n"
    "buffer_store_dwordx4 v[252:255], v251, s[44:47], s85 offen offset:3072 sc1\n"
    "ds_read_b128 v[28:31], v83 offset:12288\n"
    "v_add_u32 v83, 65536, v87\n"
    "v_cvt_pk_bf16_f32 v245, v92, v93\n"
    "v_cvt_pk_bf16_f32 v246, v94, v95\n"
    "s_waitcnt vmcnt(22) lgkmcnt(0)\n"
    "s_barrier\n"
    "v_mfma_f32_32x32x16_bf16 v[112:127], v[48:51], v[16:19], v[112:127]\n"
    "ds_read_b64_tr_b16 v[32:33], v88\n"
    "s_add_u32 m0, s87, 131072\n"
    "s_add_u32 s85, s95, 16384\n"
    "v_cvt_pk_bf16_f32 v247, v96, v97\n"
    "v_mfma_f32_32x32x16_bf16 v[128:143], v[52:55], v[16:19], v[128:143]\n"
    "buffer_store_dwordx4 v[240:243], v251, s[44:47], s85 offen sc1\n"
    "buffer_load_dwordx4 v72, s[40:43], s78 offen lds\n"
    "ds_read_b64_tr_b16 v[34:35], v89\n"
    "v_mfma_f32_32x32x16_bf16 v[144:159], v[48:51], v[20:23], v[144:159]\n"
    "ds_read_b128 v[0:3], v80\n"
    "s_add_u32 s85, s95, 16384\n"
    "v_mfma_f32_32x32x16_bf16 v[160:175], v[52:55], v[20:23], v[160:175]\n"
    "buffer_store_dwordx4 v[244:247], v251, s[44:47], s85 offen offset:1024 sc1\n"
    "buffer_load_dwordx4 v73, s[40:43], s78 offen offset:1024 lds\n"
    "ds_read_b128 v[4:7], v80 offset:4096\n"
    "v_mfma_f32_32x32x16_bf16 v[176:191], v[48:51], v[24:27], v[176:191]\n"
    "ds_read_b64_tr_b16 v[36:37], v88 offset:512\n"
    "v_mfma_f32_32x32x16_bf16 v[192:207], v[52:55], v[24:27], v[192:207]\n"
    "buffer_load_dwordx4 v74, s[40:43], s78 offen offset:2048 lds\n"
    "ds_read_b64_tr_b16 v[38:39], v89 offset:512\n"
    "v_mfma_f32_32x32x16_bf16 v[208:223], v[48:51], v[28:31], v[208:223]\n"
    "buffer_load_dwordx4 v75, s[40:43], s78 offen offset:3072 lds\n"
    "ds_read_b128 v[8:11], v80 offset:8192\n"
    "v_mfma_f32_32x32x16_bf16 v[224:239], v[52:55], v[28:31], v[224:239]\n"
    "ds_read_b128 v[12:15], v80 offset:12288\n"
    "s_add_u32 s78, s78, 524288\n"
    "s_waitcnt lgkmcnt(0)\n"
    "v_mfma_f32_32x32x16_bf16 v[112:127], v[32:35], v[0:3], v[112:127]\n"
    "ds_read_b64_tr_b16 v[48:49], v88 offset:4096\n"
    "s_add_u32 m0, s94, 32768\n"
    "v_accvgpr_read_b32 v90, a144\n"
    "v_accvgpr_read_b32 v91, a145\n"
    "v_accvgpr_read_b32 v92, a146\n"
    "v_mfma_f32_32x32x16_bf16 v[128:143], v[36:39], v[0:3], v[128:143]\n"
    "buffer_load_dwordx4 v64, s[36:39], s77 offen lds\n"
    "ds_read_b64_tr_b16 v[50:51], v89 offset:4096\n"
    "v_accvgpr_read_b32 v93, a147\n"
    "v_accvgpr_read_b32 v94, a148\n"
    "v_accvgpr_read_b32 v95, a149\n"
    "v_mfma_f32_32x32x16_bf16 v[144:159], v[32:35], v[4:7], v[144:159]\n"
    "ds_read_b128 v[16:19], v81\n"
    "v_accvgpr_read_b32 v96, a150\n"
    "v_accvgpr_read_b32 v97, a151\n"
    "v_cvt_pk_bf16_f32 v100, v90, v91\n"
    "v_cvt_pk_bf16_f32 v101, v92, v93\n"
    "v_mfma_f32_32x32x16_bf16 v[160:175], v[36:39], v[4:7], v[160:175]\n"
    "buffer_load_dwordx4 v65, s[36:39], s77 offen offset:1024 lds\n"
    "ds_read_b128 v[20:23], v81 offset:4096\n"
    "v_cvt_pk_bf16_f32 v102, v94, v95\n"
    "v_cvt_pk_bf16_f32 v103, v96, v97\n"
    "v_accvgpr_read_b32 v90, a152\n"
    "v_mfma_f32_32x32x16_bf16 v[176:191], v[32:35], v[8:11], v[176:191]\n"
    "ds_read_b64_tr_b16 v[52:53], v88 offset:4608\n"
    "v_accvgpr_read_b32 v91, a153\n"
    "v_accvgpr_read_b32 v92, a154\n"
    "v_accvgpr_read_b32 v93, a155\n"
    "v_accvgpr_read_b32 v94, a156\n"
    "v_mfma_f32_32x32x16_bf16 v[192:207], v[36:39], v[8:11], v[192:207]\n"
    "buffer_load_dwordx4 v66, s[36:39], s77 offen offset:2048 lds\n"
    "ds_read_b64_tr_b16 v[54:55], v89 offset:4608\n"
    "v_accvgpr_read_b32 v95, a157\n"
    "v_accvgpr_read_b32 v96, a158\n"
    "v_accvgpr_read_b32 v97, a159\n"
    "v_mfma_f32_32x32x16_bf16 v[208:223], v[32:35], v[12:15], v[208:223]\n"
    "ds_read_b128 v[24:27], v81 offset:8192\n"
    "v_cvt_pk_bf16_f32 v104, v90, v91\n"
    "v_cvt_pk_bf16_f32 v105, v92, v93\n"
    "v_cvt_pk_bf16_f32 v106, v94, v95\n"
    "v_cvt_pk_bf16_f32 v107, v96, v97\n"
    "v_mfma_f32_32x32x16_bf16 v[224:239], v[36:39], v[12:15], v[224:239]\n"
    "buffer_load_dwordx4 v67, s[36:39], s77 offen offset:3072 lds\n"
    "ds_read_b128 v[28:31], v81 offset:12288\n"
    "v_accvgpr_read_b32 v90, a160\n"
    "v_accvgpr_read_b32 v91, a161\n"
    "v_accvgpr_read_b32 v92, a162\n"
    "s_waitcnt lgkmcnt(0)\n"
    "v_mfma_f32_32x32x16_bf16 v[112:127], v[48:51], v[16:19], v[112:127]\n"
    "ds_read_b64_tr_b16 v[32:33], v88 offset:8192\n"
    "s_add_u32 m0, s94, 36864\n"
    "v_accvgpr_read_b32 v93, a163\n"
    "v_accvgpr_read_b32 v94, a164\n"
    "v_accvgpr_read_b32 v95, a165\n"
    "v_mfma_f32_32x32x16_bf16 v[128:143], v[52:55], v[16:19], v[128:143]\n"
    "buffer_load_dwordx4 v68, s[36:39], s77 offen lds\n"
    "ds_read_b64_tr_b16 v[34:35], v89 offset:8192\n"
    "v_accvgpr_read_b32 v96, a166\n"
    "v_accvgpr_read_b32 v97, a167\n"
    "v_cvt_pk_bf16_f32 v108, v90, v91\n"
    "v_mfma_f32_32x32x16_bf16 v[144:159], v[48:51], v[20:23], v[144:159]\n"
    "ds_read_b128 v[0:3], v82\n"
    "v_cvt_pk_bf16_f32 v109, v92, v93\n"
    "v_cvt_pk_bf16_f32 v110, v94, v95\n"
    "v_cvt_pk_bf16_f32 v111, v96, v97\n"
    "v_accvgpr_read_b32 v90, a168\n"
    "v_mfma_f32_32x32x16_bf16 v[160:175], v[52:55], v[20:23], v[160:175]\n"
    "buffer_load_dwordx4 v69, s[36:39], s77 offen offset:1024 lds\n"
    "ds_read_b128 v[4:7], v82 offset:4096\n"
    "v_accvgpr_read_b32 v91, a169\n"
    "v_accvgpr_read_b32 v92, a170\n"
    "v_accvgpr_read_b32 v93, a171\n"
    "v_mfma_f32_32x32x16_bf16 v[176:191], v[48:51], v[24:27], v[176:191]\n"
    "ds_read_b64_tr_b16 v[36:37], v88 offset:8704\n"
    "v_accvgpr_read_b32 v94, a172\n"
    "v_accvgpr_read_b32 v95, a173\n"
    "v_accvgpr_read_b32 v96, a174\n"
    "v_accvgpr_read_b32 v97, a175\n"
    "v_mfma_f32_32x32x16_bf16 v[192:207], v[52:55], v[24:27], v[192:207]\n"
    "buffer_load_dwordx4 v70, s[36:39], s77 offen offset:2048 lds\n"
    "ds_read_b64_tr_b16 v[38:39], v89 offset:8704\n"
    "v_cvt_pk_bf16_f32 v252, v90, v91\n"
    "v_cvt_pk_bf16_f32 v253, v92, v93\n"
    "v_cvt_pk_bf16_f32 v254, v94, v95\n"
    "v_mfma_f32_32x32x16_bf16 v[208:223], v[48:51], v[28:31], v[208:223]\n"
    "ds_read_b128 v[8:11], v82 offset:8192\n"
    "v_cvt_pk_bf16_f32 v255, v96, v97\n"
    "v_accvgpr_read_b32 v90, a176\n"
    "v_accvgpr_read_b32 v91, a177\n"
    "v_accvgpr_read_b32 v92, a178\n"
    "v_mfma_f32_32x32x16_bf16 v[224:239], v[52:55], v[28:31], v[224:239]\n"
    "buffer_load_dwordx4 v71, s[36:39], s77 offen offset:3072 lds\n"
    "ds_read_b128 v[12:15], v82 offset:12288\n"
    "v_accvgpr_read_b32 v93, a179\n"
    "v_accvgpr_read_b32 v94, a180\n"
    "v_accvgpr_read_b32 v95, a181\n"
    "s_waitcnt lgkmcnt(0)\n"
    "v_mfma_f32_32x32x16_bf16 v[112:127], v[32:35], v[0:3], v[112:127]\n"
    "ds_read_b64_tr_b16 v[48:49], v88 offset:12288\n"
    "s_add_u32 s77, s77, 128\n"
    "s_add_u32 s85, s95, 16384\n"
    "v_accvgpr_read_b32 v96, a182\n"
    "v_accvgpr_read_b32 v97, a183\n"
    "v_mfma_f32_32x32x16_bf16 v[128:143], v[36:39], v[0:3], v[128:143]\n"
    "buffer_store_dwordx4 v[100:103], v251, s[44:47], s85 offen offset:2048 sc1\n"
    "ds_read_b64_tr_b16 v[50:51], v89 offset:12288\n"
    "v_cvt_pk_bf16_f32 v240, v90, v91\n"
    "v_cvt_pk_bf16_f32 v241, v92, v93\n"
    "v_cvt_pk_bf16_f32 v242, v94, v95\n"
    "v_mfma_f32_32x32x16_bf16 v[144:159], v[32:35], v[4:7], v[144:159]\n"
    "ds_read_b128 v[16:19], v83\n"
    "s_add_u32 s85, s95, 16384\n"
    "v_cvt_pk_bf16_f32 v243, v96, v97\n"
    "v_accvgpr_read_b32 v90, a184\n"
    "v_accvgpr_read_b32 v91, a185\n"
    "v_mfma_f32_32x32x16_bf16 v[160:175], v[36:39], v[4:7], v[160:175]\n"
    "buffer_store_dwordx4 v[104:107], v251, s[44:47], s85 offen offset:3072 sc1\n"
    "ds_read_b128 v[20:23], v83 offset:4096\n"
    "v_accvgpr_read_b32 v92, a186\n"
    "v_accvgpr_read_b32 v93, a187\n"
    "v_accvgpr_read_b32 v94, a188\n"
    "v_mfma_f32_32x32x16_bf16 v[176:191], v[32:35], v[8:11], v[176:191]\n"
    "ds_read_b64_tr_b16 v[52:53], v88 offset:12800\n"
    "v_add_u32 v80, 0, v84\n"
    "v_add_u32 v81, 0, v85\n"
    "s_add_u32 s85, s95, 20480\n"
    "v_accvgpr_read_b32 v95, a189\n"
    "v_mfma_f32_32x32x16_bf16 v[192:207], v[36:39], v[8:11], v[192:207]\n"
    "buffer_store_dwordx4 v[108:111], v251, s[44:47], s85 offen sc1\n"
    "ds_read_b64_tr_b16 v[54:55], v89 offset:12800\n"
    "v_add_u32 v82, 0, v86\n"
    "v_accvgpr_read_b32 v96, a190\n"
    "v_accvgpr_read_b32 v97, a191\n"
    "v_mfma_f32_32x32x16_bf16 v[208:223], v[32:35], v[12:15], v[208:223]\n"
    "ds_read_b128 v[24:27], v83 offset:8192\n"
    "v_add_u32 v88, 16384, v98\n"
    "v_add_u32 v89, 16384, v99\n"
    "s_add_u32 s85, s95, 20480\n"
    "v_cvt_pk_bf16_f32 v244, v90, v91\n"
    "v_mfma_f32_32x32x16_bf16 v[224:239], v[36:39], v[12:15], v[224:239]\n"
    "buffer_store_dwordx4 v[252:255], v251, s[44:47], s85 offen offset:1024 sc1\n"
    "ds_read_b128 v[28:31], v83 offset:12288\n"
    "v_add_u32 v83, 0, v87\n"
    "v_cvt_pk_bf16_f32 v245, v92, v93\n"
    "v_cvt_pk_bf16_f32 v246, v94, v95\n"
    "s_waitcnt vmcnt(22) lgkmcnt(0)\n"
    "s_barrier\n"
    "v_mfma_f32_32x32x16_bf16 v[112:127], v[48:51], v[16:19], v[112:127]\n"
    "ds_read_b64_tr_b16 v[32:33], v88\n"
    "s_add_u32 m0, s87, 98304\n"
    "s_add_u32 s85, s95, 20480\n"
    "v_cvt_pk_bf16_f32 v247, v96, v97\n"
    "v_mfma_f32_32x32x16_bf16 v[128:143], v[52:55], v[16:19], v[128:143]\n"
    "buffer_store_dwordx4 v[240:243], v251, s[44:47], s85 offen offset:2048 sc1\n"
    "buffer_load_dwordx4 v72, s[40:43], s78 offen lds\n"
    "ds_read_b64_tr_b16 v[34:35], v89\n"
    "v_mfma_f32_32x32x16_bf16 v[144:159], v[48:51], v[20:23], v[144:159]\n"
    "ds_read_b128 v[0:3], v80\n"
    "s_add_u32 s85, s95, 20480\n"
    "v_mfma_f32_32x32x16_bf16 v[160:175], v[52:55], v[20:23], v[160:175]\n"
    "buffer_store_dwordx4 v[244:247], v251, s[44:47], s85 offen offset:3072 sc1\n"
    "buffer_load_dwordx4 v73, s[40:43], s78 offen offset:1024 lds\n"
    "ds_read_b128 v[4:7], v80 offset:4096\n"
    "v_mfma_f32_32x32x16_bf16 v[176:191], v[48:51], v[24:27], v[176:191]\n"
    "ds_read_b64_tr_b16 v[36:37], v88 offset:512\n"
    "v_mfma_f32_32x32x16_bf16 v[192:207], v[52:55], v[24:27], v[192:207]\n"
    "buffer_load_dwordx4 v74, s[40:43], s78 offen offset:2048 lds\n"
    "ds_read_b64_tr_b16 v[38:39], v89 offset:512\n"
    "v_mfma_f32_32x32x16_bf16 v[208:223], v[48:51], v[28:31], v[208:223]\n"
    "buffer_load_dwordx4 v75, s[40:43], s78 offen offset:3072 lds\n"
    "ds_read_b128 v[8:11], v80 offset:8192\n"
    "v_mfma_f32_32x32x16_bf16 v[224:239], v[52:55], v[28:31], v[224:239]\n"
    "ds_read_b128 v[12:15], v80 offset:12288\n"
    "s_add_u32 s78, s78, 524288\n"
    "s_waitcnt lgkmcnt(0)\n"
    "v_mfma_f32_32x32x16_bf16 v[112:127], v[32:35], v[0:3], v[112:127]\n"
    "ds_read_b64_tr_b16 v[48:49], v88 offset:4096\n"
    "s_add_u32 m0, s94, 65536\n"
    "v_accvgpr_read_b32 v90, a192\n"
    "v_accvgpr_read_b32 v91, a193\n"
    "v_accvgpr_read_b32 v92, a194\n"
    "v_mfma_f32_32x32x16_bf16 v[128:143], v[36:39], v[0:3], v[128:143]\n"
    "buffer_load_dwordx4 v64, s[36:39], s77 offen lds\n"
    "ds_read_b64_tr_b16 v[50:51], v89 offset:4096\n"
    "v_accvgpr_read_b32 v93, a195\n"
    "v_accvgpr_read_b32 v94, a196\n"
    "v_accvgpr_read_b32 v95, a197\n"
    "v_mfma_f32_32x32x16_bf16 v[144:159], v[32:35], v[4:7], v[144:159]\n"
    "ds_read_b128 v[16:19], v81\n"
    "v_accvgpr_read_b32 v96, a198\n"
    "v_accvgpr_read_b32 v97, a199\n"
    "v_cvt_pk_bf16_f32 v100, v90, v91\n"
    "v_cvt_pk_bf16_f32 v101, v92, v93\n"
    "v_mfma_f32_32x32x16_bf16 v[160:175], v[36:39], v[4:7], v[160:175]\n"
    "buffer_load_dwordx4 v65, s[36:39], s77 offen offset:1024 lds\n"
    "ds_read_b128 v[20:23], v81 offset:4096\n"
    "v_cvt_pk_bf16_f32 v102, v94, v95\n"
    "v_cvt_pk_bf16_f32 v103, v96, v97\n"
    "v_accvgpr_read_b32 v90, a200\n"
    "v_mfma_f32_32x32x16_bf16 v[176:191], v[32:35], v[8:11], v[176:191]\n"
    "ds_read_b64_tr_b16 v[52:53], v88 offset:4608\n"
    "v_accvgpr_read_b32 v91, a201\n"
    "v_accvgpr_read_b32 v92, a202\n"
    "v_accvgpr_read_b32 v93, a203\n"
    "v_accvgpr_read_b32 v94, a204\n"
    "v_mfma_f32_32x32x16_bf16 v[192:207], v[36:39], v[8:11], v[192:207]\n"
    "buffer_load_dwordx4 v66, s[36:39], s77 offen offset:2048 lds\n"
    "ds_read_b64_tr_b16 v[54:55], v89 offset:4608\n"
    "v_accvgpr_read_b32 v95, a205\n"
    "v_accvgpr_read_b32 v96, a206\n"
    "v_accvgpr_read_b32 v97, a207\n"
    "v_mfma_f32_32x32x16_bf16 v[208:223], v[32:35], v[12:15], v[208:223]\n"
    "ds_read_b128 v[24:27], v81 offset:8192\n"
    "v_cvt_pk_bf16_f32 v104, v90, v91\n"
    "v_cvt_pk_bf16_f32 v105, v92, v93\n"
    "v_cvt_pk_bf16_f32 v106, v94, v95\n"
    "v_cvt_pk_bf16_f32 v107, v96, v97\n"
    "v_mfma_f32_32x32x16_bf16 v[224:239], v[36:39], v[12:15], v[224:239]\n"
    "buffer_load_dwordx4 v67, s[36:39], s77 offen offset:3072 lds\n"
    "ds_read_b128 v[28:31], v81 offset:12288\n"
    "v_accvgpr_read_b32 v90, a208\n"
    "v_accvgpr_read_b32 v91, a209\n"
    "v_accvgpr_read_b32 v92, a210\n"
    "s_waitcnt lgkmcnt(0)\n"
    "v_mfma_f32_32x32x16_bf16 v[112:127], v[48:51], v[16:19], v[112:127]\n"
    "ds_read_b64_tr_b16 v[32:33], v88 offset:8192\n"
    "s_add_u32 m0, s94, 69632\n"
    "v_accvgpr_read_b32 v93, a211\n"
    "v_accvgpr_read_b32 v94, a212\n"
    "v_accvgpr_read_b32 v95, a213\n"
    "v_mfma_f32_32x32x16_bf16 v[128:143], v[52:55], v[16:19], v[128:143]\n"
    "buffer_load_dwordx4 v68, s[36:39], s77 offen lds\n"
    "ds_read_b64_tr_b16 v[34:35], v89 offset:8192\n"
    "v_accvgpr_read_b32 v96, a214\n"
    "v_accvgpr_read_b32 v97, a215\n"
    "v_cvt_pk_bf16_f32 v108, v90, v91\n"
    "v_mfma_f32_32x32x16_bf16 v[144:159], v[48:51], v[20:23], v[144:159]\n"
    "ds_read_b128 v[0:3], v82\n"
    "v_cvt_pk_bf16_f32 v109, v92, v93\n"
    "v_cvt_pk_bf16_f32 v110, v94, v95\n"
    "v_cvt_pk_bf16_f32 v111, v96, v97\n"
    "v_accvgpr_read_b32 v90, a216\n"
    "v_mfma_f32_32x32x16_bf16 v[160:175], v[52:55], v[20:23], v[160:175]\n"
    "buffer_load_dwordx4 v69, s[36:39], s77 offen offset:1024 lds\n"
    "ds_read_b128 v[4:7], v82 offset:4096\n"
    "v_accvgpr_read_b32 v91, a217\n"
    "v_accvgpr_read_b32 v92, a218\n"
    "v_accvgpr_read_b32 v93, a219\n"
    "v_mfma_f32_32x32x16_bf16 v[176:191], v[48:51], v[24:27], v[176:191]\n"
    "ds_read_b64_tr_b16 v[36:37], v88 offset:8704\n"
    "v_accvgpr_read_b32 v94, a220\n"
    "v_accvgpr_read_b32 v95, a221\n"
    "v_accvgpr_read_b32 v96, a222\n"
    "v_accvgpr_read_b32 v97, a223\n"
    "v_mfma_f32_32x32x16_bf16 v[192:207], v[52:55], v[24:27], v[192:207]\n"
    "buffer_load_dwordx4 v70, s[36:39], s77 offen offset:2048 lds\n"
    "ds_read_b64_tr_b16 v[38:39], v89 offset:8704\n"
    "v_cvt_pk_bf16_f32 v252, v90, v91\n"
    "v_cvt_pk_bf16_f32 v253, v92, v93\n"
    "v_cvt_pk_bf16_f32 v254, v94, v95\n"
    "v_mfma_f32_32x32x16_bf16 v[208:223], v[48:51], v[28:31], v[208:223]\n"
    "ds_read_b128 v[8:11], v82 offset:8192\n"
    "v_cvt_pk_bf16_f32 v255, v96, v97\n"
    "v_accvgpr_read_b32 v90, a224\n"
    "v_accvgpr_read_b32 v91, a225\n"
    "v_accvgpr_read_b32 v92, a226\n"
    "v_mfma_f32_32x32x16_bf16 v[224:239], v[52:55], v[28:31], v[224:239]\n"
    "buffer_load_dwordx4 v71, s[36:39], s77 offen offset:3072 lds\n"
    "ds_read_b128 v[12:15], v82 offset:12288\n"
    "v_accvgpr_read_b32 v93, a227\n"
    "v_accvgpr_read_b32 v94, a228\n"
    "v_accvgpr_read_b32 v95, a229\n"
    "s_waitcnt lgkmcnt(0)\n"
    "v_mfma_f32_32x32x16_bf16 v[112:127], v[32:35], v[0:3], v[112:127]\n"
    "ds_read_b64_tr_b16 v[48:49], v88 offset:12288\n"
    "s_add_u32 s77, s77, 128\n"
    "s_add_u32 s85, s95, 24576\n"
    "v_accvgpr_read_b32 v96, a230\n"
    "v_accvgpr_read_b32 v97, a231\n"
    "v_mfma_f32_32x32x16_bf16 v[128:143], v[36:39], v[0:3], v[128:143]\n"
    "buffer_store_dwordx4 v[100:103], v251, s[44:47], s85 offen sc1\n"
    "ds_read_b64_tr_b16 v[50:51], v89 offset:12288\n"
    "v_cvt_pk_bf16_f32 v240, v90, v91\n"
    "v_cvt_pk_bf16_f32 v241, v92, v93\n"
    "v_cvt_pk_bf16_f32 v242, v94, v95\n"
    "v_mfma_f32_32x32x16_bf16 v[144:159], v[32:35], v[4:7], v[144:159]\n"
    "ds_read_b128 v[16:19], v83\n"
    "s_add_u32 s85, s95, 24576\n"
    "v_cvt_pk_bf16_f32 v243, v96, v97\n"
    "v_accvgpr_read_b32 v90, a232\n"
    "v_accvgpr_read_b32 v91, a233\n"
    "v_mfma_f32_32x32x16_bf16 v[160:175], v[36:39], v[4:7], v[160:175]\n"
    "buffer_store_dwordx4 v[104:107], v251, s[44:47], s85 offen offset:1024 sc1\n"
    "ds_read_b128 v[20:23], v83 offset:4096\n"
    "v_accvgpr_read_b32 v92, a234\n"
    "v_accvgpr_read_b32 v93, a235\n"
    "v_accvgpr_read_b32 v94, a236\n"
    "v_mfma_f32_32x32x16_bf16 v[176:191], v[32:35], v[8:11], v[176:191]\n"
    "ds_read_b64_tr_b16 v[52:53], v88 offset:12800\n"
    "v_add_u32 v80, 32768, v84\n"
    "v_add_u32 v81, 32768, v85\n"
    "s_add_u32 s85, s95, 24576\n"
    "v_accvgpr_read_b32 v95, a237\n"
    "v_mfma_f32_32x32x16_bf16 v[192:207], v[36:39], v[8:11], v[192:207]\n"
    "buffer_store_dwordx4 v[108:111], v251, s[44:47], s85 offen offset:2048 sc1\n"
    "ds_read_b64_tr_b16 v[54:55], v89 offset:12800\n"
    "v_add_u32 v82, 32768, v86\n"
    "v_accvgpr_read_b32 v96, a238\n"
    "v_accvgpr_read_b32 v97, a239\n"
    "v_mfma_f32_32x32x16_bf16 v[208:223], v[32:35], v[12:15], v[208:223]\n"
    "ds_read_b128 v[24:27], v83 offset:8192\n"
    "v_add_u32 v88, 32768, v98\n"
    "v_add_u32 v89, 32768, v99\n"
    "s_add_u32 s85, s95, 24576\n"
    "v_cvt_pk_bf16_f32 v244, v90, v91\n"
    "v_mfma_f32_32x32x16_bf16 v[224:239], v[36:39], v[12:15], v[224:239]\n"
    "buffer_store_dwordx4 v[252:255], v251, s[44:47], s85 offen offset:3072 sc1\n"
    "ds_read_b128 v[28:31], v83 offset:12288\n"
    "v_add_u32 v83, 32768, v87\n"
    "v_cvt_pk_bf16_f32 v245, v92, v93\n"
    "v_cvt_pk_bf16_f32 v246, v94, v95\n"
    "s_waitcnt vmcnt(22) lgkmcnt(0)\n"
    "s_barrier\n"
    "v_mfma_f32_32x32x16_bf16 v[112:127], v[48:51], v[16:19], v[112:127]\n"
    "ds_read_b64_tr_b16 v[32:33], v88\n"
    "s_add_u32 m0, s87, 114688\n"
    "s_add_u32 s85, s95, 28672\n"
    "v_cvt_pk_bf16_f32 v247, v96, v97\n"
    "v_mfma_f32_32x32x16_bf16 v[128:143], v[52:55], v[16:19], v[128:143]\n"
    "buffer_store_dwordx4 v[240:243], v251, s[44:47], s85 offen sc1\n"
    "buffer_load_dwordx4 v72, s[40:43], s78 offen lds\n"
    "ds_read_b64_tr_b16 v[34:35], v89\n"
    "v_mfma_f32_32x32x16_bf16 v[144:159], v[48:51], v[20:23], v[144:159]\n"
    "ds_read_b128 v[0:3], v80\n"
    "s_add_u32 s85, s95, 28672\n"
    "v_mfma_f32_32x32x16_bf16 v[160:175], v[52:55], v[20:23], v[160:175]\n"
    "buffer_store_dwordx4 v[244:247], v251, s[44:47], s85 offen offset:1024 sc1\n"
    "buffer_load_dwordx4 v73, s[40:43], s78 offen offset:1024 lds\n"
    "ds_read_b128 v[4:7], v80 offset:4096\n"
    "v_mfma_f32_32x32x16_bf16 v[176:191], v[48:51], v[24:27], v[176:191]\n"
    "ds_read_b64_tr_b16 v[36:37], v88 offset:512\n"
    "v_mfma_f32_32x32x16_bf16 v[192:207], v[52:55], v[24:27], v[192:207]\n"
    "buffer_load_dwordx4 v74, s[40:43], s78 offen offset:2048 lds\n"
    "ds_read_b64_tr_b16 v[38:39], v89 offset:512\n"
    "v_mfma_f32_32x32x16_bf16 v[208:223], v[48:51], v[28:31], v[208:223]\n"
    "buffer_load_dwordx4 v75, s[40:43], s78 offen offset:3072 lds\n"
    "ds_read_b128 v[8:11], v80 offset:8192\n"
    "v_mfma_f32_32x32x16_bf16 v[224:239], v[52:55], v[28:31], v[224:239]\n"
    "ds_read_b128 v[12:15], v80 offset:12288\n"
    "s_add_u32 s78, s78, 524288\n"
    "s_waitcnt lgkmcnt(0)\n"
    "v_mfma_f32_32x32x16_bf16 v[112:127], v[32:35], v[0:3], v[112:127]\n"
    "ds_read_b64_tr_b16 v[48:49], v88 offset:4096\n"
    "s_add_u32 m0, s94, 0\n"
    "s_lshl_b32 s82, s61, 7\n"
    "s_add_u32 s56, s74, 85000192\n"
    "s_addc_u32 s57, s75, 0\n"
    "s_add_u32 s56, s56, s82\n"
    "s_addc_u32 s57, s57, 0\n"
    "s_and_b32 s57, s57, 65535\n"
    "s_mov_b32 s58, 64\n"
    "s_mov_b32 s59, 131072\n"
    "v_mfma_f32_32x32x16_bf16 v[128:143], v[36:39], v[0:3], v[128:143]\n"
    "buffer_load_dwordx4 v64, s[36:39], s77 offen lds\n"
    "ds_read_b64_tr_b16 v[50:51], v89 offset:4096\n"
    "v_accvgpr_read_b32 v90, a240\n"
    "v_accvgpr_read_b32 v91, a241\n"
    "v_accvgpr_read_b32 v92, a242\n"
    "v_mfma_f32_32x32x16_bf16 v[144:159], v[32:35], v[4:7], v[144:159]\n"
    "ds_read_b128 v[16:19], v81\n"
    "v_accvgpr_read_b32 v93, a243\n"
    "v_accvgpr_read_b32 v94, a244\n"
    "v_accvgpr_read_b32 v95, a245\n"
    "v_accvgpr_read_b32 v96, a246\n"
    "v_mfma_f32_32x32x16_bf16 v[160:175], v[36:39], v[4:7], v[160:175]\n"
    "buffer_load_dwordx4 v65, s[36:39], s77 offen offset:1024 lds\n"
    "ds_read_b128 v[20:23], v81 offset:4096\n"
    "v_accvgpr_read_b32 v97, a247\n"
    "v_cvt_pk_bf16_f32 v100, v90, v91\n"
    "v_cvt_pk_bf16_f32 v101, v92, v93\n"
    "v_mfma_f32_32x32x16_bf16 v[176:191], v[32:35], v[8:11], v[176:191]\n"
    "ds_read_b64_tr_b16 v[52:53], v88 offset:4608\n"
    "v_cvt_pk_bf16_f32 v102, v94, v95\n"
    "v_cvt_pk_bf16_f32 v103, v96, v97\n"
    "v_accvgpr_read_b32 v90, a248\n"
    "v_accvgpr_read_b32 v91, a249\n"
    "v_mfma_f32_32x32x16_bf16 v[192:207], v[36:39], v[8:11], v[192:207]\n"
    "buffer_load_dwordx4 v66, s[36:39], s77 offen offset:2048 lds\n"
    "ds_read_b64_tr_b16 v[54:55], v89 offset:4608\n"
    "v_accvgpr_read_b32 v92, a250\n"
    "v_accvgpr_read_b32 v93, a251\n"
    "v_accvgpr_read_b32 v94, a252\n"
    "v_mfma_f32_32x32x16_bf16 v[208:223], v[32:35], v[12:15], v[208:223]\n"
    "ds_read_b128 v[24:27], v81 offset:8192\n"
    "v_accvgpr_read_b32 v95, a253\n"
    "v_accvgpr_read_b32 v96, a254\n"
    "v_accvgpr_read_b32 v97, a255\n"
    "v_cvt_pk_bf16_f32 v104, v90, v91\n"
    "v_mfma_f32_32x32x16_bf16 v[224:239], v[36:39], v[12:15], v[224:239]\n"
    "buffer_load_dwordx4 v67, s[36:39], s77 offen offset:3072 lds\n"
    "ds_read_b128 v[28:31], v81 offset:12288\n"
    "v_cvt_pk_bf16_f32 v105, v92, v93\n"
    "v_cvt_pk_bf16_f32 v106, v94, v95\n"
    "v_cvt_pk_bf16_f32 v107, v96, v97\n"
    "s_waitcnt lgkmcnt(0)\n"
    "v_mfma_f32_32x32x16_bf16 v[112:127], v[48:51], v[16:19], v[112:127]\n"
    "ds_read_b64_tr_b16 v[32:33], v88 offset:8192\n"
    "s_add_u32 m0, s94, 4096\n"
    "v_mfma_f32_32x32x16_bf16 v[128:143], v[52:55], v[16:19], v[128:143]\n"
    "buffer_load_dwordx4 v68, s[36:39], s77 offen lds\n"
    "ds_read_b64_tr_b16 v[34:35], v89 offset:8192\n"
    "v_mfma_f32_32x32x16_bf16 v[144:159], v[48:51], v[20:23], v[144:159]\n"
    "ds_read_b128 v[0:3], v82\n"
    "v_mfma_f32_32x32x16_bf16 v[160:175], v[52:55], v[20:23], v[160:175]\n"
    "buffer_load_dwordx4 v69, s[36:39], s77 offen offset:1024 lds\n"
    "ds_read_b128 v[4:7], v82 offset:4096\n"
    "v_mfma_f32_32x32x16_bf16 v[176:191], v[48:51], v[24:27], v[176:191]\n"
    "ds_read_b64_tr_b16 v[36:37], v88 offset:8704\n"
    "v_mfma_f32_32x32x16_bf16 v[192:207], v[52:55], v[24:27], v[192:207]\n"
    "buffer_load_dwordx4 v70, s[36:39], s77 offen offset:2048 lds\n"
    "ds_read_b64_tr_b16 v[38:39], v89 offset:8704\n"
    "v_mfma_f32_32x32x16_bf16 v[208:223], v[48:51], v[28:31], v[208:223]\n"
    "ds_read_b128 v[8:11], v82 offset:8192\n"
    "v_mfma_f32_32x32x16_bf16 v[224:239], v[52:55], v[28:31], v[224:239]\n"
    "buffer_load_dwordx4 v71, s[36:39], s77 offen offset:3072 lds\n"
    "ds_read_b128 v[12:15], v82 offset:12288\n"
    "s_waitcnt lgkmcnt(0)\n"
    "v_mfma_f32_32x32x16_bf16 v[112:127], v[32:35], v[0:3], v[112:127]\n"
    "ds_read_b64_tr_b16 v[48:49], v88 offset:12288\n"
    "s_add_u32 s77, s77, 128\n"
    "s_add_u32 s85, s95, 28672\n"
    "v_mfma_f32_32x32x16_bf16 v[128:143], v[36:39], v[0:3], v[128:143]\n"
    "buffer_store_dwordx4 v[100:103], v251, s[44:47], s85 offen offset:2048 sc1\n"
    "ds_read_b64_tr_b16 v[50:51], v89 offset:12288\n"
    "v_mfma_f32_32x32x16_bf16 v[144:159], v[32:35], v[4:7], v[144:159]\n"
    "ds_read_b128 v[16:19], v83\n"
    "s_add_u32 s85, s95, 28672\n"
    "v_mfma_f32_32x32x16_bf16 v[160:175], v[36:39], v[4:7], v[160:175]\n"
    "buffer_store_dwordx4 v[104:107], v251, s[44:47], s85 offen offset:3072 sc1\n"
    "ds_read_b128 v[20:23], v83 offset:4096\n"
    "v_mfma_f32_32x32x16_bf16 v[176:191], v[32:35], v[8:11], v[176:191]\n"
    "ds_read_b64_tr_b16 v[52:53], v88 offset:12800\n"
    "v_add_u32 v80, 65536, v84\n"
    "v_add_u32 v81, 65536, v85\n"
    "v_mfma_f32_32x32x16_bf16 v[192:207], v[36:39], v[8:11], v[192:207]\n"
    "ds_read_b64_tr_b16 v[54:55], v89 offset:12800\n"
    "v_add_u32 v82, 65536, v86\n"
    "v_mfma_f32_32x32x16_bf16 v[208:223], v[32:35], v[12:15], v[208:223]\n"
    "ds_read_b128 v[24:27], v83 offset:8192\n"
    "v_add_u32 v88, 0, v98\n"
    "v_add_u32 v89, 0, v99\n"
    "v_mfma_f32_32x32x16_bf16 v[224:239], v[36:39], v[12:15], v[224:239]\n"
    "ds_read_b128 v[28:31], v83 offset:12288\n"
    "v_add_u32 v83, 65536, v87\n"
    "s_waitcnt vmcnt(20) lgkmcnt(0)\n"
    "s_barrier\n"
    "v_mfma_f32_32x32x16_bf16 v[112:127], v[48:51], v[16:19], v[112:127]\n"
    "ds_read_b64_tr_b16 v[32:33], v88\n"
    "s_add_u32 m0, s87, 131072\n"
    "v_mfma_f32_32x32x16_bf16 v[128:143], v[52:55], v[16:19], v[128:143]\n"
    "buffer_load_dwordx4 v72, s[40:43], s78 offen lds\n"
    "ds_read_b64_tr_b16 v[34:35], v89\n"
    "v_mfma_f32_32x32x16_bf16 v[144:159], v[48:51], v[20:23], v[144:159]\n"
    "ds_read_b128 v[0:3], v80\n"
    "v_mfma_f32_32x32x16_bf16 v[160:175], v[52:55], v[20:23], v[160:175]\n"
    "buffer_load_dwordx4 v73, s[40:43], s78 offen offset:1024 lds\n"
    "ds_read_b128 v[4:7], v80 offset:4096\n"
    "v_mfma_f32_32x32x16_bf16 v[176:191], v[48:51], v[24:27], v[176:191]\n"
    "ds_read_b64_tr_b16 v[36:37], v88 offset:512\n"
    "v_mfma_f32_32x32x16_bf16 v[192:207], v[52:55], v[24:27], v[192:207]\n"
    "buffer_load_dwordx4 v74, s[40:43], s78 offen offset:2048 lds\n"
    "ds_read_b64_tr_b16 v[38:39], v89 offset:512\n"
    "v_mfma_f32_32x32x16_bf16 v[208:223], v[48:51], v[28:31], v[208:223]\n"
    "buffer_load_dwordx4 v75, s[40:43], s78 offen offset:3072 lds\n"
    "ds_read_b128 v[8:11], v80 offset:8192\n"
    "v_mfma_f32_32x32x16_bf16 v[224:239], v[52:55], v[28:31], v[224:239]\n"
    "ds_read_b128 v[12:15], v80 offset:12288\n"
    "s_add_u32 s78, s78, 524288\n"
    "s_waitcnt lgkmcnt(0)\n"
    "v_mfma_f32_32x32x16_bf16 v[112:127], v[32:35], v[0:3], v[112:127]\n"
    "ds_read_b64_tr_b16 v[48:49], v88 offset:4096\n"
    "s_add_u32 m0, s94, 32768\n"
    "s_lshl_b32 s82, s61, 19\n"
    "s_add_u32 s48, s74, 0\n"
    "s_addc_u32 s49, s75, 0\n"
    "s_add_u32 s48, s48, s82\n"
    "s_addc_u32 s49, s49, 0\n"
    "s_and_b32 s49, s49, 65535\n"
    "s_mov_b32 s50, 0\n"
    "s_mov_b32 s51, 131072\n"
    "v_mfma_f32_32x32x16_bf16 v[128:143], v[36:39], v[0:3], v[128:143]\n"
    "buffer_load_dwordx4 v64, s[36:39], s77 offen lds\n"
    "ds_read_b64_tr_b16 v[50:51], v89 offset:4096\n"
    "s_add_u32 s52, s74, 16777216\n"
    "s_addc_u32 s53, s75, 0\n"
    "s_add_u32 s52, s52, s82\n"
    "s_addc_u32 s53, s53, 0\n"
    "s_and_b32 s53, s53, 65535\n"
    "s_mov_b32 s54, 0\n"
    "s_mov_b32 s55, 131072\n"
    "v_mfma_f32_32x32x16_bf16 v[144:159], v[32:35], v[4:7], v[144:159]\n"
    "ds_read_b128 v[16:19], v81\n"
    "s_sub_u32 s82, 16, s62\n"
    "s_lshl_b32 s82, s82, 15\n"
    "s_mov_b32 s50, s82\n"
    "s_add_u32 s82, s62, 1\n"
    "s_lshl_b32 s82, s82, 15\n"
    "s_add_u32 s96, s82, s94\n"
    "v_mfma_f32_32x32x16_bf16 v[160:175], v[36:39], v[4:7], v[160:175]\n"
    "buffer_load_dwordx4 v65, s[36:39], s77 offen offset:1024 lds\n"
    "ds_read_b128 v[20:23], v81 offset:4096\n"
    "v_mfma_f32_32x32x16_bf16 v[176:191], v[32:35], v[8:11], v[176:191]\n"
    "ds_read_b64_tr_b16 v[52:53], v88 offset:4608\n"
    "v_mfma_f32_32x32x16_bf16 v[192:207], v[36:39], v[8:11], v[192:207]\n"
    "buffer_load_dwordx4 v66, s[36:39], s77 offen offset:2048 lds\n"
    "ds_read_b64_tr_b16 v[54:55], v89 offset:4608\n"
    "v_mfma_f32_32x32x16_bf16 v[208:223], v[32:35], v[12:15], v[208:223]\n"
    "ds_read_b128 v[24:27], v81 offset:8192\n"
    "v_mfma_f32_32x32x16_bf16 v[224:239], v[36:39], v[12:15], v[224:239]\n"
    "buffer_load_dwordx4 v67, s[36:39], s77 offen offset:3072 lds\n"
    "ds_read_b128 v[28:31], v81 offset:12288\n"
    "s_waitcnt lgkmcnt(0)\n"
    "v_mfma_f32_32x32x16_bf16 v[112:127], v[48:51], v[16:19], v[112:127]\n"
    "ds_read_b64_tr_b16 v[32:33], v88 offset:8192\n"
    "s_add_u32 m0, s94, 36864\n"
    "v_mfma_f32_32x32x16_bf16 v[128:143], v[52:55], v[16:19], v[128:143]\n"
    "buffer_load_dwordx4 v68, s[36:39], s77 offen lds\n"
    "ds_read_b64_tr_b16 v[34:35], v89 offset:8192\n"
    "v_mfma_f32_32x32x16_bf16 v[144:159], v[48:51], v[20:23], v[144:159]\n"
    "ds_read_b128 v[0:3], v82\n"
    "v_mfma_f32_32x32x16_bf16 v[160:175], v[52:55], v[20:23], v[160:175]\n"
    "buffer_load_dwordx4 v69, s[36:39], s77 offen offset:1024 lds\n"
    "ds_read_b128 v[4:7], v82 offset:4096\n"
    "v_mfma_f32_32x32x16_bf16 v[176:191], v[48:51], v[24:27], v[176:191]\n"
    "ds_read_b64_tr_b16 v[36:37], v88 offset:8704\n"
    "v_mfma_f32_32x32x16_bf16 v[192:207], v[52:55], v[24:27], v[192:207]\n"
    "buffer_load_dwordx4 v70, s[36:39], s77 offen offset:2048 lds\n"
    "ds_read_b64_tr_b16 v[38:39], v89 offset:8704\n"
    "v_mfma_f32_32x32x16_bf16 v[208:223], v[48:51], v[28:31], v[208:223]\n"
    "ds_read_b128 v[8:11], v82 offset:8192\n"
    "v_mfma_f32_32x32x16_bf16 v[224:239], v[52:55], v[28:31], v[224:239]\n"
    "buffer_load_dwordx4 v71, s[36:39], s77 offen offset:3072 lds\n"
    "ds_read_b128 v[12:15], v82 offset:12288\n"
    "s_waitcnt lgkmcnt(0)\n"
    "v_mfma_f32_32x32x16_bf16 v[112:127], v[32:35], v[0:3], v[112:127]\n"
    "ds_read_b64_tr_b16 v[48:49], v88 offset:12288\n"
    "s_add_u32 s77, s77, 128\n"
    "v_mfma_f32_32x32x16_bf16 v[128:143], v[36:39], v[0:3], v[128:143]\n"
    "ds_read_b64_tr_b16 v[50:51], v89 offset:12288\n"
    "v_mfma_f32_32x32x16_bf16 v[144:159], v[32:35], v[4:7], v[144:159]\n"
    "ds_read_b128 v[16:19], v83\n"
    "v_mfma_f32_32x32x16_bf16 v[160:175], v[36:39], v[4:7], v[160:175]\n"
    "ds_read_b128 v[20:23], v83 offset:4096\n"
    "v_mfma_f32_32x32x16_bf16 v[176:191], v[32:35], v[8:11], v[176:191]\n"
    "ds_read_b64_tr_b16 v[52:53], v88 offset:12800\n"
    "v_add_u32 v80, 0, v84\n"
    "v_add_u32 v81, 0, v85\n"
    "v_mfma_f32_32x32x16_bf16 v[192:207], v[36:39], v[8:11], v[192:207]\n"
    "ds_read_b64_tr_b16 v[54:55], v89 offset:12800\n"
    "v_add_u32 v82, 0, v86\n"
    "v_mfma_f32_32x32x16_bf16 v[208:223], v[32:35], v[12:15], v[208:223]\n"
    "ds_read_b128 v[24:27], v83 offset:8192\n"
    "v_add_u32 v88, 16384, v98\n"
    "v_add_u32 v89, 16384, v99\n"
    "v_mfma_f32_32x32x16_bf16 v[224:239], v[36:39], v[12:15], v[224:239]\n"
    "ds_read_b128 v[28:31], v83 offset:12288\n"
    "v_add_u32 v83, 0, v87\n"
    "s_waitcnt vmcnt(12)\n"
    "s_waitcnt vmcnt(14) lgkmcnt(0)\n"
    "s_barrier\n"
    "v_mfma_f32_32x32x16_bf16 v[112:127], v[48:51], v[16:19], v[112:127]\n"
    "ds_read_b64_tr_b16 v[32:33], v88\n"
    "s_add_u32 m0, s87, 98304\n"
    "v_mfma_f32_32x32x16_bf16 v[128:143], v[52:55], v[16:19], v[128:143]\n"
    "buffer_load_dwordx4 v72, s[40:43], s78 offen lds\n"
    "ds_read_b64_tr_b16 v[34:35], v89\n"
    "s_cmp_lg_u32 s60, 0\n"
    "s_cbranch_scc1 L_fs_%=\n"
    "v_mov_b32 v100, s63\n"
    "s_lshl_b32 s85, s62, 2\n"
    "v_mov_b32 v101, s85\n"
    "buffer_store_dword v100, v101, s[56:59], 0 offen sc1\n"
    "L_fs_%=:\n"
    "v_mfma_f32_32x32x16_bf16 v[144:159], v[48:51], v[20:23], v[144:159]\n"
    "ds_read_b128 v[0:3], v80\n"
    "v_mfma_f32_32x32x16_bf16 v[160:175], v[52:55], v[20:23], v[160:175]\n"
    "buffer_load_dwordx4 v73, s[40:43], s78 offen offset:1024 lds\n"
    "ds_read_b128 v[4:7], v80 offset:4096\n"
    "v_mfma_f32_32x32x16_bf16 v[176:191], v[48:51], v[24:27], v[176:191]\n"
    "ds_read_b64_tr_b16 v[36:37], v88 offset:512\n"
    "v_mfma_f32_32x32x16_bf16 v[192:207], v[52:55], v[24:27], v[192:207]\n"
    "buffer_load_dwordx4 v74, s[40:43], s78 offen offset:2048 lds\n"
    "ds_read_b64_tr_b16 v[38:39], v89 offset:512\n"
    "v_mfma_f32_32x32x16_bf16 v[208:223], v[48:51], v[28:31], v[208:223]\n"
    "buffer_load_dwordx4 v75, s[40:43], s78 offen offset:3072 lds\n"
    "ds_read_b128 v[8:11], v80 offset:8192\n"
    "v_mfma_f32_32x32x16_bf16 v[224:239], v[52:55], v[28:31], v[224:239]\n"
    "ds_read_b128 v[12:15], v80 offset:12288\n"
    "s_add_u32 s78, s78, 524288\n"
    "s_waitcnt lgkmcnt(0)\n"
    "v_mfma_f32_32x32x16_bf16 v[112:127], v[32:35], v[0:3], v[112:127]\n"
    "ds_read_b64_tr_b16 v[48:49], v88 offset:4096\n"
    "s_add_u32 m0, s94, 65536\n"
    "v_mfma_f32_32x32x16_bf16 v[128:143], v[36:39], v[0:3], v[128:143]\n"
    "buffer_load_dwordx4 v64, s[36:39], s77 offen lds\n"
    "ds_read_b64_tr_b16 v[50:51], v89 offset:4096\n"
    "v_mfma_f32_32x32x16_bf16 v[144:159], v[32:35], v[4:7], v[144:159]\n"
    "ds_read_b128 v[16:19], v81\n"
    "v_mfma_f32_32x32x16_bf16 v[160:175], v[36:39], v[4:7], v[160:175]\n"
    "buffer_load_dwordx4 v65, s[36:39], s77 offen offset:1024 lds\n"
    "ds_read_b128 v[20:23], v81 offset:4096\n"
    "v_mfma_f32_32x32x16_bf16 v[176:191], v[32:35], v[8:11], v[176:191]\n"
    "ds_read_b64_tr_b16 v[52:53], v88 offset:4608\n"
    "v_mfma_f32_32x32x16_bf16 v[192:207], v[36:39], v[8:11], v[192:207]\n"
    "buffer_load_dwordx4 v66, s[36:39], s77 offen offset:2048 lds\n"
    "ds_read_b64_tr_b16 v[54:55], v89 offset:4608\n"
    "v_mfma_f32_32x32x16_bf16 v[208:223], v[32:35], v[12:15], v[208:223]\n"
    "ds_read_b128 v[24:27], v81 offset:8192\n"
    "v_mfma_f32_32x32x16_bf16 v[224:239], v[36:39], v[12:15], v[224:239]\n"
    "buffer_load_dwordx4 v67, s[36:39], s77 offen offset:3072 lds\n"
    "ds_read_b128 v[28:31], v81 offset:12288\n"
    "s_waitcnt lgkmcnt(0)\n"
    "v_mfma_f32_32x32x16_bf16 v[112:127], v[48:51], v[16:19], v[112:127]\n"
    "ds_read_b64_tr_b16 v[32:33], v88 offset:8192\n"
    "s_add_u32 m0, s94, 69632\n"
    "v_mfma_f32_32x32x16_bf16 v[128:143], v[52:55], v[16:19], v[128:143]\n"
    "buffer_load_dwordx4 v68, s[36:39], s77 offen lds\n"
    "ds_read_b64_tr_b16 v[34:35], v89 offset:8192\n"
    "v_mfma_f32_32x32x16_bf16 v[144:159], v[48:51], v[20:23], v[144:159]\n"
    "ds_read_b128 v[0:3], v82\n"
    "v_mfma_f32_32x32x16_bf16 v[160:175], v[52:55], v[20:23], v[160:175]\n"
    "buffer_load_dwordx4 v69, s[36:39], s77 offen offset:1024 lds\n"
    "ds_read_b128 v[4:7], v82 offset:4096\n"
    "v_mfma_f32_32x32x16_bf16 v[176:191], v[48:51], v[24:27], v[176:191]\n"
    "ds_read_b64_tr_b16 v[36:37], v88 offset:8704\n"
    "v_mfma_f32_32x32x16_bf16 v[192:207], v[52:55], v[24:27], v[192:207]\n"
    "buffer_load_dwordx4 v70, s[36:39], s77 offen offset:2048 lds\n"
    "ds_read_b64_tr_b16 v[38:39], v89 offset:8704\n"
    "v_mfma_f32_32x32x16_bf16 v[208:223], v[48:51], v[28:31], v[208:223]\n"
    "ds_read_b128 v[8:11], v82 offset:8192\n"
    "v_mfma_f32_32x32x16_bf16 v[224:239], v[52:55], v[28:31], v[224:239]\n"
    "buffer_load_dwordx4 v71, s[36:39], s77 offen offset:3072 lds\n"
    "ds_read_b128 v[12:15], v82 offset:12288\n"
    "s_waitcnt lgkmcnt(0)\n"
    "v_mfma_f32_32x32x16_bf16 v[112:127], v[32:35], v[0:3], v[112:127]\n"
    "ds_read_b64_tr_b16 v[48:49], v88 offset:12288\n"
    "s_add_u32 s77, s77, 128\n"
    "v_mfma_f32_32x32x16_bf16 v[128:143], v[36:39], v[0:3], v[128:143]\n"
    "ds_read_b64_tr_b16 v[50:51], v89 offset:12288\n"
    "v_mfma_f32_32x32x16_bf16 v[144:159], v[32:35], v[4:7], v[144:159]\n"
    "ds_read_b128 v[16:19], v83\n"
    "v_mfma_f32_32x32x16_bf16 v[160:175], v[36:39], v[4:7], v[160:175]\n"
    "ds_read_b128 v[20:23], v83 offset:4096\n"
    "v_mfma_f32_32x32x16_bf16 v[176:191], v[32:35], v[8:11], v[176:191]\n"
    "ds_read_b64_tr_b16 v[52:53], v88 offset:12800\n"
    "v_add_u32 v80, 32768, v84\n"
    "v_add_u32 v81, 32768, v85\n"
    "v_mfma_f32_32x32x16_bf16 v[192:207], v[36:39], v[8:11], v[192:207]\n"
    "ds_read_b64_tr_b16 v[54:55], v89 offset:12800\n"
    "v_add_u32 v82, 32768, v86\n"
    "v_mfma_f32_32x32x16_bf16 v[208:223], v[32:35], v[12:15], v[208:223]\n"
    "ds_read_b128 v[24:27], v83 offset:8192\n"
    "v_add_u32 v88, 32768, v98\n"
    "v_add_u32 v89, 32768, v99\n"
    "v_mfma_f32_32x32x16_bf16 v[224:239], v[36:39], v[12:15], v[224:239]\n"
    "ds_read_b128 v[28:31], v83 offset:12288\n"
    "v_add_u32 v83, 32768, v87\n"
    "s_waitcnt vmcnt(12) lgkmcnt(0)\n"
    "s_barrier\n"
    "v_mfma_f32_32x32x16_bf16 v[112:127], v[48:51], v[16:19], v[112:127]\n"
    "ds_read_b64_tr_b16 v[32:33], v88\n"
    "v_mfma_f32_32x32x16_bf16 v[128:143], v[52:55], v[16:19], v[128:143]\n"
    "ds_read_b64_tr_b16 v[34:35], v89\n"
    "v_mfma_f32_32x32x16_bf16 v[144:159], v[48:51], v[20:23], v[144:159]\n"
    "ds_read_b128 v[0:3], v80\n"
    "v_mfma_f32_32x32x16_bf16 v[160:175], v[52:55], v[20:23], v[160:175]\n"
    "ds_read_b128 v[4:7], v80 offset:4096\n"
    "v_mfma_f32_32x32x16_bf16 v[176:191], v[48:51], v[24:27], v[176:191]\n"
    "ds_read_b64_tr_b16 v[36:37], v88 offset:512\n"
    "v_mfma_f32_32x32x16_bf16 v[192:207], v[52:55], v[24:27], v[192:207]\n"
    "ds_read_b64_tr_b16 v[38:39], v89 offset:512\n"
    "v_mfma_f32_32x32x16_bf16 v[208:223], v[48:51], v[28:31], v[208:223]\n"
    "ds_read_b128 v[8:11], v80 offset:8192\n"
    "v_mfma_f32_32x32x16_bf16 v[224:239], v[52:55], v[28:31], v[224:239]\n"
    "ds_read_b128 v[12:15], v80 offset:12288\n"
    "s_waitcnt lgkmcnt(0)\n"
    "v_mfma_f32_32x32x16_bf16 v[112:127], v[32:35], v[0:3], v[112:127]\n"
    "ds_read_b64_tr_b16 v[48:49], v88 offset:4096\n"
    "v_mfma_f32_32x32x16_bf16 v[128:143], v[36:39], v[0:3], v[128:143]\n"
    "ds_read_b64_tr_b16 v[50:51], v89 offset:4096\n"
    "v_and_b32 v103, 7, v250\n"
    "v_lshlrev_b32 v103, 2, v103\n"
    "v_mfma_f32_32x32x16_bf16 v[144:159], v[32:35], v[4:7], v[144:159]\n"
    "ds_read_b128 v[16:19], v81\n"
    "buffer_load_dword v102, v103, s[56:59], 0 offen sc1\n"
    "v_mfma_f32_32x32x16_bf16 v[160:175], v[36:39], v[4:7], v[160:175]\n"
    "ds_read_b128 v[20:23], v81 offset:4096\n"
    "s_mov_b32 s77, s94\n"
    "v_mfma_f32_32x32x16_bf16 v[176:191], v[32:35], v[8:11], v[176:191]\n"
    "ds_read_b64_tr_b16 v[52:53], v88 offset:4608\n"
    "v_mfma_f32_32x32x16_bf16 v[192:207], v[36:39], v[8:11], v[192:207]\n"
    "ds_read_b64_tr_b16 v[54:55], v89 offset:4608\n"
    "v_mfma_f32_32x32x16_bf16 v[208:223], v[32:35], v[12:15], v[208:223]\n"
    "ds_read_b128 v[24:27], v81 offset:8192\n"
    "v_mfma_f32_32x32x16_bf16 v[224:239], v[36:39], v[12:15], v[224:239]\n"
    "ds_read_b128 v[28:31], v81 offset:12288\n"
    "s_waitcnt lgkmcnt(0)\n"
    "v_mfma_f32_32x32x16_bf16 v[112:127], v[48:51], v[16:19], v[112:127]\n"
    "ds_read_b64_tr_b16 v[32:33], v88 offset:8192\n"
    "v_mfma_f32_32x32x16_bf16 v[128:143], v[52:55], v[16:19], v[128:143]\n"
    "ds_read_b64_tr_b16 v[34:35], v89 offset:8192\n"
    "v_mfma_f32_32x32x16_bf16 v[144:159], v[48:51], v[20:23], v[144:159]\n"
    "ds_read_b128 v[0:3], v82\n"
    "v_mfma_f32_32x32x16_bf16 v[160:175], v[52:55], v[20:23], v[160:175]\n"
    "ds_read_b128 v[4:7], v82 offset:4096\n"
    "v_mfma_f32_32x32x16_bf16 v[176:191], v[48:51], v[24:27], v[176:191]\n"
    "ds_read_b64_tr_b16 v[36:37], v88 offset:8704\n"
    "v_mfma_f32_32x32x16_bf16 v[192:207], v[52:55], v[24:27], v[192:207]\n"
    "ds_read_b64_tr_b16 v[38:39], v89 offset:8704\n"
    "v_mfma_f32_32x32x16_bf16 v[208:223], v[48:51], v[28:31], v[208:223]\n"
    "ds_read_b128 v[8:11], v82 offset:8192\n"
    "v_mfma_f32_32x32x16_bf16 v[224:239], v[52:55], v[28:31], v[224:239]\n"
    "ds_read_b128 v[12:15], v82 offset:12288\n"
    "s_waitcnt lgkmcnt(0)\n"
    "v_mfma_f32_32x32x16_bf16 v[112:127], v[32:35], v[0:3], v[112:127]\n"
    "ds_read_b64_tr_b16 v[48:49], v88 offset:12288\n"
    "v_mfma_f32_32x32x16_bf16 v[128:143], v[36:39], v[0:3], v[128:143]\n"
    "ds_read_b64_tr_b16 v[50:51], v89 offset:12288\n"
    "v_mfma_f32_32x32x16_bf16 v[144:159], v[32:35], v[4:7], v[144:159]\n"
    "ds_read_b128 v[16:19], v83\n"
    "v_mfma_f32_32x32x16_bf16 v[160:175], v[36:39], v[4:7], v[160:175]\n"
    "ds_read_b128 v[20:23], v83 offset:4096\n"
    "v_mfma_f32_32x32x16_bf16 v[176:191], v[32:35], v[8:11], v[176:191]\n"
    "ds_read_b64_tr_b16 v[52:53], v88 offset:12800\n"
    "v_add_u32 v80, 65536, v84\n"
    "v_add_u32 v81, 65536, v85\n"
    "v_mfma_f32_32x32x16_bf16 v[192:207], v[36:39], v[8:11], v[192:207]\n"
    "ds_read_b64_tr_b16 v[54:55], v89 offset:12800\n"
    "v_add_u32 v82, 65536, v86\n"
    "v_mfma_f32_32x32x16_bf16 v[208:223], v[32:35], v[12:15], v[208:223]\n"
    "ds_read_b128 v[24:27], v83 offset:8192\n"
    "v_add_u32 v88, 0, v98\n"
    "v_add_u32 v89, 0, v99\n"
    "v_mfma_f32_32x32x16_bf16 v[224:239], v[36:39], v[12:15], v[224:239]\n"
    "ds_read_b128 v[28:31], v83 offset:12288\n"
    "v_add_u32 v83, 65536, v87\n"
    "s_cmp_lg_u32 s60, 0\n"
    "s_cbranch_scc1 L_pl_%=\n"
    "s_waitcnt vmcnt(0)\n"
    "v_cmp_ne_u32 vcc, s63, v102\n"
    "s_cbranch_vccz L_plo_%=\n"
    "s_mov_b32 s85, 0\n"
    "L_pls_%=:\n"
    "s_sleep 4\n"
    "buffer_load_dword v102, v103, s[56:59], 0 offen sc1\n"
    "s_waitcnt vmcnt(0)\n"
    "s_add_u32 s85, s85, 1\n"
    "v_cmp_ne_u32 vcc, s63, v102\n"
    "s_cbranch_vccz L_plo_%=\n"
    "s_cmp_lt_u32 s85, 200000\n"
    "s_cbranch_scc1 L_pls_%=\n"
    "L_plo_%=:\n"
    "buffer_inv sc1\n"
    "s_cmp_lg_u32 s62, 0\n"
    "s_cbranch_scc1 L_pl_%=e\n"
    "v_mov_b32 v100, s63\n"
    "v_mov_b32 v101, 32\n"
    "buffer_store_dword v100, v101, s[56:59], 0 offen sc1\n"
    "L_pl_%=e:\n"
    "s_waitcnt vmcnt(0)\n"
    "L_pl_%=:\n"
    "s_waitcnt vmcnt(1) lgkmcnt(0)\n"
    "s_barrier\n"
    "v_mfma_f32_32x32x16_bf16 v[112:127], v[48:51], v[16:19], v[112:127]\n"
    "ds_read_b64_tr_b16 v[32:33], v88\n"
    "v_mfma_f32_32x32x16_bf16 v[128:143], v[52:55], v[16:19], v[128:143]\n"
    "ds_read_b64_tr_b16 v[34:35], v89\n"
    "v_mfma_f32_32x32x16_bf16 v[144:159], v[48:51], v[20:23], v[144:159]\n"
    "ds_read_b128 v[0:3], v80\n"
    "v_mfma_f32_32x32x16_bf16 v[160:175], v[52:55], v[20:23], v[160:175]\n"
    "ds_read_b128 v[4:7], v80 offset:4096\n"
    "v_mfma_f32_32x32x16_bf16 v[176:191], v[48:51], v[24:27], v[176:191]\n"
    "ds_read_b64_tr_b16 v[36:37], v88 offset:512\n"
    "v_mfma_f32_32x32x16_bf16 v[192:207], v[52:55], v[24:27], v[192:207]\n"
    "ds_read_b64_tr_b16 v[38:39], v89 offset:512\n"
    "v_mfma_f32_32x32x16_bf16 v[208:223], v[48:51], v[28:31], v[208:223]\n"
    "ds_read_b128 v[8:11], v80 offset:8192\n"
    "v_mfma_f32_32x32x16_bf16 v[224:239], v[52:55], v[28:31], v[224:239]\n"
    "ds_read_b128 v[12:15], v80 offset:12288\n"
    "s_waitcnt lgkmcnt(0)\n"
    "v_mfma_f32_32x32x16_bf16 v[112:127], v[32:35], v[0:3], v[112:127]\n"
    "ds_read_b64_tr_b16 v[48:49], v88 offset:4096\n"
    "s_add_u32 m0, s94, 0\n"
    "v_mfma_f32_32x32x16_bf16 v[128:143], v[36:39], v[0:3], v[128:143]\n"
    "buffer_load_dwordx4 v251, s[48:51], s77 offen sc1 lds\n"
    "ds_read_b64_tr_b16 v[50:51], v89 offset:4096\n"
    "v_mfma_f32_32x32x16_bf16 v[144:159], v[32:35], v[4:7], v[144:159]\n"
    "ds_read_b128 v[16:19], v81\n"
    "v_mfma_f32_32x32x16_bf16 v[160:175], v[36:39], v[4:7], v[160:175]\n"
    "buffer_load_dwordx4 v251, s[48:51], s77 offen offset:1024 sc1 lds\n"
    "ds_read_b128 v[20:23], v81 offset:4096\n"
    "v_mfma_f32_32x32x16_bf16 v[176:191], v[32:35], v[8:11], v[176:191]\n"
    "ds_read_b64_tr_b16 v[52:53], v88 offset:4608\n"
    "v_mfma_f32_32x32x16_bf16 v[192:207], v[36:39], v[8:11], v[192:207]\n"
    "buffer_load_dwordx4 v251, s[48:51], s77 offen offset:2048 sc1 lds\n"
    "ds_read_b64_tr_b16 v[54:55], v89 offset:4608\n"
    "v_mfma_f32_32x32x16_bf16 v[208:223], v[32:35], v[12:15], v[208:223]\n"
    "ds_read_b128 v[24:27], v81 offset:8192\n"
    "v_mfma_f32_32x32x16_bf16 v[224:239], v[36:39], v[12:15], v[224:239]\n"
    "buffer_load_dwordx4 v251, s[48:51], s77 offen offset:3072 sc1 lds\n"
    "ds_read_b128 v[28:31], v81 offset:12288\n"
    "s_waitcnt lgkmcnt(0)\n"
    "v_mfma_f32_32x32x16_bf16 v[112:127], v[48:51], v[16:19], v[112:127]\n"
    "ds_read_b64_tr_b16 v[32:33], v88 offset:8192\n"
    "s_add_u32 m0, s94, 4096\n"
    "v_mfma_f32_32x32x16_bf16 v[128:143], v[52:55], v[16:19], v[128:143]\n"
    "buffer_load_dwordx4 v248, s[48:51], s77 offen sc1 lds\n"
    "ds_read_b64_tr_b16 v[34:35], v89 offset:8192\n"
    "v_mfma_f32_32x32x16_bf16 v[144:159], v[48:51], v[20:23], v[144:159]\n"
    "ds_read_b128 v[0:3], v82\n"
    "v_mfma_f32_32x32x16_bf16 v[160:175], v[52:55], v[20:23], v[160:175]\n"
    "buffer_load_dwordx4 v248, s[48:51], s77 offen offset:1024 sc1 lds\n"
    "ds_read_b128 v[4:7], v82 offset:4096\n"
    "v_mfma_f32_32x32x16_bf16 v[176:191], v[48:51], v[24:27], v[176:191]\n"
    "ds_read_b64_tr_b16 v[36:37], v88 offset:8704\n"
    "v_mfma_f32_32x32x16_bf16 v[192:207], v[52:55], v[24:27], v[192:207]\n"
    "buffer_load_dwordx4 v248, s[48:51], s77 offen offset:2048 sc1 lds\n"
    "ds_read_b64_tr_b16 v[38:39], v89 offset:8704\n"
    "v_mfma_f32_32x32x16_bf16 v[208:223], v[48:51], v[28:31], v[208:223]\n"
    "ds_read_b128 v[8:11], v82 offset:8192\n"
    "v_mfma_f32_32x32x16_bf16 v[224:239], v[52:55], v[28:31], v[224:239]\n"
    "buffer_load_dwordx4 v248, s[48:51], s77 offen offset:3072 sc1 lds\n"
    "ds_read_b128 v[12:15], v82 offset:12288\n"
    "s_waitcnt lgkmcnt(0)\n"
    "v_mfma_f32_32x32x16_bf16 v[112:127], v[32:35], v[0:3], v[112:127]\n"
    "ds_read_b64_tr_b16 v[48:49], v88 offset:12288\n"
    "s_add_u32 m0, s94, 32768\n"
    "s_add_u32 s77, s77, 32768\n"
    "v_mfma_f32_32x32x16_bf16 v[128:143], v[36:39], v[0:3], v[128:143]\n"
    "buffer_load_dwordx4 v251, s[48:51], s77 offen sc1 lds\n"
    "ds_read_b64_tr_b16 v[50:51], v89 offset:12288\n"
    "v_mfma_f32_32x32x16_bf16 v[144:159], v[32:35], v[4:7], v[144:159]\n"
    "ds_read_b128 v[16:19], v83\n"
    "v_mfma_f32_32x32x16_bf16 v[160:175], v[36:39], v[4:7], v[160:175]\n"
    "buffer_load_dwordx4 v251, s[48:51], s77 offen offset:1024 sc1 lds\n"
    "ds_read_b128 v[20:23], v83 offset:4096\n"
    "v_mfma_f32_32x32x16_bf16 v[176:191], v[32:35], v[8:11], v[176:191]\n"
    "ds_read_b64_tr_b16 v[52:53], v88 offset:12800\n"
    "v_mfma_f32_32x32x16_bf16 v[192:207], v[36:39], v[8:11], v[192:207]\n"
    "buffer_load_dwordx4 v251, s[48:51], s77 offen offset:2048 sc1 lds\n"
    "ds_read_b64_tr_b16 v[54:55], v89 offset:12800\n"
    "v_mfma_f32_32x32x16_bf16 v[208:223], v[32:35], v[12:15], v[208:223]\n"
    "ds_read_b128 v[24:27], v83 offset:8192\n"
    "v_mfma_f32_32x32x16_bf16 v[224:239], v[36:39], v[12:15], v[224:239]\n"
    "buffer_load_dwordx4 v251, s[48:51], s77 offen offset:3072 sc1 lds\n"
    "ds_read_b128 v[28:31], v83 offset:12288\n"
    "s_waitcnt lgkmcnt(0)\n"
    "s_barrier\n"
    "v_mfma_f32_32x32x16_bf16 v[112:127], v[48:51], v[16:19], v[112:127]\n"
    "s_add_u32 m0, s94, 36864\n"
    "v_mfma_f32_32x32x16_bf16 v[128:143], v[52:55], v[16:19], v[128:143]\n"
    "buffer_load_dwordx4 v248, s[48:51], s77 offen sc1 lds\n"
    "v_mfma_f32_32x32x16_bf16 v[144:159], v[48:51], v[20:23], v[144:159]\n"
    "v_mfma_f32_32x32x16_bf16 v[160:175], v[52:55], v[20:23], v[160:175]\n"
    "buffer_load_dwordx4 v248, s[48:51], s77 offen offset:1024 sc1 lds\n"
    "v_mfma_f32_32x32x16_bf16 v[176:191], v[48:51], v[24:27], v[176:191]\n"
    "v_mfma_f32_32x32x16_bf16 v[192:207], v[52:55], v[24:27], v[192:207]\n"
    "buffer_load_dwordx4 v248, s[48:51], s77 offen offset:2048 sc1 lds\n"
    "v_mfma_f32_32x32x16_bf16 v[208:223], v[48:51], v[28:31], v[208:223]\n"
    "v_mfma_f32_32x32x16_bf16 v[224:239], v[52:55], v[28:31], v[224:239]\n"
    "buffer_load_dwordx4 v248, s[48:51], s77 offen offset:3072 sc1 lds\n"
    "s_add_u32 s77, s77, 32768\n"
    "s_mov_b32 s84, 0x3e0293ee\n"
    "s_nop 4\n"
    "s_lshr_b32 s82, s60, 1\n"
    "s_lshl_b32 s82, s82, 15\n"
    "s_and_b32 s83, s60, 1\n"
    "s_lshl_b32 s83, s83, 12\n"
    "s_add_u32 s83, s83, s82\n"
    "s_add_u32 s83, s83, 98304\n"
    "v_add_u32 v90, s83, v251\n"
    "v_mul_f32 v112, s84, v112\n"
    "v_mul_f32 v113, s84, v113\n"
    "v_mul_f32 v114, s84, v114\n"
    "v_mul_f32 v115, s84, v115\n"
    "v_mul_f32 v116, s84, v116\n"
    "v_mul_f32 v117, s84, v117\n"
    "v_mul_f32 v118, s84, v118\n"
    "v_mul_f32 v119, s84, v119\n"
    "v_mul_f32 v120, s84, v120\n"
    "v_mul_f32 v121, s84, v121\n"
    "v_mul_f32 v122, s84, v122\n"
    "v_mul_f32 v123, s84, v123\n"
    "v_mul_f32 v124, s84, v124\n"
    "v_mul_f32 v125, s84, v125\n"
    "v_mul_f32 v126, s84, v126\n"
    "v_mul_f32 v127, s84, v127\n"
    "v_cvt_pk_bf16_f32 v112, v112, v113\n"
    "v_cvt_pk_bf16_f32 v113, v114, v115\n"
    "v_cvt_pk_bf16_f32 v114, v116, v117\n"
    "v_cvt_pk_bf16_f32 v115, v118, v119\n"
    "v_cvt_pk_bf16_f32 v116, v120, v121\n"
    "v_cvt_pk_bf16_f32 v117, v122, v123\n"
    "v_cvt_pk_bf16_f32 v118, v124, v125\n"
    "v_cvt_pk_bf16_f32 v119, v126, v127\n"
    "ds_write_b128 v90, v[112:115]\n"
    "ds_write_b128 v90, v[116:119] offset:1024\n"
    "v_mul_f32 v128, s84, v128\n"
    "v_mul_f32 v129, s84, v129\n"
    "v_mul_f32 v130, s84, v130\n"
    "v_mul_f32 v131, s84, v131\n"
    "v_mul_f32 v132, s84, v132\n"
    "v_mul_f32 v133, s84, v133\n"
    "v_mul_f32 v134, s84, v134\n"
    "v_mul_f32 v135, s84, v135\n"
    "v_mul_f32 v136, s84, v136\n"
    "v_mul_f32 v137, s84, v137\n"
    "v_mul_f32 v138, s84, v138\n"
    "v_mul_f32 v139, s84, v139\n"
    "v_mul_f32 v140, s84, v140\n"
    "v_mul_f32 v141, s84, v141\n"
    "v_mul_f32 v142, s84, v142\n"
    "v_mul_f32 v143, s84, v143\n"
    "v_cvt_pk_bf16_f32 v128, v128, v129\n"
    "v_cvt_pk_bf16_f32 v129, v130, v131\n"
    "v_cvt_pk_bf16_f32 v130, v132, v133\n"
    "v_cvt_pk_bf16_f32 v131, v134, v135\n"
    "v_cvt_pk_bf16_f32 v132, v136, v137\n"
    "v_cvt_pk_bf16_f32 v133, v138, v139\n"
    "v_cvt_pk_bf16_f32 v134, v140, v141\n"
    "v_cvt_pk_bf16_f32 v135, v142, v143\n"
    "ds_write_b128 v90, v[128:131] offset:2048\n"
    "ds_write_b128 v90, v[132:135] offset:3072\n"
    "v_mul_f32 v144, s84, v144\n"
    "v_mul_f32 v145, s84, v145\n"
    "v_mul_f32 v146, s84, v146\n"
    "v_mul_f32 v147, s84, v147\n"
    "v_mul_f32 v148, s84, v148\n"
    "v_mul_f32 v149, s84, v149\n"
    "v_mul_f32 v150, s84, v150\n"
    "v_mul_f32 v151, s84, v151\n"
    "v_mul_f32 v152, s84, v152\n"
    "v_mul_f32 v153, s84, v153\n"
    "v_mul_f32 v154, s84, v154\n"
    "v_mul_f32 v155, s84, v155\n"
    "v_mul_f32 v156, s84, v156\n"
    "v_mul_f32 v157, s84, v157\n"
    "v_mul_f32 v158, s84, v158\n"
    "v_mul_f32 v159, s84, v159\n"
    "v_cvt_pk_bf16_f32 v144, v144, v145\n"
    "v_cvt_pk_bf16_f32 v145, v146, v147\n"
    "v_cvt_pk_bf16_f32 v146, v148, v149\n"
    "v_cvt_pk_bf16_f32 v147, v150, v151\n"
    "v_cvt_pk_bf16_f32 v148, v152, v153\n"
    "v_cvt_pk_bf16_f32 v149, v154, v155\n"
    "v_cvt_pk_bf16_f32 v150, v156, v157\n"
    "v_cvt_pk_bf16_f32 v151, v158, v159\n"
    "ds_write_b128 v90, v[144:147] offset:8192\n"
    "ds_write_b128 v90, v[148:151] offset:9216\n"
    "v_mul_f32 v160, s84, v160\n"
    "v_mul_f32 v161, s84, v161\n"
    "v_mul_f32 v162, s84, v162\n"
    "v_mul_f32 v163, s84, v163\n"
    "v_mul_f32 v164, s84, v164\n"
    "v_mul_f32 v165, s84, v165\n"
    "v_mul_f32 v166, s84, v166\n"
    "v_mul_f32 v167, s84, v167\n"
    "v_mul_f32 v168, s84, v168\n"
    "v_mul_f32 v169, s84, v169\n"
    "v_mul_f32 v170, s84, v170\n"
    "v_mul_f32 v171, s84, v171\n"
    "v_mul_f32 v172, s84, v172\n"
    "v_mul_f32 v173, s84, v173\n"
    "v_mul_f32 v174, s84, v174\n"
    "v_mul_f32 v175, s84, v175\n"
    "v_cvt_pk_bf16_f32 v160, v160, v161\n"
    "v_cvt_pk_bf16_f32 v161, v162, v163\n"
    "v_cvt_pk_bf16_f32 v162, v164, v165\n"
    "v_cvt_pk_bf16_f32 v163, v166, v167\n"
    "v_cvt_pk_bf16_f32 v164, v168, v169\n"
    "v_cvt_pk_bf16_f32 v165, v170, v171\n"
    "v_cvt_pk_bf16_f32 v166, v172, v173\n"
    "v_cvt_pk_bf16_f32 v167, v174, v175\n"
    "ds_write_b128 v90, v[160:163] offset:10240\n"
    "ds_write_b128 v90, v[164:167] offset:11264\n"
    "v_mul_f32 v176, s84, v176\n"
    "v_mul_f32 v177, s84, v177\n"
    "v_mul_f32 v178, s84, v178\n"
    "v_mul_f32 v179, s84, v179\n"
    "v_mul_f32 v180, s84, v180\n"
    "v_mul_f32 v181, s84, v181\n"
    "v_mul_f32 v182, s84, v182\n"
    "v_mul_f32 v183, s84, v183\n"
    "v_mul_f32 v184, s84, v184\n"
    "v_mul_f32 v185, s84, v185\n"
    "v_mul_f32 v186, s84, v186\n"
    "v_mul_f32 v187, s84, v187\n"
    "v_mul_f32 v188, s84, v188\n"
    "v_mul_f32 v189, s84, v189\n"
    "v_mul_f32 v190, s84, v190\n"
    "v_mul_f32 v191, s84, v191\n"
    "v_cvt_pk_bf16_f32 v176, v176, v177\n"
    "v_cvt_pk_bf16_f32 v177, v178, v179\n"
    "v_cvt_pk_bf16_f32 v178, v180, v181\n"
    "v_cvt_pk_bf16_f32 v179, v182, v183\n"
    "v_cvt_pk_bf16_f32 v180, v184, v185\n"
    "v_cvt_pk_bf16_f32 v181, v186, v187\n"
    "v_cvt_pk_bf16_f32 v182, v188, v189\n"
    "v_cvt_pk_bf16_f32 v183, v190, v191\n"
    "ds_write_b128 v90, v[176:179] offset:16384\n"
    "ds_write_b128 v90, v[180:183] offset:17408\n"
    "v_mul_f32 v192, s84, v192\n"
    "v_mul_f32 v193, s84, v193\n"
    "v_mul_f32 v194, s84, v194\n"
    "v_mul_f32 v195, s84, v195\n"
    "v_mul_f32 v196, s84, v196\n"
    "v_mul_f32 v197, s84, v197\n"
    "v_mul_f32 v198, s84, v198\n"
    "v_mul_f32 v199, s84, v199\n"
    "v_mul_f32 v200, s84, v200\n"
    "v_mul_f32 v201, s84, v201\n"
    "v_mul_f32 v202, s84, v202\n"
    "v_mul_f32 v203, s84, v203\n"
    "v_mul_f32 v204, s84, v204\n"
    "v_mul_f32 v205, s84, v205\n"
    "v_mul_f32 v206, s84, v206\n"
    "v_mul_f32 v207, s84, v207\n"
    "v_cvt_pk_bf16_f32 v192, v192, v193\n"
    "v_cvt_pk_bf16_f32 v193, v194, v195\n"
    "v_cvt_pk_bf16_f32 v194, v196, v197\n"
    "v_cvt_pk_bf16_f32 v195, v198, v199\n"
    "v_cvt_pk_bf16_f32 v196, v200, v201\n"
    "v_cvt_pk_bf16_f32 v197, v202, v203\n"
    "v_cvt_pk_bf16_f32 v198, v204, v205\n"
    "v_cvt_pk_bf16_f32 v199, v206, v207\n"
    "ds_write_b128 v90, v[192:195] offset:18432\n"
    "ds_write_b128 v90, v[196:199] offset:19456\n"
    "v_mul_f32 v208, s84, v208\n"
    "v_mul_f32 v209, s84, v209\n"
    "v_mul_f32 v210, s84, v210\n"
    "v_mul_f32 v211, s84, v211\n"
    "v_mul_f32 v212, s84, v212\n"
    "v_mul_f32 v213, s84, v213\n"
    "v_mul_f32 v214, s84, v214\n"
    "v_mul_f32 v215, s84, v215\n"
    "v_mul_f32 v216, s84, v216\n"
    "v_mul_f32 v217, s84, v217\n"
    "v_mul_f32 v218, s84, v218\n"
    "v_mul_f32 v219, s84, v219\n"
    "v_mul_f32 v220, s84, v220\n"
    "v_mul_f32 v221, s84, v221\n"
    "v_mul_f32 v222, s84, v222\n"
    "v_mul_f32 v223, s84, v223\n"
    "v_cvt_pk_bf16_f32 v208, v208, v209\n"
    "v_cvt_pk_bf16_f32 v209, v210, v211\n"
    "v_cvt_pk_bf16_f32 v210, v212, v213\n"
    "v_cvt_pk_bf16_f32 v211, v214, v215\n"
    "v_cvt_pk_bf16_f32 v212, v216, v217\n"
    "v_cvt_pk_bf16_f32 v213, v218, v219\n"
    "v_cvt_pk_bf16_f32 v214, v220, v221\n"
    "v_cvt_pk_bf16_f32 v215, v222, v223\n"
    "ds_write_b128 v90, v[208:211] offset:24576\n"
    "ds_write_b128 v90, v[212:215] offset:25600\n"
    "v_mul_f32 v224, s84, v224\n"
    "v_mul_f32 v225, s84, v225\n"
    "v_mul_f32 v226, s84, v226\n"
    "v_mul_f32 v227, s84, v227\n"
    "v_mul_f32 v228, s84, v228\n"
    "v_mul_f32 v229, s84, v229\n"
    "v_mul_f32 v230, s84, v230\n"
    "v_mul_f32 v231, s84, v231\n"
    "v_mul_f32 v232, s84, v232\n"
    "v_mul_f32 v233, s84, v233\n"
    "v_mul_f32 v234, s84, v234\n"
    "v_mul_f32 v235, s84, v235\n"
    "v_mul_f32 v236, s84, v236\n"
    "v_mul_f32 v237, s84, v237\n"
    "v_mul_f32 v238, s84, v238\n"
    "v_mul_f32 v239, s84, v239\n"
    "v_cvt_pk_bf16_f32 v224, v224, v225\n"
    "v_cvt_pk_bf16_f32 v225, v226, v227\n"
    "v_cvt_pk_bf16_f32 v226, v228, v229\n"
    "v_cvt_pk_bf16_f32 v227, v230, v231\n"
    "v_cvt_pk_bf16_f32 v228, v232, v233\n"
    "v_cvt_pk_bf16_f32 v229, v234, v235\n"
    "v_cvt_pk_bf16_f32 v230, v236, v237\n"
    "v_cvt_pk_bf16_f32 v231, v238, v239\n"
    "ds_write_b128 v90, v[224:227] offset:26624\n"
    "ds_write_b128 v90, v[228:231] offset:27648\n"
    "s_waitcnt lgkmcnt(0)\n"
    "s_barrier\n"
    "s_lshl_b32 s82, s60, 13\n"
    "s_add_u32 s82, s82, 98304\n"
    "v_add_u32 v90, s82, v251\n"
    "ds_read_b128 a[128:131], v90\n"
    "ds_read_b128 a[132:135], v90 offset:1024\n"
    "ds_read_b128 a[136:139], v90 offset:2048\n"
    "ds_read_b128 a[140:143], v90 offset:3072\n"
    "ds_read_b128 a[144:147], v90 offset:4096\n"
    "ds_read_b128 a[148:151], v90 offset:5120\n"
    "ds_read_b128 a[152:155], v90 offset:6144\n"
    "ds_read_b128 a[156:159], v90 offset:7168\n"
    "ds_read_b128 a[160:163], v90 offset:32768\n"
    "ds_read_b128 a[164:167], v90 offset:33792\n"
    "ds_read_b128 a[168:171], v90 offset:34816\n"
    "ds_read_b128 a[172:175], v90 offset:35840\n"
    "ds_read_b128 a[176:179], v90 offset:36864\n"
    "ds_read_b128 a[180:183], v90 offset:37888\n"
    "ds_read_b128 a[184:187], v90 offset:38912\n"
    "ds_read_b128 a[188:191], v90 offset:39936\n"
    "v_and_b32 v206, 31, v250\n"
    "v_lshrrev_b32 v210, 5, v250\n"
    "v_lshlrev_b32 v210, 2, v210\n"
    "v_sub_u32 v206, v206, v210\n"
    "s_lshl_b32 s82, s60, 5\n"
    "v_add_u32 v206, s82, v206\n"
    "v_add_u32 v207, -64, v206\n"
    "s_add_u32 s44, s74, 50331648\n"
    "s_addc_u32 s45, s75, 0\n"
    "s_and_b32 s45, s45, 65535\n"
    "s_mov_b32 s46, 16777216\n"
    "s_mov_b32 s47, 131072\n"
    "s_mov_b32 s90, 0x41000000\n"
    "s_mov_b32 s54, s50\n"
    "s_mov_b32 s80, 0\n"
    "s_add_u32 s79, s94, 65536\n"
    "s_waitcnt vmcnt(8) lgkmcnt(0)\n"
    "s_barrier\n"
    "s_mov_b32 s78, s94\n"
    "s_mov_b32 s91, 0\n"
    "s_mov_b32 s76, 0\n"
    "v_mov_b32 v196, 0\n"
    "v_mov_b32 v197, 0\n"
    "v_mov_b32 v198, 0\n"
    "v_mov_b32 v199, 0\n"
    "v_add_u32 v204, s80, v251\n"
    "v_add_u32 v205, 131072, v251\n"
    "s_add_u32 m0, s94, 98304\n"
    "s_nop 0\n"
    "buffer_load_dwordx4 v251, s[52:55], s78 offen sc1 lds\n"
    "buffer_load_dwordx4 v251, s[52:55], s78 offen offset:1024 sc1 lds\n"
    "buffer_load_dwordx4 v251, s[52:55], s78 offen offset:2048 sc1 lds\n"
    "buffer_load_dwordx4 v251, s[52:55], s78 offen offset:3072 sc1 lds\n"
    "s_add_u32 m0, s94, 102400\n"
    "s_nop 0\n"
    "buffer_load_dwordx4 v248, s[52:55], s78 offen sc1 lds\n"
    "buffer_load_dwordx4 v248, s[52:55], s78 offen offset:1024 sc1 lds\n"
    "buffer_load_dwordx4 v248, s[52:55], s78 offen offset:2048 sc1 lds\n"
    "buffer_load_dwordx4 v248, s[52:55], s78 offen offset:3072 sc1 lds\n"
    "s_add_u32 s78, s78, 32768\n"
    "s_add_u32 s81, s94, 131072\n"
    "ds_read_b128 a[192:195], v204\n"
    "ds_read_b128 a[196:199], v204 offset:1024\n"
    "ds_read_b128 a[200:203], v204 offset:2048\n"
    "ds_read_b128 a[204:207], v204 offset:3072\n"
    "s_waitcnt lgkmcnt(1)\n"
    "v_mfma_f32_32x32x16_bf16 v[0:15], a[192:195], a[128:131], 0\n"
    "ds_read_b128 a[224:227], v204 offset:4096\n"
    "v_accvgpr_write_b32 a0, 0\n"
    "v_accvgpr_write_b32 a1, 0\n"
    "v_accvgpr_write_b32 a2, 0\n"
    "v_accvgpr_write_b32 a3, 0\n"
    "v_mfma_f32_32x32x16_bf16 v[16:31], a[192:195], a[160:163], 0\n"
    "ds_read_b128 a[228:231], v204 offset:5120\n"
    "v_accvgpr_write_b32 a4, 0\n"
    "v_accvgpr_write_b32 a5, 0\n"
    "v_accvgpr_write_b32 a6, 0\n"
    "v_accvgpr_write_b32 a7, 0\n"
    "v_mfma_f32_32x32x16_bf16 v[0:15], a[196:199], a[132:135], v[0:15]\n"
    "ds_read_b128 a[232:235], v204 offset:6144\n"
    "s_add_u32 m0, s79, 0\n"
    "v_accvgpr_write_b32 a8, 0\n"
    "v_accvgpr_write_b32 a9, 0\n"
    "v_accvgpr_write_b32 a10, 0\n"
    "v_mfma_f32_32x32x16_bf16 v[16:31], a[196:199], a[164:167], v[16:31]\n"
    "buffer_load_dwordx4 v251, s[48:51], s77 offen sc1 lds\n"
    "ds_read_b128 a[236:239], v204 offset:7168\n"
    "v_accvgpr_write_b32 a11, 0\n"
    "v_accvgpr_write_b32 a12, 0\n"
    "v_accvgpr_write_b32 a13, 0\n"
    "v_mfma_f32_32x32x16_bf16 v[0:15], a[200:203], a[136:139], v[0:15]\n"
    "v_accvgpr_write_b32 a14, 0\n"
    "v_accvgpr_write_b32 a15, 0\n"
    "v_accvgpr_write_b32 a16, 0\n"
    "v_accvgpr_write_b32 a17, 0\n"
    "v_accvgpr_write_b32 a18, 0\n"
    "v_mfma_f32_32x32x16_bf16 v[16:31], a[200:203], a[168:171], v[16:31]\n"
    "v_accvgpr_write_b32 a19, 0\n"
    "v_accvgpr_write_b32 a20, 0\n"
    "v_accvgpr_write_b32 a21, 0\n"
    "v_accvgpr_write_b32 a22, 0\n"
    "v_accvgpr_write_b32 a23, 0\n"
    "s_waitcnt lgkmcnt(2)\n"
    "v_mfma_f32_32x32x16_bf16 v[0:15], a[204:207], a[140:143], v[0:15]\n"
    "buffer_load_dwordx4 v251, s[48:51], s77 offen offset:1024 sc1 lds\n"
    "v_accvgpr_write_b32 a24, 0\n"
    "v_accvgpr_write_b32 a25, 0\n"
    "v_accvgpr_write_b32 a26, 0\n"
    "v_accvgpr_write_b32 a27, 0\n"
    "v_mfma_f32_32x32x16_bf16 v[16:31], a[204:207], a[172:175], v[16:31]\n"
    "v_accvgpr_write_b32 a28, 0\n"
    "v_accvgpr_write_b32 a29, 0\n"
    "v_accvgpr_write_b32 a30, 0\n"
    "v_accvgpr_write_b32 a31, 0\n"
    "v_accvgpr_write_b32 a32, 0\n"
    "v_mfma_f32_32x32x16_bf16 v[0:15], a[224:227], a[144:147], v[0:15]\n"
    "ds_read_b128 a[192:195], v204 offset:8192\n"
    "v_accvgpr_write_b32 a33, 0\n"
    "v_accvgpr_write_b32 a34, 0\n"
    "v_accvgpr_write_b32 a35, 0\n"
    "v_accvgpr_write_b32 a36, 0\n"
    "v_mfma_f32_32x32x16_bf16 v[16:31], a[224:227], a[176:179], v[16:31]\n"
    "buffer_load_dwordx4 v251, s[48:51], s77 offen offset:2048 sc1 lds\n"
    "ds_read_b128 a[196:199], v204 offset:9216\n"
    "v_accvgpr_write_b32 a37, 0\n"
    "v_accvgpr_write_b32 a38, 0\n"
    "v_accvgpr_write_b32 a39, 0\n"
    "v_mfma_f32_32x32x16_bf16 v[0:15], a[228:231], a[148:151], v[0:15]\n"
    "ds_read_b128 a[200:203], v204 offset:10240\n"
    "v_accvgpr_write_b32 a40, 0\n"
    "v_accvgpr_write_b32 a41, 0\n"
    "v_accvgpr_write_b32 a42, 0\n"
    "v_accvgpr_write_b32 a43, 0\n"
    "v_mfma_f32_32x32x16_bf16 v[16:31], a[228:231], a[180:183], v[16:31]\n"
    "ds_read_b128 a[204:207], v204 offset:11264\n"
    "v_accvgpr_write_b32 a44, 0\n"
    "v_accvgpr_write_b32 a45, 0\n"
    "v_accvgpr_write_b32 a46, 0\n"
    "v_accvgpr_write_b32 a47, 0\n"
    "s_waitcnt lgkmcnt(3)\n"
    "v_mfma_f32_32x32x16_bf16 v[0:15], a[232:235], a[152:155], v[0:15]\n"
    "buffer_load_dwordx4 v251, s[48:51], s77 offen offset:3072 sc1 lds\n"
    "v_accvgpr_write_b32 a48, 0\n"
    "v_accvgpr_write_b32 a49, 0\n"
    "v_accvgpr_write_b32 a50, 0\n"
    "v_accvgpr_write_b32 a51, 0\n"
    "v_mfma_f32_32x32x16_bf16 v[16:31], a[232:235], a[184:187], v[16:31]\n"
    "v_accvgpr_write_b32 a52, 0\n"
    "v_accvgpr_write_b32 a53, 0\n"
    "v_accvgpr_write_b32 a54, 0\n"
    "v_accvgpr_write_b32 a55, 0\n"
    "v_accvgpr_write_b32 a56, 0\n"
    "v_mfma_f32_32x32x16_bf16 v[0:15], a[236:239], a[156:159], v[0:15]\n"
    "s_add_u32 m0, s79, 4096\n"
    "v_accvgpr_write_b32 a57, 0\n"
    "v_accvgpr_write_b32 a58, 0\n"
    "v_accvgpr_write_b32 a59, 0\n"
    "v_accvgpr_write_b32 a60, 0\n"
    "v_mfma_f32_32x32x16_bf16 v[16:31], a[236:239], a[188:191], v[16:31]\n"
    "buffer_load_dwordx4 v248, s[48:51], s77 offen sc1 lds\n"
    "v_accvgpr_write_b32 a61, 0\n"
    "v_accvgpr_write_b32 a62, 0\n"
    "v_accvgpr_write_b32 a63, 0\n"
    "v_accvgpr_write_b32 a64, 0\n"
    "v_mfma_f32_32x32x16_bf16 v[32:47], a[192:195], a[128:131], 0\n"
    "ds_read_b128 a[224:227], v204 offset:12288\n"
    "v_accvgpr_write_b32 a65, 0\n"
    "v_accvgpr_write_b32 a66, 0\n"
    "v_accvgpr_write_b32 a67, 0\n"
    "v_accvgpr_write_b32 a68, 0\n"
    "v_mfma_f32_32x32x16_bf16 v[48:63], a[192:195], a[160:163], 0\n"
    "ds_read_b128 a[228:231], v204 offset:13312\n"
    "v_accvgpr_write_b32 a69, 0\n"
    "v_accvgpr_write_b32 a70, 0\n"
    "v_accvgpr_write_b32 a71, 0\n"
    "v_accvgpr_write_b32 a72, 0\n"
    "s_waitcnt lgkmcnt(2)\n"
    "v_mfma_f32_32x32x16_bf16 v[32:47], a[196:199], a[132:135], v[32:47]\n"
    "buffer_load_dwordx4 v248, s[48:51], s77 offen offset:1024 sc1 lds\n"
    "ds_read_b128 a[232:235], v204 offset:14336\n"
    "v_accvgpr_write_b32 a73, 0\n"
    "v_accvgpr_write_b32 a74, 0\n"
    "v_accvgpr_write_b32 a75, 0\n"
    "v_mfma_f32_32x32x16_bf16 v[48:63], a[196:199], a[164:167], v[48:63]\n"
    "ds_read_b128 a[236:239], v204 offset:15360\n"
    "v_accvgpr_write_b32 a76, 0\n"
    "v_accvgpr_write_b32 a77, 0\n"
    "v_accvgpr_write_b32 a78, 0\n"
    "v_accvgpr_write_b32 a79, 0\n"
    "v_mfma_f32_32x32x16_bf16 v[32:47], a[200:203], a[136:139], v[32:47]\n"
    "v_accvgpr_write_b32 a80, 0\n"
    "v_accvgpr_write_b32 a81, 0\n"
    "v_accvgpr_write_b32 a82, 0\n"
    "v_accvgpr_write_b32 a83, 0\n"
    "v_accvgpr_write_b32 a84, 0\n"
    "v_mfma_f32_32x32x16_bf16 v[48:63], a[200:203], a[168:171], v[48:63]\n"
    "buffer_load_dwordx4 v248, s[48:51], s77 offen offset:2048 sc1 lds\n"
    "v_accvgpr_write_b32 a85, 0\n"
    "v_accvgpr_write_b32 a86, 0\n"
    "v_accvgpr_write_b32 a87, 0\n"
    "v_accvgpr_write_b32 a88, 0\n"
    "v_mfma_f32_32x32x16_bf16 v[32:47], a[204:207], a[140:143], v[32:47]\n"
    "v_accvgpr_write_b32 a89, 0\n"
    "v_accvgpr_write_b32 a90, 0\n"
    "v_accvgpr_write_b32 a91, 0\n"
    "v_accvgpr_write_b32 a92, 0\n"
    "v_accvgpr_write_b32 a93, 0\n"
    "v_mfma_f32_32x32x16_bf16 v[48:63], a[204:207], a[172:175], v[48:63]\n"
    "v_accvgpr_write_b32 a94, 0\n"
    "v_accvgpr_write_b32 a95, 0\n"
    "v_accvgpr_write_b32 a96, 0\n"
    "v_accvgpr_write_b32 a97, 0\n"
    "v_accvgpr_write_b32 a98, 0\n"
    "s_waitcnt lgkmcnt(1)\n"
    "v_mfma_f32_32x32x16_bf16 v[32:47], a[224:227], a[144:147], v[32:47]\n"
    "buffer_load_dwordx4 v248, s[48:51], s77 offen offset:3072 sc1 lds\n"
    "v_accvgpr_write_b32 a99, 0\n"
    "v_accvgpr_write_b32 a100, 0\n"
    "v_accvgpr_write_b32 a101, 0\n"
    "v_accvgpr_write_b32 a102, 0\n"
    "v_mfma_f32_32x32x16_bf16 v[48:63], a[224:227], a[176:179], v[48:63]\n"
    "v_accvgpr_write_b32 a103, 0\n"
    "v_accvgpr_write_b32 a104, 0\n"
    "v_accvgpr_write_b32 a105, 0\n"
    "v_accvgpr_write_b32 a106, 0\n"
    "v_accvgpr_write_b32 a107, 0\n"
    "v_mfma_f32_32x32x16_bf16 v[32:47], a[228:231], a[148:151], v[32:47]\n"
    "v_accvgpr_write_b32 a108, 0\n"
    "v_accvgpr_write_b32 a109, 0\n"
    "v_accvgpr_write_b32 a110, 0\n"
    "v_accvgpr_write_b32 a111, 0\n"
    "v_accvgpr_write_b32 a112, 0\n"
    "v_mfma_f32_32x32x16_bf16 v[48:63], a[228:231], a[180:183], v[48:63]\n"
    "v_accvgpr_write_b32 a113, 0\n"
    "v_accvgpr_write_b32 a114, 0\n"
    "v_accvgpr_write_b32 a115, 0\n"
    "v_accvgpr_write_b32 a116, 0\n"
    "v_accvgpr_write_b32 a117, 0\n"
    "v_mfma_f32_32x32x16_bf16 v[32:47], a[232:235], a[152:155], v[32:47]\n"
    "v_accvgpr_write_b32 a118, 0\n"
    "v_accvgpr_write_b32 a119, 0\n"
    "v_accvgpr_write_b32 a120, 0\n"
    "v_accvgpr_write_b32 a121, 0\n"
    "v_accvgpr_write_b32 a122, 0\n"
    "v_mfma_f32_32x32x16_bf16 v[48:63], a[232:235], a[184:187], v[48:63]\n"
    "v_accvgpr_write_b32 a123, 0\n"
    "v_accvgpr_write_b32 a124, 0\n"
    "v_accvgpr_write_b32 a125, 0\n"
    "v_accvgpr_write_b32 a126, 0\n"
    "v_accvgpr_write_b32 a127, 0\n"
    "s_waitcnt lgkmcnt(0)\n"
    "v_mfma_f32_32x32x16_bf16 v[32:47], a[236:239], a[156:159], v[32:47]\n"
    "v_mfma_f32_32x32x16_bf16 v[48:63], a[236:239], a[188:191], v[48:63]\n"
    "s_add_u32 s77, s77, 32768\n"
    "v_xor_b32 v205, 229376, v205\n"
    "s_nop 5\n"
    "s_cmp_lg_u32 s62, 0\n"
    "s_cbranch_scc1 L_nomask_%=\n"
    "v_mov_b32 v210, 4286578688\n"
    "v_cmp_gt_i32 s[82:83], 0, v206\n"
    "v_cmp_gt_i32 s[84:85], 1, v206\n"
    "v_cmp_gt_i32 s[86:87], 2, v206\n"
    "v_cndmask_b32 v0, v0, v210, s[82:83]\n"
    "v_cmp_gt_i32 s[82:83], 3, v206\n"
    "v_cndmask_b32 v1, v1, v210, s[84:85]\n"
    "v_cmp_gt_i32 s[84:85], 8, v206\n"
    "v_cndmask_b32 v2, v2, v210, s[86:87]\n"
    "v_cmp_gt_i32 s[86:87], 9, v206\n"
    "v_cndmask_b32 v3, v3, v210, s[82:83]\n"
    "v_cmp_gt_i32 s[82:83], 10, v206\n"
    "v_cndmask_b32 v4, v4, v210, s[84:85]\n"
    "v_cmp_gt_i32 s[84:85], 11, v206\n"
    "v_cndmask_b32 v5, v5, v210, s[86:87]\n"
    "v_cmp_gt_i32 s[86:87], 16, v206\n"
    "v_cndmask_b32 v6, v6, v210, s[82:83]\n"
    "v_cmp_gt_i32 s[82:83], 17, v206\n"
    "v_cndmask_b32 v7, v7, v210, s[84:85]\n"
    "v_cmp_gt_i32 s[84:85], 18, v206\n"
    "v_cndmask_b32 v8, v8, v210, s[86:87]\n"
    "v_cmp_gt_i32 s[86:87], 19, v206\n"
    "v_cndmask_b32 v9, v9, v210, s[82:83]\n"
    "v_cmp_gt_i32 s[82:83], 24, v206\n"
    "v_cndmask_b32 v10, v10, v210, s[84:85]\n"
    "v_cmp_gt_i32 s[84:85], 25, v206\n"
    "v_cndmask_b32 v11, v11, v210, s[86:87]\n"
    "v_cmp_gt_i32 s[86:87], 26, v206\n"
    "v_cndmask_b32 v12, v12, v210, s[82:83]\n"
    "v_cmp_gt_i32 s[82:83], 27, v206\n"
    "v_cndmask_b32 v13, v13, v210, s[84:85]\n"
    "s_nop 0\n"
    "v_cndmask_b32 v14, v14, v210, s[86:87]\n"
    "s_nop 0\n"
    "v_cndmask_b32 v15, v15, v210, s[82:83]\n"
    "v_mov_b32 v210, 4286578688\n"
    "v_cmp_gt_i32 s[82:83], 32, v206\n"
    "v_cmp_gt_i32 s[84:85], 33, v206\n"
    "v_cmp_gt_i32 s[86:87], 34, v206\n"
    "v_cndmask_b32 v32, v32, v210, s[82:83]\n"
    "v_cmp_gt_i32 s[82:83], 35, v206\n"
    "v_cndmask_b32 v33, v33, v210, s[84:85]\n"
    "v_cmp_gt_i32 s[84:85], 40, v206\n"
    "v_cndmask_b32 v34, v34, v210, s[86:87]\n"
    "v_cmp_gt_i32 s[86:87], 41, v206\n"
    "v_cndmask_b32 v35, v35, v210, s[82:83]\n"
    "v_cmp_gt_i32 s[82:83], 42, v206\n"
    "v_cndmask_b32 v36, v36, v210, s[84:85]\n"
    "v_cmp_gt_i32 s[84:85], 43, v206\n"
    "v_cndmask_b32 v37, v37, v210, s[86:87]\n"
    "v_cmp_gt_i32 s[86:87], 48, v206\n"
    "v_cndmask_b32 v38, v38, v210, s[82:83]\n"
    "v_cmp_gt_i32 s[82:83], 49, v206\n"
    "v_cndmask_b32 v39, v39, v210, s[84:85]\n"
    "v_cmp_gt_i32 s[84:85], 50, v206\n"
    "v_cndmask_b32 v40, v40, v210, s[86:87]\n"
    "v_cmp_gt_i32 s[86:87], 51, v206\n"
    "v_cndmask_b32 v41, v41, v210, s[82:83]\n"
    "v_cmp_gt_i32 s[82:83], 56, v206\n"
    "v_cndmask_b32 v42, v42, v210, s[84:85]\n"
    "v_cmp_gt_i32 s[84:85], 57, v206\n"
    "v_cndmask_b32 v43, v43, v210, s[86:87]\n"
    "v_cmp_gt_i32 s[86:87], 58, v206\n"
    "v_cndmask_b32 v44, v44, v210, s[82:83]\n"
    "v_cmp_gt_i32 s[82:83], 59, v206\n"
    "v_cndmask_b32 v45, v45, v210, s[84:85]\n"
    "s_nop 0\n"
    "v_cndmask_b32 v46, v46, v210, s[86:87]\n"
    "s_nop 0\n"
    "v_cndmask_b32 v47, v47, v210, s[82:83]\n"
    "L_nomask_%=:\n"
    "v_max3_f32 v192, v0, v1, v2\n"
    "v_max3_f32 v192, v192, v3, v4\n"
    "v_max3_f32 v192, v192, v5, v6\n"
    "v_max3_f32 v192, v192, v7, v8\n"
    "v_max3_f32 v192, v192, v9, v10\n"
    "v_max3_f32 v192, v192, v11, v12\n"
    "v_max3_f32 v192, v192, v13, v14\n"
    "v_max3_f32 v192, v192, v15, v32\n"
    "v_max3_f32 v192, v192, v33, v34\n"
    "v_max3_f32 v192, v192, v35, v36\n"
    "v_max3_f32 v192, v192, v37, v38\n"
    "v_max3_f32 v192, v192, v39, v40\n"
    "v_max3_f32 v192, v192, v41, v42\n"
    "v_max3_f32 v192, v192, v43, v44\n"
    "v_max3_f32 v192, v192, v45, v46\n"
    "v_max_f32 v192, v192, v47\n"
    "v_max3_f32 v193, v16, v17, v18\n"
    "v_max3_f32 v193, v193, v19, v20\n"
    "v_max3_f32 v193, v193, v21, v22\n"
    "v_max3_f32 v193, v193, v23, v24\n"
    "v_max3_f32 v193, v193, v25, v26\n"
    "v_max3_f32 v193, v193, v27, v28\n"
    "v_max3_f32 v193, v193, v29, v30\n"
    "v_max3_f32 v193, v193, v31, v48\n"
    "v_max3_f32 v193, v193, v49, v50\n"
    "v_max3_f32 v193, v193, v51, v52\n"
    "v_max3_f32 v193, v193, v53, v54\n"
    "v_max3_f32 v193, v193, v55, v56\n"
    "v_max3_f32 v193, v193, v57, v58\n"
    "v_max3_f32 v193, v193, v59, v60\n"
    "v_max3_f32 v193, v193, v61, v62\n"
    "v_max_f32 v193, v193, v63\n"
    "v_mov_b32 v211, v192\n"
    "s_nop 1\n"
    "v_permlane32_swap_b32 v211, v192\n"
    "s_nop 0\n"
    "v_max_f32 v200, v211, v192\n"
    "v_max_f32 v200, 0xf149f2ca, v200\n"
    "v_mov_b32 v211, v193\n"
    "s_nop 1\n"
    "v_permlane32_swap_b32 v211, v193\n"
    "s_nop 0\n"
    "v_max_f32 v201, v211, v193\n"
    "v_max_f32 v201, 0xf149f2ca, v201\n"
    "v_sub_f32 v0, v0, v200\n"
    "v_sub_f32 v1, v1, v200\n"
    "v_sub_f32 v2, v2, v200\n"
    "v_sub_f32 v3, v3, v200\n"
    "v_sub_f32 v4, v4, v200\n"
    "v_sub_f32 v5, v5, v200\n"
    "v_sub_f32 v6, v6, v200\n"
    "v_sub_f32 v7, v7, v200\n"
    "v_sub_f32 v8, v8, v200\n"
    "v_sub_f32 v9, v9, v200\n"
    "v_sub_f32 v10, v10, v200\n"
    "v_sub_f32 v11, v11, v200\n"
    "v_sub_f32 v12, v12, v200\n"
    "v_sub_f32 v13, v13, v200\n"
    "v_sub_f32 v14, v14, v200\n"
    "v_sub_f32 v15, v15, v200\n"
    "v_sub_f32 v32, v32, v200\n"
    "v_sub_f32 v33, v33, v200\n"
    "v_sub_f32 v34, v34, v200\n"
    "v_sub_f32 v35, v35, v200\n"
    "v_sub_f32 v36, v36, v200\n"
    "v_sub_f32 v37, v37, v200\n"
    "v_sub_f32 v38, v38, v200\n"
    "v_sub_f32 v39, v39, v200\n"
    "v_sub_f32 v40, v40, v200\n"
    "v_sub_f32 v41, v41, v200\n"
    "v_sub_f32 v42, v42, v200\n"
    "v_sub_f32 v43, v43, v200\n"
    "v_sub_f32 v44, v44, v200\n"
    "v_sub_f32 v45, v45, v200\n"
    "v_sub_f32 v46, v46, v200\n"
    "v_sub_f32 v47, v47, v200\n"
    "v_sub_f32 v160, 0, v200\n"
    "v_sub_f32 v161, 0, v200\n"
    "v_sub_f32 v162, 0, v200\n"
    "v_sub_f32 v163, 0, v200\n"
    "v_sub_f32 v164, 0, v200\n"
    "v_sub_f32 v165, 0, v200\n"
    "v_sub_f32 v166, 0, v200\n"
    "v_sub_f32 v167, 0, v200\n"
    "v_sub_f32 v168, 0, v200\n"
    "v_sub_f32 v169, 0, v200\n"
    "v_sub_f32 v170, 0, v200\n"
    "v_sub_f32 v171, 0, v200\n"
    "v_sub_f32 v172, 0, v200\n"
    "v_sub_f32 v173, 0, v200\n"
    "v_sub_f32 v174, 0, v200\n"
    "v_sub_f32 v175, 0, v200\n"
    "v_sub_f32 v16, v16, v201\n"
    "v_sub_f32 v17, v17, v201\n"
    "v_sub_f32 v18, v18, v201\n"
    "v_sub_f32 v19, v19, v201\n"
    "v_sub_f32 v20, v20, v201\n"
    "v_sub_f32 v21, v21, v201\n"
    "v_sub_f32 v22, v22, v201\n"
    "v_sub_f32 v23, v23, v201\n"
    "v_sub_f32 v24, v24, v201\n"
    "v_sub_f32 v25, v25, v201\n"
    "v_sub_f32 v26, v26, v201\n"
    "v_sub_f32 v27, v27, v201\n"
    "v_sub_f32 v28, v28, v201\n"
    "v_sub_f32 v29, v29, v201\n"
    "v_sub_f32 v30, v30, v201\n"
    "v_sub_f32 v31, v31, v201\n"
    "v_sub_f32 v48, v48, v201\n"
    "v_sub_f32 v49, v49, v201\n"
    "v_sub_f32 v50, v50, v201\n"
    "v_sub_f32 v51, v51, v201\n"
    "v_sub_f32 v52, v52, v201\n"
    "v_sub_f32 v53, v53, v201\n"
    "v_sub_f32 v54, v54, v201\n"
    "v_sub_f32 v55, v55, v201\n"
    "v_sub_f32 v56, v56, v201\n"
    "v_sub_f32 v57, v57, v201\n"
    "v_sub_f32 v58, v58, v201\n"
    "v_sub_f32 v59, v59, v201\n"
    "v_sub_f32 v60, v60, v201\n"
    "v_sub_f32 v61, v61, v201\n"
    "v_sub_f32 v62, v62, v201\n"
    "v_sub_f32 v63, v63, v201\n"
    "v_sub_f32 v176, 0, v201\n"
    "v_sub_f32 v177, 0, v201\n"
    "v_sub_f32 v178, 0, v201\n"
    "v_sub_f32 v179, 0, v201\n"
    "v_sub_f32 v180, 0, v201\n"
    "v_sub_f32 v181, 0, v201\n"
    "v_sub_f32 v182, 0, v201\n"
    "v_sub_f32 v183, 0, v201\n"
    "v_sub_f32 v184, 0, v201\n"
    "v_sub_f32 v185, 0, v201\n"
    "v_sub_f32 v186, 0, v201\n"
    "v_sub_f32 v187, 0, v201\n"
    "v_sub_f32 v188, 0, v201\n"
    "v_sub_f32 v189, 0, v201\n"
    "v_sub_f32 v190, 0, v201\n"
    "v_sub_f32 v191, 0, v201\n"
    "v_exp_f32 v0, v0\n"
    "v_exp_f32 v1, v1\n"
    "v_add_f32 v196, v196, v0\n"
    "v_exp_f32 v2, v2\n"
    "v_add_f32 v197, v197, v1\n"
    "v_exp_f32 v3, v3\n"
    "v_add_f32 v196, v196, v2\n"
    "v_exp_f32 v4, v4\n"
    "v_add_f32 v197, v197, v3\n"
    "v_exp_f32 v5, v5\n"
    "v_add_f32 v196, v196, v4\n"
    "v_exp_f32 v6, v6\n"
    "v_add_f32 v197, v197, v5\n"
    "v_exp_f32 v7, v7\n"
    "v_add_f32 v196, v196, v6\n"
    "v_exp_f32 v16, v16\n"
    "v_add_f32 v197, v197, v7\n"
    "v_exp_f32 v17, v17\n"
    "v_add_f32 v198, v198, v16\n"
    "v_exp_f32 v18, v18\n"
    "v_add_f32 v199, v199, v17\n"
    "v_exp_f32 v19, v19\n"
    "v_add_f32 v198, v198, v18\n"
    "v_exp_f32 v20, v20\n"
    "v_add_f32 v199, v199, v19\n"
    "v_exp_f32 v21, v21\n"
    "v_add_f32 v198, v198, v20\n"
    "v_exp_f32 v22, v22\n"
    "v_add_f32 v199, v199, v21\n"
    "v_exp_f32 v23, v23\n"
    "v_add_f32 v198, v198, v22\n"
    "v_exp_f32 v8, v8\n"
    "v_add_f32 v199, v199, v23\n"
    "v_exp_f32 v9, v9\n"
    "v_add_f32 v196, v196, v8\n"
    "v_exp_f32 v10, v10\n"
    "v_add_f32 v197, v197, v9\n"
    "v_exp_f32 v11, v11\n"
    "v_add_f32 v196, v196, v10\n"
    "v_exp_f32 v12, v12\n"
    "v_add_f32 v197, v197, v11\n"
    "v_exp_f32 v13, v13\n"
    "v_add_f32 v196, v196, v12\n"
    "v_exp_f32 v14, v14\n"
    "v_add_f32 v197, v197, v13\n"
    "v_exp_f32 v15, v15\n"
    "v_add_f32 v196, v196, v14\n"
    "v_exp_f32 v24, v24\n"
    "v_add_f32 v197, v197, v15\n"
    "v_exp_f32 v25, v25\n"
    "v_add_f32 v198, v198, v24\n"
    "v_exp_f32 v26, v26\n"
    "v_add_f32 v199, v199, v25\n"
    "v_exp_f32 v27, v27\n"
    "v_add_f32 v198, v198, v26\n"
    "v_exp_f32 v28, v28\n"
    "v_add_f32 v199, v199, v27\n"
    "v_exp_f32 v29, v29\n"
    "v_add_f32 v198, v198, v28\n"
    "v_exp_f32 v30, v30\n"
    "v_add_f32 v199, v199, v29\n"
    "v_exp_f32 v31, v31\n"
    "v_add_f32 v198, v198, v30\n"
    "s_nop 0\n"
    "v_add_f32 v199, v199, v31\n"
    "s_waitcnt vmcnt(8) lgkmcnt(0)\n"
    "s_barrier\n"
    "ds_read_b128 a[192:195], v204 offset:16384\n"
    "ds_read_b128 a[196:199], v204 offset:17408\n"
    "ds_read_b128 a[200:203], v204 offset:18432\n"
    "ds_read_b128 a[204:207], v204 offset:19456\n"
    "s_cmp_eq_u32 s76, s62\n"
    "s_cselect_b32 s88, 1, 0\n"
    "s_branch L_b_%=\n"
    "L_a_%=:\n"
    "s_cmp_eq_u32 s76, s62\n"
    "s_cselect_b32 s88, 1, 0\n"
    "s_waitcnt lgkmcnt(5)\n"
    "v_mfma_f32_32x32x16_bf16 v[0:15], a[192:195], a[128:131], v[160:175]\n"
    "ds_read_b128 a[224:227], v204 offset:4096\n"
    "v_exp_f32 v96, v96\n"
    "v_exp_f32 v97, v97\n"
    "v_cvt_pk_bf16_f32 v128, v64, v65\n"
    "v_mfma_f32_32x32x16_bf16 v[16:31], a[192:195], a[160:163], v[176:191]\n"
    "ds_read_b128 a[228:231], v204 offset:5120\n"
    "v_exp_f32 v98, v98\n"
    "v_cvt_pk_bf16_f32 v129, v66, v67\n"
    "v_cvt_pk_bf16_f32 v130, v68, v69\n"
    "v_cvt_pk_bf16_f32 v131, v70, v71\n"
    "v_mfma_f32_32x32x16_bf16 v[0:15], a[196:199], a[132:135], v[0:15]\n"
    "ds_read_b128 a[232:235], v204 offset:6144\n"
    "v_exp_f32 v99, v99\n"
    "v_cvt_pk_bf16_f32 v144, v80, v81\n"
    "v_cvt_pk_bf16_f32 v145, v82, v83\n"
    "s_add_u32 m0, s79, 0\n"
    "v_mfma_f32_32x32x16_bf16 v[16:31], a[196:199], a[164:167], v[16:31]\n"
    "buffer_load_dwordx4 v251, s[48:51], s77 offen sc1 lds\n"
    "ds_read_b128 a[236:239], v204 offset:7168\n"
    "v_exp_f32 v100, v100\n"
    "v_cvt_pk_bf16_f32 v146, v84, v85\n"
    "v_cvt_pk_bf16_f32 v147, v86, v87\n"
    "v_mfma_f32_32x32x16_bf16 v[0:15], a[200:203], a[136:139], v[0:15]\n"
    "v_add_f32 v196, v196, v96\n"
    "v_add_f32 v197, v197, v97\n"
    "v_add_f32 v196, v196, v98\n"
    "v_add_f32 v197, v197, v99\n"
    "v_exp_f32 v101, v101\n"
    "v_mfma_f32_32x32x16_bf16 v[16:31], a[200:203], a[168:171], v[16:31]\n"
    "v_add_f32 v196, v196, v100\n"
    "v_add_f32 v197, v197, v101\n"
    "v_exp_f32 v102, v102\n"
    "v_cvt_pk_bf16_f32 v132, v72, v73\n"
    "v_cvt_pk_bf16_f32 v133, v74, v75\n"
    "s_waitcnt lgkmcnt(6)\n"
    "v_mfma_f32_32x32x16_bf16 v[0:15], a[204:207], a[140:143], v[0:15]\n"
    "v_add_f32 v196, v196, v102\n"
    "v_exp_f32 v103, v103\n"
    "v_cvt_pk_bf16_f32 v134, v76, v77\n"
    "v_cvt_pk_bf16_f32 v135, v78, v79\n"
    "v_cvt_pk_bf16_f32 v148, v88, v89\n"
    "v_mfma_f32_32x32x16_bf16 v[16:31], a[204:207], a[172:175], v[16:31]\n"
    "v_add_f32 v197, v197, v103\n"
    "v_exp_f32 v112, v112\n"
    "v_cvt_pk_bf16_f32 v149, v90, v91\n"
    "v_cvt_pk_bf16_f32 v150, v92, v93\n"
    "v_cvt_pk_bf16_f32 v151, v94, v95\n"
    "s_waitcnt lgkmcnt(1)\n"
    "v_mfma_f32_32x32x16_bf16 v[0:15], a[224:227], a[144:147], v[0:15]\n"
    "ds_read_b128 a[192:195], v204 offset:8192\n"
    "v_add_f32 v198, v198, v112\n"
    "v_exp_f32 v113, v113\n"
    "v_cvt_pk_bf16_f32 v136, v96, v97\n"
    "v_cvt_pk_bf16_f32 v137, v98, v99\n"
    "v_mfma_f32_32x32x16_bf16 v[16:31], a[224:227], a[176:179], v[16:31]\n"
    "buffer_load_dwordx4 v251, s[48:51], s77 offen offset:1024 sc1 lds\n"
    "ds_read_b128 a[196:199], v204 offset:9216\n"
    "v_exp_f32 v114, v114\n"
    "v_exp_f32 v115, v115\n"
    "v_mfma_f32_32x32x16_bf16 v[0:15], a[228:231], a[148:151], v[0:15]\n"
    "ds_read_b128 a[200:203], v204 offset:10240\n"
    "v_add_f32 v199, v199, v113\n"
    "v_add_f32 v198, v198, v114\n"
    "v_add_f32 v199, v199, v115\n"
    "v_exp_f32 v116, v116\n"
    "v_mfma_f32_32x32x16_bf16 v[16:31], a[228:231], a[180:183], v[16:31]\n"
    "ds_read_b128 a[204:207], v204 offset:11264\n"
    "v_add_f32 v198, v198, v116\n"
    "v_exp_f32 v117, v117\n"
    "v_cvt_pk_bf16_f32 v138, v100, v101\n"
    "v_cvt_pk_bf16_f32 v139, v102, v103\n"
    "v_mfma_f32_32x32x16_bf16 v[0:15], a[232:235], a[152:155], v[0:15]\n"
    "v_add_f32 v199, v199, v117\n"
    "v_exp_f32 v118, v118\n"
    "v_cvt_pk_bf16_f32 v152, v112, v113\n"
    "v_cvt_pk_bf16_f32 v153, v114, v115\n"
    "v_cvt_pk_bf16_f32 v154, v116, v117\n"
    "v_mfma_f32_32x32x16_bf16 v[16:31], a[232:235], a[184:187], v[16:31]\n"
    "v_add_f32 v198, v198, v118\n"
    "v_exp_f32 v119, v119\n"
    "s_waitcnt lgkmcnt(2)\n"
    "v_mfma_f32_32x32x16_bf16 v[0:15], a[236:239], a[156:159], v[0:15]\n"
    "v_add_f32 v199, v199, v119\n"
    "v_exp_f32 v104, v104\n"
    "v_cvt_pk_bf16_f32 v155, v118, v119\n"
    "v_mfma_f32_32x32x16_bf16 v[16:31], a[236:239], a[188:191], v[16:31]\n"
    "buffer_load_dwordx4 v251, s[48:51], s77 offen offset:2048 sc1 lds\n"
    "v_add_f32 v196, v196, v104\n"
    "v_exp_f32 v105, v105\n"
    "v_mfma_f32_32x32x16_bf16 v[32:47], a[192:195], a[128:131], v[160:175]\n"
    "ds_read_b128 a[224:227], v204 offset:12288\n"
    "v_add_f32 v197, v197, v105\n"
    "v_exp_f32 v106, v106\n"
    "v_cvt_pk_bf16_f32 v140, v104, v105\n"
    "v_mfma_f32_32x32x16_bf16 v[48:63], a[192:195], a[160:163], v[176:191]\n"
    "ds_read_b128 a[228:231], v204 offset:13312\n"
    "v_add_f32 v196, v196, v106\n"
    "v_exp_f32 v107, v107\n"
    "v_mfma_f32_32x32x16_bf16 v[32:47], a[196:199], a[132:135], v[32:47]\n"
    "ds_read_b128 a[232:235], v204 offset:14336\n"
    "v_add_f32 v197, v197, v107\n"
    "v_exp_f32 v108, v108\n"
    "v_exp_f32 v109, v109\n"
    "v_mfma_f32_32x32x16_bf16 v[48:63], a[196:199], a[164:167], v[48:63]\n"
    "ds_read_b128 a[236:239], v204 offset:15360\n"
    "v_add_f32 v196, v196, v108\n"
    "v_add_f32 v197, v197, v109\n"
    "v_exp_f32 v110, v110\n"
    "v_cvt_pk_bf16_f32 v141, v106, v107\n"
    "s_waitcnt lgkmcnt(3)\n"
    "v_mfma_f32_32x32x16_bf16 v[32:47], a[200:203], a[136:139], v[32:47]\n"
    "v_add_f32 v196, v196, v110\n"
    "v_exp_f32 v111, v111\n"
    "s_cmp_lg_u32 s88, 0\n"
    "s_cbranch_scc1 L_a_m0_%=\n"
    "L_a_m0_%=r:\n"
    "v_mfma_f32_32x32x16_bf16 v[48:63], a[200:203], a[168:171], v[48:63]\n"
    "buffer_load_dwordx4 v251, s[48:51], s77 offen offset:3072 sc1 lds\n"
    "v_exp_f32 v120, v120\n"
    "v_max3_f32 v192, v0, v1, v2\n"
    "v_max3_f32 v192, v192, v3, v4\n"
    "v_max3_f32 v192, v192, v5, v6\n"
    "v_mfma_f32_32x32x16_bf16 v[32:47], a[204:207], a[140:143], v[32:47]\n"
    "v_exp_f32 v121, v121\n"
    "v_max3_f32 v192, v192, v7, v8\n"
    "v_max3_f32 v192, v192, v9, v10\n"
    "v_max3_f32 v192, v192, v11, v12\n"
    "v_max3_f32 v192, v192, v13, v14\n"
    "v_mfma_f32_32x32x16_bf16 v[48:63], a[204:207], a[172:175], v[48:63]\n"
    "v_exp_f32 v122, v122\n"
    "v_max_f32 v192, v192, v15\n"
    "v_max3_f32 v193, v16, v17, v18\n"
    "v_max3_f32 v193, v193, v19, v20\n"
    "v_max3_f32 v193, v193, v21, v22\n"
    "v_mfma_f32_32x32x16_bf16 v[32:47], a[224:227], a[144:147], v[32:47]\n"
    "ds_read_b128 a[192:195], v205 offset:16384\n"
    "v_exp_f32 v123, v123\n"
    "v_max3_f32 v193, v193, v23, v24\n"
    "v_max3_f32 v193, v193, v25, v26\n"
    "v_max3_f32 v193, v193, v27, v28\n"
    "v_mfma_f32_32x32x16_bf16 v[48:63], a[224:227], a[176:179], v[48:63]\n"
    "ds_read_b128 a[196:199], v205 offset:18432\n"
    "v_add_f32 v197, v197, v111\n"
    "v_exp_f32 v124, v124\n"
    "v_max3_f32 v193, v193, v29, v30\n"
    "v_max_f32 v193, v193, v31\n"
    "s_waitcnt lgkmcnt(2)\n"
    "v_mfma_f32_32x32x16_bf16 v[32:47], a[228:231], a[148:151], v[32:47]\n"
    "ds_read_b128 a[200:203], v205 offset:20480\n"
    "v_add_f32 v198, v198, v120\n"
    "v_add_f32 v199, v199, v121\n"
    "v_exp_f32 v125, v125\n"
    "s_add_u32 m0, s79, 4096\n"
    "v_mfma_f32_32x32x16_bf16 v[48:63], a[228:231], a[180:183], v[48:63]\n"
    "buffer_load_dwordx4 v248, s[48:51], s77 offen sc1 lds\n"
    "ds_read_b128 a[204:207], v205 offset:22528\n"
    "v_exp_f32 v126, v126\n"
    "v_exp_f32 v127, v127\n"
    "v_mfma_f32_32x32x16_bf16 v[32:47], a[232:235], a[152:155], v[32:47]\n"
    "v_add_f32 v198, v198, v122\n"
    "v_add_f32 v199, v199, v123\n"
    "v_add_f32 v198, v198, v124\n"
    "v_add_f32 v199, v199, v125\n"
    "v_add_f32 v198, v198, v126\n"
    "v_mfma_f32_32x32x16_bf16 v[48:63], a[232:235], a[184:187], v[48:63]\n"
    "v_add_f32 v199, v199, v127\n"
    "v_cvt_pk_bf16_f32 v142, v108, v109\n"
    "v_cvt_pk_bf16_f32 v143, v110, v111\n"
    "v_cvt_pk_bf16_f32 v156, v120, v121\n"
    "v_cvt_pk_bf16_f32 v157, v122, v123\n"
    "v_mfma_f32_32x32x16_bf16 v[32:47], a[236:239], a[156:159], v[32:47]\n"
    "v_cvt_pk_bf16_f32 v158, v124, v125\n"
    "v_cvt_pk_bf16_f32 v159, v126, v127\n"
    "v_mfma_f32_32x32x16_bf16 v[48:63], a[236:239], a[188:191], v[48:63]\n"
    "s_waitcnt lgkmcnt(1)\n"
    "v_mfma_f32_32x32x16_bf16 a[0:15], a[192:195], v[128:131], a[0:15]\n"
    "ds_read_b128 a[224:227], v205 offset:17408\n"
    "v_mfma_f32_32x32x16_bf16 a[16:31], a[192:195], v[144:147], a[16:31]\n"
    "buffer_load_dwordx4 v248, s[48:51], s77 offen offset:1024 sc1 lds\n"
    "ds_read_b128 a[228:231], v205 offset:19456\n"
    "v_mfma_f32_32x32x16_bf16 a[32:47], a[196:199], v[128:131], a[32:47]\n"
    "ds_read_b128 a[232:235], v205 offset:21504\n"
    "v_mfma_f32_32x32x16_bf16 a[48:63], a[196:199], v[144:147], a[48:63]\n"
    "ds_read_b128 a[236:239], v205 offset:23552\n"
    "v_mfma_f32_32x32x16_bf16 a[64:79], a[200:203], v[128:131], a[64:79]\n"
    "s_cmp_lg_u32 s88, 0\n"
    "s_cbranch_scc1 L_a_m1_%=\n"
    "L_a_m1_%=r:\n"
    "v_mfma_f32_32x32x16_bf16 a[80:95], a[200:203], v[144:147], a[80:95]\n"
    "v_max3_f32 v192, v192, v32, v33\n"
    "v_max3_f32 v192, v192, v34, v35\n"
    "v_max3_f32 v192, v192, v36, v37\n"
    "v_max3_f32 v192, v192, v38, v39\n"
    "v_max3_f32 v192, v192, v40, v41\n"
    "s_waitcnt lgkmcnt(2)\n"
    "v_mfma_f32_32x32x16_bf16 a[96:111], a[204:207], v[128:131], a[96:111]\n"
    "v_max3_f32 v192, v192, v42, v43\n"
    "v_max3_f32 v192, v192, v44, v45\n"
    "v_max3_f32 v192, v192, v46, v47\n"
    "v_max3_f32 v193, v193, v48, v49\n"
    "v_max3_f32 v193, v193, v50, v51\n"
    "v_mfma_f32_32x32x16_bf16 a[112:127], a[204:207], v[144:147], a[112:127]\n"
    "buffer_load_dwordx4 v248, s[48:51], s77 offen offset:2048 sc1 lds\n"
    "v_max3_f32 v193, v193, v52, v53\n"
    "v_max3_f32 v193, v193, v54, v55\n"
    "v_max3_f32 v193, v193, v56, v57\n"
    "v_max3_f32 v193, v193, v58, v59\n"
    "v_mfma_f32_32x32x16_bf16 a[0:15], a[224:227], v[132:135], a[0:15]\n"
    "ds_read_b128 a[192:195], v205 offset:24576\n"
    "v_max3_f32 v193, v193, v60, v61\n"
    "v_max3_f32 v193, v193, v62, v63\n"
    "v_max_f32 v194, v192, v193\n"
    "v_cmp_lt_f32 vcc, s90, v194\n"
    "v_mfma_f32_32x32x16_bf16 a[16:31], a[224:227], v[148:151], a[16:31]\n"
    "ds_read_b128 a[196:199], v205 offset:26624\n"
    "s_cbranch_vccnz L_a_ra_%=\n"
    "L_a_ra_%=r:\n"
    "v_mfma_f32_32x32x16_bf16 a[32:47], a[228:231], v[132:135], a[32:47]\n"
    "ds_read_b128 a[200:203], v205 offset:28672\n"
    "v_exp_f32 v0, v0\n"
    "v_exp_f32 v1, v1\n"
    "v_mfma_f32_32x32x16_bf16 a[48:63], a[228:231], v[148:151], a[48:63]\n"
    "ds_read_b128 a[204:207], v205 offset:30720\n"
    "v_add_f32 v196, v196, v0\n"
    "v_exp_f32 v2, v2\n"
    "v_exp_f32 v3, v3\n"
    "s_waitcnt lgkmcnt(3)\n"
    "v_mfma_f32_32x32x16_bf16 a[64:79], a[232:235], v[132:135], a[64:79]\n"
    "v_add_f32 v197, v197, v1\n"
    "v_add_f32 v196, v196, v2\n"
    "v_add_f32 v197, v197, v3\n"
    "v_exp_f32 v4, v4\n"
    "v_mfma_f32_32x32x16_bf16 a[80:95], a[232:235], v[148:151], a[80:95]\n"
    "buffer_load_dwordx4 v248, s[48:51], s77 offen offset:3072 sc1 lds\n"
    "v_add_f32 v196, v196, v4\n"
    "v_exp_f32 v5, v5\n"
    "v_exp_f32 v6, v6\n"
    "v_mfma_f32_32x32x16_bf16 a[96:111], a[236:239], v[132:135], a[96:111]\n"
    "v_add_f32 v197, v197, v5\n"
    "v_add_f32 v196, v196, v6\n"
    "v_exp_f32 v7, v7\n"
    "v_exp_f32 v16, v16\n"
    "v_mfma_f32_32x32x16_bf16 a[112:127], a[236:239], v[148:151], a[112:127]\n"
    "v_add_f32 v197, v197, v7\n"
    "v_add_f32 v198, v198, v16\n"
    "v_exp_f32 v17, v17\n"
    "s_add_u32 s77, s77, 32768\n"
    "v_mfma_f32_32x32x16_bf16 a[0:15], a[192:195], v[136:139], a[0:15]\n"
    "ds_read_b128 a[224:227], v205 offset:25600\n"
    "v_add_f32 v199, v199, v17\n"
    "v_exp_f32 v18, v18\n"
    "v_exp_f32 v19, v19\n"
    "v_mfma_f32_32x32x16_bf16 a[16:31], a[192:195], v[152:155], a[16:31]\n"
    "ds_read_b128 a[228:231], v205 offset:27648\n"
    "v_add_f32 v198, v198, v18\n"
    "v_add_f32 v199, v199, v19\n"
    "v_exp_f32 v20, v20\n"
    "s_waitcnt lgkmcnt(2)\n"
    "v_mfma_f32_32x32x16_bf16 a[32:47], a[196:199], v[136:139], a[32:47]\n"
    "ds_read_b128 a[232:235], v205 offset:29696\n"
    "v_add_f32 v198, v198, v20\n"
    "v_exp_f32 v21, v21\n"
    "v_exp_f32 v22, v22\n"
    "v_mfma_f32_32x32x16_bf16 a[48:63], a[196:199], v[152:155], a[48:63]\n"
    "ds_read_b128 a[236:239], v205 offset:31744\n"
    "v_add_f32 v199, v199, v21\n"
    "v_exp_f32 v23, v23\n"
    "v_exp_f32 v8, v8\n"
    "v_mfma_f32_32x32x16_bf16 a[64:79], a[200:203], v[136:139], a[64:79]\n"
    "v_add_f32 v198, v198, v22\n"
    "v_add_f32 v199, v199, v23\n"
    "v_add_f32 v196, v196, v8\n"
    "v_exp_f32 v9, v9\n"
    "v_mfma_f32_32x32x16_bf16 a[80:95], a[200:203], v[152:155], a[80:95]\n"
    "v_add_f32 v197, v197, v9\n"
    "v_exp_f32 v10, v10\n"
    "v_exp_f32 v11, v11\n"
    "v_mfma_f32_32x32x16_bf16 a[96:111], a[204:207], v[136:139], a[96:111]\n"
    "v_add_f32 v196, v196, v10\n"
    "v_add_f32 v197, v197, v11\n"
    "v_exp_f32 v12, v12\n"
    "v_mfma_f32_32x32x16_bf16 a[112:127], a[204:207], v[152:155], a[112:127]\n"
    "v_add_f32 v196, v196, v12\n"
    "v_exp_f32 v13, v13\n"
    "v_exp_f32 v14, v14\n"
    "s_waitcnt vmcnt(8) lgkmcnt(0)\n"
    "s_barrier\n"
    "v_mfma_f32_32x32x16_bf16 a[0:15], a[224:227], v[140:143], a[0:15]\n"
    "ds_read_b128 a[192:195], v204 offset:16384\n"
    "v_add_f32 v197, v197, v13\n"
    "v_exp_f32 v15, v15\n"
    "v_mfma_f32_32x32x16_bf16 a[16:31], a[224:227], v[156:159], a[16:31]\n"
    "ds_read_b128 a[196:199], v204 offset:17408\n"
    "v_add_f32 v196, v196, v14\n"
    "v_exp_f32 v24, v24\n"
    "v_exp_f32 v25, v25\n"
    "v_mfma_f32_32x32x16_bf16 a[32:47], a[228:231], v[140:143], a[32:47]\n"
    "ds_read_b128 a[200:203], v204 offset:18432\n"
    "v_add_f32 v197, v197, v15\n"
    "v_exp_f32 v26, v26\n"
    "v_exp_f32 v27, v27\n"
    "v_mfma_f32_32x32x16_bf16 a[48:63], a[228:231], v[156:159], a[48:63]\n"
    "ds_read_b128 a[204:207], v204 offset:19456\n"
    "v_add_f32 v198, v198, v24\n"
    "v_exp_f32 v28, v28\n"
    "v_exp_f32 v29, v29\n"
    "v_mfma_f32_32x32x16_bf16 a[64:79], a[232:235], v[140:143], a[64:79]\n"
    "v_add_f32 v199, v199, v25\n"
    "v_add_f32 v198, v198, v26\n"
    "v_add_f32 v199, v199, v27\n"
    "v_add_f32 v198, v198, v28\n"
    "v_exp_f32 v30, v30\n"
    "v_mfma_f32_32x32x16_bf16 a[80:95], a[232:235], v[156:159], a[80:95]\n"
    "v_add_f32 v199, v199, v29\n"
    "v_add_f32 v198, v198, v30\n"
    "v_exp_f32 v31, v31\n"
    "v_xor_b32 v205, 229376, v205\n"
    "v_mfma_f32_32x32x16_bf16 a[96:111], a[236:239], v[140:143], a[96:111]\n"
    "v_add_f32 v199, v199, v31\n"
    "v_mfma_f32_32x32x16_bf16 a[112:127], a[236:239], v[156:159], a[112:127]\n"
    "s_cmp_lg_u32 s91, 0\n"
    "s_cbranch_scc0 L_rba_%=\n"
    "s_call_b64 s[98:99], L_rareb_%=\n"
    "L_rba_%=:\n"
    "L_b_%=:\n"
    "s_waitcnt lgkmcnt(1)\n"
    "v_mfma_f32_32x32x16_bf16 v[64:79], a[192:195], a[128:131], v[160:175]\n"
    "ds_read_b128 a[224:227], v204 offset:20480\n"
    "v_exp_f32 v32, v32\n"
    "v_exp_f32 v33, v33\n"
    "v_cvt_pk_bf16_f32 v128, v0, v1\n"
    "v_mfma_f32_32x32x16_bf16 v[80:95], a[192:195], a[160:163], v[176:191]\n"
    "ds_read_b128 a[228:231], v204 offset:21504\n"
    "v_exp_f32 v34, v34\n"
    "v_cvt_pk_bf16_f32 v129, v2, v3\n"
    "v_cvt_pk_bf16_f32 v130, v4, v5\n"
    "v_cvt_pk_bf16_f32 v131, v6, v7\n"
    "v_mfma_f32_32x32x16_bf16 v[64:79], a[196:199], a[132:135], v[64:79]\n"
    "ds_read_b128 a[232:235], v204 offset:22528\n"
    "v_exp_f32 v35, v35\n"
    "v_cvt_pk_bf16_f32 v144, v16, v17\n"
    "v_cvt_pk_bf16_f32 v145, v18, v19\n"
    "s_add_u32 m0, s81, 0\n"
    "v_mfma_f32_32x32x16_bf16 v[80:95], a[196:199], a[164:167], v[80:95]\n"
    "buffer_load_dwordx4 v251, s[52:55], s78 offen sc1 lds\n"
    "ds_read_b128 a[236:239], v204 offset:23552\n"
    "v_exp_f32 v36, v36\n"
    "v_cvt_pk_bf16_f32 v146, v20, v21\n"
    "v_cvt_pk_bf16_f32 v147, v22, v23\n"
    "v_mfma_f32_32x32x16_bf16 v[64:79], a[200:203], a[136:139], v[64:79]\n"
    "v_add_f32 v196, v196, v32\n"
    "v_add_f32 v197, v197, v33\n"
    "v_add_f32 v196, v196, v34\n"
    "v_add_f32 v197, v197, v35\n"
    "v_exp_f32 v37, v37\n"
    "v_mfma_f32_32x32x16_bf16 v[80:95], a[200:203], a[168:171], v[80:95]\n"
    "v_add_f32 v196, v196, v36\n"
    "v_add_f32 v197, v197, v37\n"
    "v_exp_f32 v38, v38\n"
    "v_cvt_pk_bf16_f32 v132, v8, v9\n"
    "v_cvt_pk_bf16_f32 v133, v10, v11\n"
    "s_waitcnt lgkmcnt(2)\n"
    "v_mfma_f32_32x32x16_bf16 v[64:79], a[204:207], a[140:143], v[64:79]\n"
    "v_add_f32 v196, v196, v38\n"
    "v_exp_f32 v39, v39\n"
    "v_cvt_pk_bf16_f32 v134, v12, v13\n"
    "v_cvt_pk_bf16_f32 v135, v14, v15\n"
    "v_cvt_pk_bf16_f32 v148, v24, v25\n"
    "v_mfma_f32_32x32x16_bf16 v[80:95], a[204:207], a[172:175], v[80:95]\n"
    "v_add_f32 v197, v197, v39\n"
    "v_exp_f32 v48, v48\n"
    "v_cvt_pk_bf16_f32 v149, v26, v27\n"
    "v_cvt_pk_bf16_f32 v150, v28, v29\n"
    "v_cvt_pk_bf16_f32 v151, v30, v31\n"
    "v_mfma_f32_32x32x16_bf16 v[64:79], a[224:227], a[144:147], v[64:79]\n"
    "ds_read_b128 a[192:195], v204 offset:24576\n"
    "v_add_f32 v198, v198, v48\n"
    "v_exp_f32 v49, v49\n"
    "v_cvt_pk_bf16_f32 v136, v32, v33\n"
    "v_cvt_pk_bf16_f32 v137, v34, v35\n"
    "v_mfma_f32_32x32x16_bf16 v[80:95], a[224:227], a[176:179], v[80:95]\n"
    "buffer_load_dwordx4 v251, s[52:55], s78 offen offset:1024 sc1 lds\n"
    "ds_read_b128 a[196:199], v204 offset:25600\n"
    "v_exp_f32 v50, v50\n"
    "v_exp_f32 v51, v51\n"
    "v_mfma_f32_32x32x16_bf16 v[64:79], a[228:231], a[148:151], v[64:79]\n"
    "ds_read_b128 a[200:203], v204 offset:26624\n"
    "v_add_f32 v199, v199, v49\n"
    "v_add_f32 v198, v198, v50\n"
    "v_add_f32 v199, v199, v51\n"
    "v_exp_f32 v52, v52\n"
    "v_mfma_f32_32x32x16_bf16 v[80:95], a[228:231], a[180:183], v[80:95]\n"
    "ds_read_b128 a[204:207], v204 offset:27648\n"
    "v_add_f32 v198, v198, v52\n"
    "v_exp_f32 v53, v53\n"
    "v_cvt_pk_bf16_f32 v138, v36, v37\n"
    "v_cvt_pk_bf16_f32 v139, v38, v39\n"
    "s_waitcnt lgkmcnt(3)\n"
    "v_mfma_f32_32x32x16_bf16 v[64:79], a[232:235], a[152:155], v[64:79]\n"
    "v_add_f32 v199, v199, v53\n"
    "v_exp_f32 v54, v54\n"
    "v_cvt_pk_bf16_f32 v152, v48, v49\n"
    "v_cvt_pk_bf16_f32 v153, v50, v51\n"
    "v_cvt_pk_bf16_f32 v154, v52, v53\n"
    "v_mfma_f32_32x32x16_bf16 v[80:95], a[232:235], a[184:187], v[80:95]\n"
    "v_add_f32 v198, v198, v54\n"
    "v_exp_f32 v55, v55\n"
    "v_mfma_f32_32x32x16_bf16 v[64:79], a[236:239], a[156:159], v[64:79]\n"
    "v_add_f32 v199, v199, v55\n"
    "v_exp_f32 v40, v40\n"
    "v_cvt_pk_bf16_f32 v155, v54, v55\n"
    "v_mfma_f32_32x32x16_bf16 v[80:95], a[236:239], a[188:191], v[80:95]\n"
    "buffer_load_dwordx4 v251, s[52:55], s78 offen offset:2048 sc1 lds\n"
    "v_add_f32 v196, v196, v40\n"
    "v_exp_f32 v41, v41\n"
    "v_mfma_f32_32x32x16_bf16 v[96:111], a[192:195], a[128:131], v[160:175]\n"
    "ds_read_b128 a[224:227], v204 offset:28672\n"
    "v_add_f32 v197, v197, v41\n"
    "v_exp_f32 v42, v42\n"
    "v_cvt_pk_bf16_f32 v140, v40, v41\n"
    "v_mfma_f32_32x32x16_bf16 v[112:127], a[192:195], a[160:163], v[176:191]\n"
    "ds_read_b128 a[228:231], v204 offset:29696\n"
    "v_add_f32 v196, v196, v42\n"
    "v_exp_f32 v43, v43\n"
    "s_waitcnt lgkmcnt(2)\n"
    "v_mfma_f32_32x32x16_bf16 v[96:111], a[196:199], a[132:135], v[96:111]\n"
    "ds_read_b128 a[232:235], v204 offset:30720\n"
    "v_add_f32 v197, v197, v43\n"
    "v_exp_f32 v44, v44\n"
    "v_exp_f32 v45, v45\n"
    "v_mfma_f32_32x32x16_bf16 v[112:127], a[196:199], a[164:167], v[112:127]\n"
    "ds_read_b128 a[236:239], v204 offset:31744\n"
    "v_add_f32 v196, v196, v44\n"
    "v_add_f32 v197, v197, v45\n"
    "v_exp_f32 v46, v46\n"
    "v_cvt_pk_bf16_f32 v141, v42, v43\n"
    "v_mfma_f32_32x32x16_bf16 v[96:111], a[200:203], a[136:139], v[96:111]\n"
    "v_add_f32 v196, v196, v46\n"
    "v_exp_f32 v47, v47\n"
    "s_cmp_lg_u32 s88, 0\n"
    "s_cbranch_scc1 L_b_m0_%=\n"
    "L_b_m0_%=r:\n"
    "v_mfma_f32_32x32x16_bf16 v[112:127], a[200:203], a[168:171], v[112:127]\n"
    "buffer_load_dwordx4 v251, s[52:55], s78 offen offset:3072 sc1 lds\n"
    "v_exp_f32 v56, v56\n"
    "v_max3_f32 v192, v64, v65, v66\n"
    "v_max3_f32 v192, v192, v67, v68\n"
    "v_max3_f32 v192, v192, v69, v70\n"
    "v_mfma_f32_32x32x16_bf16 v[96:111], a[204:207], a[140:143], v[96:111]\n"
    "v_exp_f32 v57, v57\n"
    "v_max3_f32 v192, v192, v71, v72\n"
    "v_max3_f32 v192, v192, v73, v74\n"
    "v_max3_f32 v192, v192, v75, v76\n"
    "v_max3_f32 v192, v192, v77, v78\n"
    "v_mfma_f32_32x32x16_bf16 v[112:127], a[204:207], a[172:175], v[112:127]\n"
    "v_exp_f32 v58, v58\n"
    "v_max_f32 v192, v192, v79\n"
    "v_max3_f32 v193, v80, v81, v82\n"
    "v_max3_f32 v193, v193, v83, v84\n"
    "v_max3_f32 v193, v193, v85, v86\n"
    "s_waitcnt lgkmcnt(1)\n"
    "v_mfma_f32_32x32x16_bf16 v[96:111], a[224:227], a[144:147], v[96:111]\n"
    "ds_read_b128 a[192:195], v205\n"
    "v_exp_f32 v59, v59\n"
    "v_max3_f32 v193, v193, v87, v88\n"
    "v_max3_f32 v193, v193, v89, v90\n"
    "v_max3_f32 v193, v193, v91, v92\n"
    "v_mfma_f32_32x32x16_bf16 v[112:127], a[224:227], a[176:179], v[112:127]\n"
    "ds_read_b128 a[196:199], v205 offset:2048\n"
    "v_add_f32 v197, v197, v47\n"
    "v_exp_f32 v60, v60\n"
    "v_max3_f32 v193, v193, v93, v94\n"
    "v_max_f32 v193, v193, v95\n"
    "v_mfma_f32_32x32x16_bf16 v[96:111], a[228:231], a[148:151], v[96:111]\n"
    "ds_read_b128 a[200:203], v205 offset:4096\n"
    "v_add_f32 v198, v198, v56\n"
    "v_add_f32 v199, v199, v57\n"
    "v_exp_f32 v61, v61\n"
    "s_add_u32 m0, s81, 4096\n"
    "v_mfma_f32_32x32x16_bf16 v[112:127], a[228:231], a[180:183], v[112:127]\n"
    "buffer_load_dwordx4 v248, s[52:55], s78 offen sc1 lds\n"
    "ds_read_b128 a[204:207], v205 offset:6144\n"
    "v_exp_f32 v62, v62\n"
    "v_exp_f32 v63, v63\n"
    "v_mfma_f32_32x32x16_bf16 v[96:111], a[232:235], a[152:155], v[96:111]\n"
    "v_add_f32 v198, v198, v58\n"
    "v_add_f32 v199, v199, v59\n"
    "v_add_f32 v198, v198, v60\n"
    "v_add_f32 v199, v199, v61\n"
    "v_add_f32 v198, v198, v62\n"
    "v_mfma_f32_32x32x16_bf16 v[112:127], a[232:235], a[184:187], v[112:127]\n"
    "v_add_f32 v199, v199, v63\n"
    "v_cvt_pk_bf16_f32 v142, v44, v45\n"
    "v_cvt_pk_bf16_f32 v143, v46, v47\n"
    "v_cvt_pk_bf16_f32 v156, v56, v57\n"
    "v_cvt_pk_bf16_f32 v157, v58, v59\n"
    "s_waitcnt lgkmcnt(2)\n"
    "v_mfma_f32_32x32x16_bf16 v[96:111], a[236:239], a[156:159], v[96:111]\n"
    "v_cvt_pk_bf16_f32 v158, v60, v61\n"
    "v_cvt_pk_bf16_f32 v159, v62, v63\n"
    "v_mfma_f32_32x32x16_bf16 v[112:127], a[236:239], a[188:191], v[112:127]\n"
    "v_mfma_f32_32x32x16_bf16 a[0:15], a[192:195], v[128:131], a[0:15]\n"
    "ds_read_b128 a[224:227], v205 offset:1024\n"
    "v_mfma_f32_32x32x16_bf16 a[16:31], a[192:195], v[144:147], a[16:31]\n"
    "buffer_load_dwordx4 v248, s[52:55], s78 offen offset:1024 sc1 lds\n"
    "ds_read_b128 a[228:231], v205 offset:3072\n"
    "v_mfma_f32_32x32x16_bf16 a[32:47], a[196:199], v[128:131], a[32:47]\n"
    "ds_read_b128 a[232:235], v205 offset:5120\n"
    "v_mfma_f32_32x32x16_bf16 a[48:63], a[196:199], v[144:147], a[48:63]\n"
    "ds_read_b128 a[236:239], v205 offset:7168\n"
    "s_waitcnt lgkmcnt(3)\n"
    "v_mfma_f32_32x32x16_bf16 a[64:79], a[200:203], v[128:131], a[64:79]\n"
    "s_cmp_lg_u32 s88, 0\n"
    "s_cbranch_scc1 L_b_m1_%=\n"
    "L_b_m1_%=r:\n"
    "v_mfma_f32_32x32x16_bf16 a[80:95], a[200:203], v[144:147], a[80:95]\n"
    "v_max3_f32 v192, v192, v96, v97\n"
    "v_max3_f32 v192, v192, v98, v99\n"
    "v_max3_f32 v192, v192, v100, v101\n"
    "v_max3_f32 v192, v192, v102, v103\n"
    "v_max3_f32 v192, v192, v104, v105\n"
    "v_mfma_f32_32x32x16_bf16 a[96:111], a[204:207], v[128:131], a[96:111]\n"
    "v_max3_f32 v192, v192, v106, v107\n"
    "v_max3_f32 v192, v192, v108, v109\n"
    "v_max3_f32 v192, v192, v110, v111\n"
    "v_max3_f32 v193, v193, v112, v113\n"
    "v_max3_f32 v193, v193, v114, v115\n"
    "v_mfma_f32_32x32x16_bf16 a[112:127], a[204:207], v[144:147], a[112:127]\n"
    "buffer_load_dwordx4 v248, s[52:55], s78 offen offset:2048 sc1 lds\n"
    "v_max3_f32 v193, v193, v116, v117\n"
    "v_max3_f32 v193, v193, v118, v119\n"
    "v_max3_f32 v193, v193, v120, v121\n"
    "v_max3_f32 v193, v193, v122, v123\n"
    "v_mfma_f32_32x32x16_bf16 a[0:15], a[224:227], v[132:135], a[0:15]\n"
    "ds_read_b128 a[192:195], v205 offset:8192\n"
    "v_max3_f32 v193, v193, v124, v125\n"
    "v_max3_f32 v193, v193, v126, v127\n"
    "v_max_f32 v194, v192, v193\n"
    "v_cmp_lt_f32 vcc, s90, v194\n"
    "v_mfma_f32_32x32x16_bf16 a[16:31], a[224:227], v[148:151], a[16:31]\n"
    "ds_read_b128 a[196:199], v205 offset:10240\n"
    "s_cbranch_vccnz L_b_ra_%=\n"
    "L_b_ra_%=r:\n"
    "s_waitcnt lgkmcnt(2)\n"
    "v_mfma_f32_32x32x16_bf16 a[32:47], a[228:231], v[132:135], a[32:47]\n"
    "ds_read_b128 a[200:203], v205 offset:12288\n"
    "v_exp_f32 v64, v64\n"
    "v_exp_f32 v65, v65\n"
    "v_mfma_f32_32x32x16_bf16 a[48:63], a[228:231], v[148:151], a[48:63]\n"
    "ds_read_b128 a[204:207], v205 offset:14336\n"
    "v_add_f32 v196, v196, v64\n"
    "v_exp_f32 v66, v66\n"
    "v_exp_f32 v67, v67\n"
    "v_mfma_f32_32x32x16_bf16 a[64:79], a[232:235], v[132:135], a[64:79]\n"
    "v_add_f32 v197, v197, v65\n"
    "v_add_f32 v196, v196, v66\n"
    "v_exp_f32 v68, v68\n"
    "s_add_u32 s79, s80, s94\n"
    "s_add_u32 s80, s80, 32768\n"
    "v_mfma_f32_32x32x16_bf16 a[80:95], a[232:235], v[148:151], a[80:95]\n"
    "buffer_load_dwordx4 v248, s[52:55], s78 offen offset:3072 sc1 lds\n"
    "v_add_f32 v197, v197, v67\n"
    "v_exp_f32 v69, v69\n"
    "s_cmp_eq_u32 s80, 98304\n"
    "s_cselect_b32 s80, 0, s80\n"
    "v_mfma_f32_32x32x16_bf16 a[96:111], a[236:239], v[132:135], a[96:111]\n"
    "v_add_f32 v196, v196, v68\n"
    "v_exp_f32 v70, v70\n"
    "v_exp_f32 v71, v71\n"
    "v_add_u32 v204, s80, v251\n"
    "v_mfma_f32_32x32x16_bf16 a[112:127], a[236:239], v[148:151], a[112:127]\n"
    "v_exp_f32 v80, v80\n"
    "v_exp_f32 v81, v81\n"
    "s_add_u32 s78, s78, 32768\n"
    "s_xor_b32 s81, s81, 229376\n"
    "s_waitcnt lgkmcnt(1)\n"
    "v_mfma_f32_32x32x16_bf16 a[0:15], a[192:195], v[136:139], a[0:15]\n"
    "ds_read_b128 a[224:227], v205 offset:9216\n"
    "v_add_f32 v197, v197, v69\n"
    "v_exp_f32 v82, v82\n"
    "v_exp_f32 v83, v83\n"
    "v_mfma_f32_32x32x16_bf16 a[16:31], a[192:195], v[152:155], a[16:31]\n"
    "ds_read_b128 a[228:231], v205 offset:11264\n"
    "v_add_f32 v196, v196, v70\n"
    "v_add_f32 v197, v197, v71\n"
    "v_add_f32 v198, v198, v80\n"
    "v_exp_f32 v84, v84\n"
    "v_mfma_f32_32x32x16_bf16 a[32:47], a[196:199], v[136:139], a[32:47]\n"
    "ds_read_b128 a[232:235], v205 offset:13312\n"
    "v_add_f32 v199, v199, v81\n"
    "v_exp_f32 v85, v85\n"
    "v_exp_f32 v86, v86\n"
    "v_mfma_f32_32x32x16_bf16 a[48:63], a[196:199], v[152:155], a[48:63]\n"
    "ds_read_b128 a[236:239], v205 offset:15360\n"
    "v_add_f32 v198, v198, v82\n"
    "v_exp_f32 v87, v87\n"
    "v_exp_f32 v72, v72\n"
    "v_mfma_f32_32x32x16_bf16 a[64:79], a[200:203], v[136:139], a[64:79]\n"
    "v_add_f32 v199, v199, v83\n"
    "v_add_f32 v198, v198, v84\n"
    "v_add_f32 v199, v199, v85\n"
    "v_add_f32 v198, v198, v86\n"
    "v_exp_f32 v73, v73\n"
    "v_mfma_f32_32x32x16_bf16 a[80:95], a[200:203], v[152:155], a[80:95]\n"
    "v_add_f32 v199, v199, v87\n"
    "v_add_f32 v196, v196, v72\n"
    "v_exp_f32 v74, v74\n"
    "v_exp_f32 v75, v75\n"
    "s_waitcnt lgkmcnt(2)\n"
    "v_mfma_f32_32x32x16_bf16 a[96:111], a[204:207], v[136:139], a[96:111]\n"
    "v_add_f32 v197, v197, v73\n"
    "v_add_f32 v196, v196, v74\n"
    "v_add_f32 v197, v197, v75\n"
    "v_exp_f32 v76, v76\n"
    "v_mfma_f32_32x32x16_bf16 a[112:127], a[204:207], v[152:155], a[112:127]\n"
    "v_add_f32 v196, v196, v76\n"
    "v_exp_f32 v77, v77\n"
    "v_exp_f32 v78, v78\n"
    "s_waitcnt vmcnt(16) lgkmcnt(0)\n"
    "s_barrier\n"
    "v_mfma_f32_32x32x16_bf16 a[0:15], a[224:227], v[140:143], a[0:15]\n"
    "ds_read_b128 a[192:195], v204\n"
    "v_add_f32 v197, v197, v77\n"
    "v_exp_f32 v79, v79\n"
    "v_mfma_f32_32x32x16_bf16 a[16:31], a[224:227], v[156:159], a[16:31]\n"
    "ds_read_b128 a[196:199], v204 offset:1024\n"
    "v_add_f32 v196, v196, v78\n"
    "v_exp_f32 v88, v88\n"
    "v_exp_f32 v89, v89\n"
    "v_mfma_f32_32x32x16_bf16 a[32:47], a[228:231], v[140:143], a[32:47]\n"
    "ds_read_b128 a[200:203], v204 offset:2048\n"
    "v_add_f32 v197, v197, v79\n"
    "v_exp_f32 v90, v90\n"
    "v_exp_f32 v91, v91\n"
    "v_mfma_f32_32x32x16_bf16 a[48:63], a[228:231], v[156:159], a[48:63]\n"
    "ds_read_b128 a[204:207], v204 offset:3072\n"
    "v_add_f32 v198, v198, v88\n"
    "v_exp_f32 v92, v92\n"
    "v_exp_f32 v93, v93\n"
    "v_mfma_f32_32x32x16_bf16 a[64:79], a[232:235], v[140:143], a[64:79]\n"
    "ds_read_b128 a[208:211], v204 offset:4096\n"
    "v_add_f32 v199, v199, v89\n"
    "v_add_f32 v198, v198, v90\n"
    "v_add_f32 v199, v199, v91\n"
    "v_exp_f32 v94, v94\n"
    "v_mfma_f32_32x32x16_bf16 a[80:95], a[232:235], v[156:159], a[80:95]\n"
    "ds_read_b128 a[212:215], v204 offset:5120\n"
    "v_add_f32 v198, v198, v92\n"
    "v_add_f32 v199, v199, v93\n"
    "v_add_f32 v198, v198, v94\n"
    "v_exp_f32 v95, v95\n"
    "v_mfma_f32_32x32x16_bf16 a[96:111], a[236:239], v[140:143], a[96:111]\n"
    "ds_read_b128 a[216:219], v204 offset:6144\n"
    "v_add_f32 v199, v199, v95\n"
    "v_mfma_f32_32x32x16_bf16 a[112:127], a[236:239], v[156:159], a[112:127]\n"
    "ds_read_b128 a[220:223], v204 offset:7168\n"
    "s_cmp_lg_u32 s91, 0\n"
    "s_cbranch_scc0 L_rbb_%=\n"
    "s_call_b64 s[98:99], L_rareb_%=\n"
    "L_rbb_%=:\n"
    "s_add_u32 s76, s76, 1\n"
    "s_cmp_le_u32 s76, s62\n"
    "s_cbranch_scc1 L_a_%=\n"
    "s_lshl_b32 s76, s62, 1\n"
    "s_sub_u32 s76, 14, s76\n"
    "s_cmp_eq_u32 s76, 0\n"
    "s_cselect_b32 s88, 1, 0\n"
    "s_waitcnt lgkmcnt(4)\n"
    "v_mfma_f32_32x32x16_bf16 v[0:15], a[192:195], a[160:163], v[176:191]\n"
    "ds_read_b128 a[224:227], v204 offset:8192\n"
    "v_exp_f32 v96, v96\n"
    "v_exp_f32 v97, v97\n"
    "v_cvt_pk_bf16_f32 v128, v64, v65\n"
    "v_cvt_pk_bf16_f32 v129, v66, v67\n"
    "v_mfma_f32_32x32x16_bf16 v[0:15], a[196:199], a[164:167], v[0:15]\n"
    "ds_read_b128 a[228:231], v204 offset:9216\n"
    "v_exp_f32 v98, v98\n"
    "v_cvt_pk_bf16_f32 v130, v68, v69\n"
    "v_cvt_pk_bf16_f32 v131, v70, v71\n"
    "v_cvt_pk_bf16_f32 v144, v80, v81\n"
    "v_cvt_pk_bf16_f32 v145, v82, v83\n"
    "v_mfma_f32_32x32x16_bf16 v[0:15], a[200:203], a[168:171], v[0:15]\n"
    "ds_read_b128 a[232:235], v204 offset:10240\n"
    "v_add_f32 v196, v196, v96\n"
    "v_add_f32 v197, v197, v97\n"
    "v_exp_f32 v99, v99\n"
    "v_cvt_pk_bf16_f32 v146, v84, v85\n"
    "v_cvt_pk_bf16_f32 v147, v86, v87\n"
    "v_mfma_f32_32x32x16_bf16 v[0:15], a[204:207], a[172:175], v[0:15]\n"
    "ds_read_b128 a[236:239], v204 offset:11264\n"
    "v_add_f32 v196, v196, v98\n"
    "v_add_f32 v197, v197, v99\n"
    "v_exp_f32 v100, v100\n"
    "v_cvt_pk_bf16_f32 v132, v72, v73\n"
    "v_cvt_pk_bf16_f32 v133, v74, v75\n"
    "s_waitcnt lgkmcnt(4)\n"
    "v_mfma_f32_32x32x16_bf16 v[0:15], a[208:211], a[176:179], v[0:15]\n"
    "ds_read_b128 a[240:243], v204 offset:12288\n"
    "v_add_f32 v196, v196, v100\n"
    "v_exp_f32 v101, v101\n"
    "v_cvt_pk_bf16_f32 v134, v76, v77\n"
    "v_cvt_pk_bf16_f32 v135, v78, v79\n"
    "v_cvt_pk_bf16_f32 v148, v88, v89\n"
    "v_mfma_f32_32x32x16_bf16 v[0:15], a[212:215], a[180:183], v[0:15]\n"
    "ds_read_b128 a[244:247], v204 offset:13312\n"
    "v_add_f32 v197, v197, v101\n"
    "v_exp_f32 v102, v102\n"
    "v_cvt_pk_bf16_f32 v149, v90, v91\n"
    "v_cvt_pk_bf16_f32 v150, v92, v93\n"
    "v_cvt_pk_bf16_f32 v151, v94, v95\n"
    "v_mfma_f32_32x32x16_bf16 v[0:15], a[216:219], a[184:187], v[0:15]\n"
    "ds_read_b128 a[248:251], v204 offset:14336\n"
    "v_add_f32 v196, v196, v102\n"
    "v_exp_f32 v103, v103\n"
    "v_cvt_pk_bf16_f32 v136, v96, v97\n"
    "v_cvt_pk_bf16_f32 v137, v98, v99\n"
    "v_cvt_pk_bf16_f32 v138, v100, v101\n"
    "v_mfma_f32_32x32x16_bf16 v[0:15], a[220:223], a[188:191], v[0:15]\n"
    "ds_read_b128 a[252:255], v204 offset:15360\n"
    "v_add_f32 v197, v197, v103\n"
    "v_exp_f32 v112, v112\n"
    "v_cvt_pk_bf16_f32 v139, v102, v103\n"
    "s_waitcnt lgkmcnt(4)\n"
    "v_mfma_f32_32x32x16_bf16 v[16:31], a[224:227], a[160:163], v[176:191]\n"
    "ds_read_b128 a[192:195], v204 offset:16384\n"
    "v_add_f32 v198, v198, v112\n"
    "v_exp_f32 v113, v113\n"
    "v_mfma_f32_32x32x16_bf16 v[16:31], a[228:231], a[164:167], v[16:31]\n"
    "ds_read_b128 a[196:199], v204 offset:17408\n"
    "v_add_f32 v199, v199, v113\n"
    "v_exp_f32 v114, v114\n"
    "v_exp_f32 v115, v115\n"
    "v_cvt_pk_bf16_f32 v152, v112, v113\n"
    "v_mfma_f32_32x32x16_bf16 v[16:31], a[232:235], a[168:171], v[16:31]\n"
    "ds_read_b128 a[200:203], v204 offset:18432\n"
    "v_add_f32 v198, v198, v114\n"
    "v_add_f32 v199, v199, v115\n"
    "v_exp_f32 v116, v116\n"
    "v_cvt_pk_bf16_f32 v153, v114, v115\n"
    "v_mfma_f32_32x32x16_bf16 v[16:31], a[236:239], a[172:175], v[16:31]\n"
    "ds_read_b128 a[204:207], v204 offset:19456\n"
    "v_add_f32 v198, v198, v116\n"
    "v_exp_f32 v117, v117\n"
    "s_waitcnt lgkmcnt(4)\n"
    "v_mfma_f32_32x32x16_bf16 v[16:31], a[240:243], a[176:179], v[16:31]\n"
    "ds_read_b128 a[208:211], v204 offset:20480\n"
    "v_add_f32 v199, v199, v117\n"
    "v_exp_f32 v118, v118\n"
    "v_cvt_pk_bf16_f32 v154, v116, v117\n"
    "v_mfma_f32_32x32x16_bf16 v[16:31], a[244:247], a[180:183], v[16:31]\n"
    "ds_read_b128 a[212:215], v204 offset:21504\n"
    "v_add_f32 v198, v198, v118\n"
    "v_exp_f32 v119, v119\n"
    "v_mfma_f32_32x32x16_bf16 v[16:31], a[248:251], a[184:187], v[16:31]\n"
    "ds_read_b128 a[216:219], v204 offset:22528\n"
    "v_add_f32 v199, v199, v119\n"
    "v_exp_f32 v104, v104\n"
    "v_cvt_pk_bf16_f32 v155, v118, v119\n"
    "v_mfma_f32_32x32x16_bf16 v[16:31], a[252:255], a[188:191], v[16:31]\n"
    "ds_read_b128 a[220:223], v204 offset:23552\n"
    "v_add_f32 v196, v196, v104\n"
    "v_exp_f32 v105, v105\n"
    "s_waitcnt lgkmcnt(4)\n"
    "v_mfma_f32_32x32x16_bf16 v[32:47], a[192:195], a[160:163], v[176:191]\n"
    "ds_read_b128 a[224:227], v204 offset:24576\n"
    "v_add_f32 v197, v197, v105\n"
    "v_exp_f32 v106, v106\n"
    "v_cvt_pk_bf16_f32 v140, v104, v105\n"
    "v_mfma_f32_32x32x16_bf16 v[32:47], a[196:199], a[164:167], v[32:47]\n"
    "ds_read_b128 a[228:231], v204 offset:25600\n"
    "v_add_f32 v196, v196, v106\n"
    "v_exp_f32 v107, v107\n"
    "v_mfma_f32_32x32x16_bf16 v[32:47], a[200:203], a[168:171], v[32:47]\n"
    "ds_read_b128 a[232:235], v204 offset:26624\n"
    "v_add_f32 v197, v197, v107\n"
    "v_exp_f32 v108, v108\n"
    "v_exp_f32 v109, v109\n"
    "v_cvt_pk_bf16_f32 v141, v106, v107\n"
    "v_mfma_f32_32x32x16_bf16 v[32:47], a[204:207], a[172:175], v[32:47]\n"
    "ds_read_b128 a[236:239], v204 offset:27648\n"
    "v_add_f32 v196, v196, v108\n"
    "v_add_f32 v197, v197, v109\n"
    "v_exp_f32 v110, v110\n"
    "v_cvt_pk_bf16_f32 v142, v108, v109\n"
    "s_waitcnt lgkmcnt(4)\n"
    "v_mfma_f32_32x32x16_bf16 v[32:47], a[208:211], a[176:179], v[32:47]\n"
    "ds_read_b128 a[240:243], v204 offset:28672\n"
    "v_add_f32 v196, v196, v110\n"
    "v_exp_f32 v111, v111\n"
    "s_cmp_lg_u32 s88, 0\n"
    "s_cbranch_scc1 L_t_m0_%=\n"
    "L_t_m0_%=r:\n"
    "v_mfma_f32_32x32x16_bf16 v[32:47], a[212:215], a[180:183], v[32:47]\n"
    "ds_read_b128 a[244:247], v204 offset:29696\n"
    "v_exp_f32 v120, v120\n"
    "v_max3_f32 v193, v0, v1, v2\n"
    "v_max3_f32 v193, v193, v3, v4\n"
    "v_max3_f32 v193, v193, v5, v6\n"
    "v_max3_f32 v193, v193, v7, v8\n"
    "v_mfma_f32_32x32x16_bf16 v[32:47], a[216:219], a[184:187], v[32:47]\n"
    "ds_read_b128 a[248:251], v204 offset:30720\n"
    "v_exp_f32 v121, v121\n"
    "v_max3_f32 v193, v193, v9, v10\n"
    "v_max3_f32 v193, v193, v11, v12\n"
    "v_max3_f32 v193, v193, v13, v14\n"
    "v_max_f32 v193, v193, v15\n"
    "v_mfma_f32_32x32x16_bf16 v[32:47], a[220:223], a[188:191], v[32:47]\n"
    "ds_read_b128 a[252:255], v204 offset:31744\n"
    "v_exp_f32 v122, v122\n"
    "v_max3_f32 v192, v16, v17, v18\n"
    "v_max3_f32 v192, v192, v19, v20\n"
    "v_max3_f32 v192, v192, v21, v22\n"
    "v_max3_f32 v192, v192, v23, v24\n"
    "s_waitcnt lgkmcnt(4)\n"
    "v_mfma_f32_32x32x16_bf16 v[48:63], a[224:227], a[160:163], v[176:191]\n"
    "ds_read_b128 a[192:195], v205 offset:16384\n"
    "v_exp_f32 v123, v123\n"
    "v_max3_f32 v192, v192, v25, v26\n"
    "v_max3_f32 v192, v192, v27, v28\n"
    "v_max3_f32 v192, v192, v29, v30\n"
    "s_add_u32 m0, s79, 0\n"
    "v_mfma_f32_32x32x16_bf16 v[48:63], a[228:231], a[164:167], v[48:63]\n"
    "buffer_load_dwordx4 v251, s[48:51], s77 offen sc1 lds\n"
    "ds_read_b128 a[196:199], v205 offset:18432\n"
    "v_add_f32 v197, v197, v111\n"
    "v_add_f32 v198, v198, v120\n"
    "v_exp_f32 v124, v124\n"
    "v_max_f32 v192, v192, v31\n"
    "v_mfma_f32_32x32x16_bf16 v[48:63], a[232:235], a[168:171], v[48:63]\n"
    "ds_read_b128 a[200:203], v205 offset:20480\n"
    "v_add_f32 v199, v199, v121\n"
    "v_add_f32 v198, v198, v122\n"
    "v_add_f32 v199, v199, v123\n"
    "v_add_f32 v198, v198, v124\n"
    "v_exp_f32 v125, v125\n"
    "v_mfma_f32_32x32x16_bf16 v[48:63], a[236:239], a[172:175], v[48:63]\n"
    "ds_read_b128 a[204:207], v205 offset:22528\n"
    "v_add_f32 v199, v199, v125\n"
    "v_exp_f32 v126, v126\n"
    "v_exp_f32 v127, v127\n"
    "v_cvt_pk_bf16_f32 v143, v110, v111\n"
    "s_waitcnt lgkmcnt(4)\n"
    "v_mfma_f32_32x32x16_bf16 v[48:63], a[240:243], a[176:179], v[48:63]\n"
    "v_add_f32 v198, v198, v126\n"
    "v_add_f32 v199, v199, v127\n"
    "v_cvt_pk_bf16_f32 v156, v120, v121\n"
    "v_cvt_pk_bf16_f32 v157, v122, v123\n"
    "v_cvt_pk_bf16_f32 v158, v124, v125\n"
    "v_cvt_pk_bf16_f32 v159, v126, v127\n"
    "v_mfma_f32_32x32x16_bf16 v[48:63], a[244:247], a[180:183], v[48:63]\n"
    "buffer_load_dwordx4 v251, s[48:51], s77 offen offset:1024 sc1 lds\n"
    "v_mfma_f32_32x32x16_bf16 v[48:63], a[248:251], a[184:187], v[48:63]\n"
    "v_mfma_f32_32x32x16_bf16 v[48:63], a[252:255], a[188:191], v[48:63]\n"
    "s_waitcnt lgkmcnt(0)\n"
    "v_mfma_f32_32x32x16_bf16 a[0:15], a[192:195], v[128:131], a[0:15]\n"
    "ds_read_b128 a[224:227], v205 offset:17408\n"
    "v_mfma_f32_32x32x16_bf16 a[16:31], a[192:195], v[144:147], a[16:31]\n"
    "buffer_load_dwordx4 v251, s[48:51], s77 offen offset:2048 sc1 lds\n"
    "ds_read_b128 a[228:231], v205 offset:19456\n"
    "v_mfma_f32_32x32x16_bf16 a[32:47], a[196:199], v[128:131], a[32:47]\n"
    "ds_read_b128 a[232:235], v205 offset:21504\n"
    "v_mfma_f32_32x32x16_bf16 a[48:63], a[196:199], v[144:147], a[48:63]\n"
    "ds_read_b128 a[236:239], v205 offset:23552\n"
    "v_mfma_f32_32x32x16_bf16 a[64:79], a[200:203], v[128:131], a[64:79]\n"
    "s_cmp_lg_u32 s88, 0\n"
    "s_cbranch_scc1 L_t_m1_%=\n"
    "L_t_m1_%=r:\n"
    "v_mfma_f32_32x32x16_bf16 a[80:95], a[200:203], v[144:147], a[80:95]\n"
    "buffer_load_dwordx4 v251, s[48:51], s77 offen offset:3072 sc1 lds\n"
    "v_max3_f32 v193, v193, v32, v33\n"
    "v_max3_f32 v193, v193, v34, v35\n"
    "v_max3_f32 v193, v193, v36, v37\n"
    "v_max3_f32 v193, v193, v38, v39\n"
    "v_max3_f32 v193, v193, v40, v41\n"
    "v_mfma_f32_32x32x16_bf16 a[96:111], a[204:207], v[128:131], a[96:111]\n"
    "v_max3_f32 v193, v193, v42, v43\n"
    "v_max3_f32 v193, v193, v44, v45\n"
    "v_max3_f32 v193, v193, v46, v47\n"
    "v_max3_f32 v192, v192, v48, v49\n"
    "v_max3_f32 v192, v192, v50, v51\n"
    "v_max3_f32 v192, v192, v52, v53\n"
    "v_mfma_f32_32x32x16_bf16 a[112:127], a[204:207], v[144:147], a[112:127]\n"
    "v_max3_f32 v192, v192, v54, v55\n"
    "v_max3_f32 v192, v192, v56, v57\n"
    "v_max3_f32 v192, v192, v58, v59\n"
    "v_max3_f32 v192, v192, v60, v61\n"
    "v_max3_f32 v192, v192, v62, v63\n"
    "s_waitcnt lgkmcnt(0)\n"
    "v_mfma_f32_32x32x16_bf16 a[0:15], a[224:227], v[132:135], a[0:15]\n"
    "ds_read_b128 a[192:195], v205 offset:24576\n"
    "v_max_f32 v193, v192, v193\n"
    "v_cmp_lt_f32 vcc, s90, v193\n"
    "s_add_u32 m0, s79, 4096\n"
    "v_mfma_f32_32x32x16_bf16 a[16:31], a[224:227], v[148:151], a[16:31]\n"
    "buffer_load_dwordx4 v248, s[48:51], s77 offen sc1 lds\n"
    "ds_read_b128 a[196:199], v205 offset:26624\n"
    "s_cbranch_vccnz L_t_ra_%=\n"
    "L_t_ra_%=r:\n"
    "v_mfma_f32_32x32x16_bf16 a[32:47], a[228:231], v[132:135], a[32:47]\n"
    "ds_read_b128 a[200:203], v205 offset:28672\n"
    "v_exp_f32 v0, v0\n"
    "v_exp_f32 v1, v1\n"
    "v_mfma_f32_32x32x16_bf16 a[48:63], a[228:231], v[148:151], a[48:63]\n"
    "ds_read_b128 a[204:207], v205 offset:30720\n"
    "v_add_f32 v198, v198, v0\n"
    "v_add_f32 v199, v199, v1\n"
    "v_exp_f32 v2, v2\n"
    "v_exp_f32 v3, v3\n"
    "v_mfma_f32_32x32x16_bf16 a[64:79], a[232:235], v[132:135], a[64:79]\n"
    "v_add_f32 v198, v198, v2\n"
    "v_add_f32 v199, v199, v3\n"
    "v_exp_f32 v4, v4\n"
    "s_add_u32 s79, s80, s94\n"
    "s_add_u32 s80, s80, 32768\n"
    "v_mfma_f32_32x32x16_bf16 a[80:95], a[232:235], v[148:151], a[80:95]\n"
    "buffer_load_dwordx4 v248, s[48:51], s77 offen offset:1024 sc1 lds\n"
    "v_exp_f32 v5, v5\n"
    "v_exp_f32 v6, v6\n"
    "s_cmp_eq_u32 s80, 98304\n"
    "s_cselect_b32 s80, 0, s80\n"
    "v_mfma_f32_32x32x16_bf16 a[96:111], a[236:239], v[132:135], a[96:111]\n"
    "v_add_f32 v198, v198, v4\n"
    "v_add_f32 v199, v199, v5\n"
    "v_exp_f32 v7, v7\n"
    "v_exp_f32 v8, v8\n"
    "v_add_u32 v204, s80, v251\n"
    "v_mfma_f32_32x32x16_bf16 a[112:127], a[236:239], v[148:151], a[112:127]\n"
    "v_add_f32 v198, v198, v6\n"
    "v_add_f32 v199, v199, v7\n"
    "v_add_f32 v198, v198, v8\n"
    "v_exp_f32 v9, v9\n"
    "s_waitcnt lgkmcnt(0)\n"
    "v_mfma_f32_32x32x16_bf16 a[0:15], a[192:195], v[136:139], a[0:15]\n"
    "ds_read_b128 a[224:227], v205 offset:25600\n"
    "v_add_f32 v199, v199, v9\n"
    "v_exp_f32 v10, v10\n"
    "v_exp_f32 v11, v11\n"
    "v_mfma_f32_32x32x16_bf16 a[16:31], a[192:195], v[152:155], a[16:31]\n"
    "buffer_load_dwordx4 v248, s[48:51], s77 offen offset:2048 sc1 lds\n"
    "ds_read_b128 a[228:231], v205 offset:27648\n"
    "v_add_f32 v198, v198, v10\n"
    "v_add_f32 v199, v199, v11\n"
    "v_exp_f32 v12, v12\n"
    "v_mfma_f32_32x32x16_bf16 a[32:47], a[196:199], v[136:139], a[32:47]\n"
    "ds_read_b128 a[232:235], v205 offset:29696\n"
    "v_add_f32 v198, v198, v12\n"
    "v_exp_f32 v13, v13\n"
    "v_exp_f32 v14, v14\n"
    "v_mfma_f32_32x32x16_bf16 a[48:63], a[196:199], v[152:155], a[48:63]\n"
    "ds_read_b128 a[236:239], v205 offset:31744\n"
    "v_add_f32 v199, v199, v13\n"
    "v_add_f32 v198, v198, v14\n"
    "v_exp_f32 v15, v15\n"
    "v_exp_f32 v16, v16\n"
    "v_mfma_f32_32x32x16_bf16 a[64:79], a[200:203], v[136:139], a[64:79]\n"
    "v_add_f32 v199, v199, v15\n"
    "v_add_f32 v198, v198, v16\n"
    "v_exp_f32 v17, v17\n"
    "v_mfma_f32_32x32x16_bf16 a[80:95], a[200:203], v[152:155], a[80:95]\n"
    "buffer_load_dwordx4 v248, s[48:51], s77 offen offset:3072 sc1 lds\n"
    "v_add_f32 v199, v199, v17\n"
    "v_exp_f32 v18, v18\n"
    "v_exp_f32 v19, v19\n"
    "v_mfma_f32_32x32x16_bf16 a[96:111], a[204:207], v[136:139], a[96:111]\n"
    "v_add_f32 v198, v198, v18\n"
    "v_add_f32 v199, v199, v19\n"
    "v_exp_f32 v20, v20\n"
    "s_add_u32 s77, s77, 32768\n"
    "v_mfma_f32_32x32x16_bf16 a[112:127], a[204:207], v[152:155], a[112:127]\n"
    "v_add_f32 v198, v198, v20\n"
    "v_exp_f32 v21, v21\n"
    "v_exp_f32 v22, v22\n"
    "s_waitcnt vmcnt(8) lgkmcnt(0)\n"
    "s_barrier\n"
    "v_mfma_f32_32x32x16_bf16 a[0:15], a[224:227], v[140:143], a[0:15]\n"
    "ds_read_b128 a[192:195], v204\n"
    "v_add_f32 v199, v199, v21\n"
    "v_exp_f32 v23, v23\n"
    "s_add_u32 m0, s81, 0\n"
    "v_mfma_f32_32x32x16_bf16 a[16:31], a[224:227], v[156:159], a[16:31]\n"
    "buffer_load_dwordx4 v251, s[52:55], s78 offen sc1 lds\n"
    "ds_read_b128 a[196:199], v204 offset:1024\n"
    "v_add_f32 v198, v198, v22\n"
    "v_exp_f32 v24, v24\n"
    "v_exp_f32 v25, v25\n"
    "v_mfma_f32_32x32x16_bf16 a[32:47], a[228:231], v[140:143], a[32:47]\n"
    "ds_read_b128 a[200:203], v204 offset:2048\n"
    "v_add_f32 v199, v199, v23\n"
    "v_add_f32 v198, v198, v24\n"
    "v_exp_f32 v26, v26\n"
    "v_exp_f32 v27, v27\n"
    "v_mfma_f32_32x32x16_bf16 a[48:63], a[228:231], v[156:159], a[48:63]\n"
    "ds_read_b128 a[204:207], v204 offset:3072\n"
    "v_add_f32 v199, v199, v25\n"
    "v_add_f32 v198, v198, v26\n"
    "v_exp_f32 v28, v28\n"
    "v_exp_f32 v29, v29\n"
    "v_mfma_f32_32x32x16_bf16 a[64:79], a[232:235], v[140:143], a[64:79]\n"
    "ds_read_b128 a[208:211], v204 offset:4096\n"
    "v_add_f32 v199, v199, v27\n"
    "v_add_f32 v198, v198, v28\n"
    "v_add_f32 v199, v199, v29\n"
    "v_exp_f32 v30, v30\n"
    "v_xor_b32 v205, 229376, v205\n"
    "v_mfma_f32_32x32x16_bf16 a[80:95], a[232:235], v[156:159], a[80:95]\n"
    "buffer_load_dwordx4 v251, s[52:55], s78 offen offset:1024 sc1 lds\n"
    "ds_read_b128 a[212:215], v204 offset:5120\n"
    "v_add_f32 v198, v198, v30\n"
    "v_exp_f32 v31, v31\n"
    "v_mfma_f32_32x32x16_bf16 a[96:111], a[236:239], v[140:143], a[96:111]\n"
    "ds_read_b128 a[216:219], v204 offset:6144\n"
    "v_add_f32 v199, v199, v31\n"
    "v_mfma_f32_32x32x16_bf16 a[112:127], a[236:239], v[156:159], a[112:127]\n"
    "ds_read_b128 a[220:223], v204 offset:7168\n"
    "s_cmp_lg_u32 s91, 0\n"
    "s_cbranch_scc0 L_rbt_%=\n"
    "s_call_b64 s[98:99], L_rareb_%=\n"
    "L_rbt_%=:\n"
    "s_nop 15\n"
    "v_add_f32 v196, v196, v197\n"
    "v_mov_b32 v211, v196\n"
    "s_nop 1\n"
    "v_permlane32_swap_b32 v211, v196\n"
    "s_nop 0\n"
    "v_add_f32 v211, v211, v196\n"
    "v_rcp_f32 v219, v211\n"
    "s_lshl_b32 s82, s62, 2\n"
    "s_add_u32 s82, s82, s60\n"
    "s_lshl_b32 s82, s82, 18\n"
    "s_lshl_b32 s83, s61, 12\n"
    "s_add_u32 s82, s82, s83\n"
    "v_bfe_u32 v222, v250, 4, 1\n"
    "v_lshlrev_b32 v222, 17, v222\n"
    "v_lshrrev_b32 v223, 5, v250\n"
    "v_lshlrev_b32 v223, 8, v223\n"
    "v_add_u32 v222, v222, v223\n"
    "v_and_b32 v223, 15, v250\n"
    "v_lshlrev_b32 v223, 4, v223\n"
    "v_add_u32 v222, v222, v223\n"
    "v_accvgpr_read_b32 v64, a0\n"
    "v_accvgpr_read_b32 v65, a1\n"
    "v_accvgpr_read_b32 v66, a2\n"
    "v_accvgpr_read_b32 v67, a3\n"
    "v_accvgpr_read_b32 v68, a4\n"
    "v_accvgpr_read_b32 v69, a5\n"
    "v_accvgpr_read_b32 v70, a6\n"
    "v_accvgpr_read_b32 v71, a7\n"
    "v_accvgpr_read_b32 v72, a8\n"
    "v_accvgpr_read_b32 v73, a9\n"
    "v_accvgpr_read_b32 v74, a10\n"
    "v_accvgpr_read_b32 v75, a11\n"
    "v_accvgpr_read_b32 v76, a12\n"
    "v_accvgpr_read_b32 v77, a13\n"
    "v_accvgpr_read_b32 v78, a14\n"
    "v_accvgpr_read_b32 v79, a15\n"
    "v_mul_f32 v64, v64, v219\n"
    "v_mul_f32 v65, v65, v219\n"
    "v_mul_f32 v66, v66, v219\n"
    "v_mul_f32 v67, v67, v219\n"
    "v_mul_f32 v68, v68, v219\n"
    "v_mul_f32 v69, v69, v219\n"
    "v_mul_f32 v70, v70, v219\n"
    "v_mul_f32 v71, v71, v219\n"
    "v_mul_f32 v72, v72, v219\n"
    "v_mul_f32 v73, v73, v219\n"
    "v_mul_f32 v74, v74, v219\n"
    "v_mul_f32 v75, v75, v219\n"
    "v_mul_f32 v76, v76, v219\n"
    "v_mul_f32 v77, v77, v219\n"
    "v_mul_f32 v78, v78, v219\n"
    "v_mul_f32 v79, v79, v219\n"
    "v_cvt_pk_bf16_f32 v80, v64, v65\n"
    "v_cvt_pk_bf16_f32 v81, v66, v67\n"
    "v_cvt_pk_bf16_f32 v82, v68, v69\n"
    "v_cvt_pk_bf16_f32 v83, v70, v71\n"
    "v_cvt_pk_bf16_f32 v84, v72, v73\n"
    "v_cvt_pk_bf16_f32 v85, v74, v75\n"
    "v_cvt_pk_bf16_f32 v86, v76, v77\n"
    "v_cvt_pk_bf16_f32 v87, v78, v79\n"
    "buffer_store_dwordx4 v[80:83], v222, s[44:47], s82 offen sc1\n"
    "buffer_store_dwordx4 v[84:87], v222, s[44:47], s82 offen offset:512 sc1\n"
    "v_accvgpr_read_b32 v96, a32\n"
    "v_accvgpr_read_b32 v97, a33\n"
    "v_accvgpr_read_b32 v98, a34\n"
    "v_accvgpr_read_b32 v99, a35\n"
    "v_accvgpr_read_b32 v100, a36\n"
    "v_accvgpr_read_b32 v101, a37\n"
    "v_accvgpr_read_b32 v102, a38\n"
    "v_accvgpr_read_b32 v103, a39\n"
    "v_accvgpr_read_b32 v104, a40\n"
    "v_accvgpr_read_b32 v105, a41\n"
    "v_accvgpr_read_b32 v106, a42\n"
    "v_accvgpr_read_b32 v107, a43\n"
    "v_accvgpr_read_b32 v108, a44\n"
    "v_accvgpr_read_b32 v109, a45\n"
    "v_accvgpr_read_b32 v110, a46\n"
    "v_accvgpr_read_b32 v111, a47\n"
    "v_mul_f32 v96, v96, v219\n"
    "v_mul_f32 v97, v97, v219\n"
    "v_mul_f32 v98, v98, v219\n"
    "v_mul_f32 v99, v99, v219\n"
    "v_mul_f32 v100, v100, v219\n"
    "v_mul_f32 v101, v101, v219\n"
    "v_mul_f32 v102, v102, v219\n"
    "v_mul_f32 v103, v103, v219\n"
    "v_mul_f32 v104, v104, v219\n"
    "v_mul_f32 v105, v105, v219\n"
    "v_mul_f32 v106, v106, v219\n"
    "v_mul_f32 v107, v107, v219\n"
    "v_mul_f32 v108, v108, v219\n"
    "v_mul_f32 v109, v109, v219\n"
    "v_mul_f32 v110, v110, v219\n"
    "v_mul_f32 v111, v111, v219\n"
    "v_cvt_pk_bf16_f32 v112, v96, v97\n"
    "v_cvt_pk_bf16_f32 v113, v98, v99\n"
    "v_cvt_pk_bf16_f32 v114, v100, v101\n"
    "v_cvt_pk_bf16_f32 v115, v102, v103\n"
    "v_cvt_pk_bf16_f32 v116, v104, v105\n"
    "v_cvt_pk_bf16_f32 v117, v106, v107\n"
    "v_cvt_pk_bf16_f32 v118, v108, v109\n"
    "v_cvt_pk_bf16_f32 v119, v110, v111\n"
    "buffer_store_dwordx4 v[112:115], v222, s[44:47], s82 offen offset:1024 sc1\n"
    "buffer_store_dwordx4 v[116:119], v222, s[44:47], s82 offen offset:1536 sc1\n"
    "v_accvgpr_read_b32 v64, a64\n"
    "v_accvgpr_read_b32 v65, a65\n"
    "v_accvgpr_read_b32 v66, a66\n"
    "v_accvgpr_read_b32 v67, a67\n"
    "v_accvgpr_read_b32 v68, a68\n"
    "v_accvgpr_read_b32 v69, a69\n"
    "v_accvgpr_read_b32 v70, a70\n"
    "v_accvgpr_read_b32 v71, a71\n"
    "v_accvgpr_read_b32 v72, a72\n"
    "v_accvgpr_read_b32 v73, a73\n"
    "v_accvgpr_read_b32 v74, a74\n"
    "v_accvgpr_read_b32 v75, a75\n"
    "v_accvgpr_read_b32 v76, a76\n"
    "v_accvgpr_read_b32 v77, a77\n"
    "v_accvgpr_read_b32 v78, a78\n"
    "v_accvgpr_read_b32 v79, a79\n"
    "v_mul_f32 v64, v64, v219\n"
    "v_mul_f32 v65, v65, v219\n"
    "v_mul_f32 v66, v66, v219\n"
    "v_mul_f32 v67, v67, v219\n"
    "v_mul_f32 v68, v68, v219\n"
    "v_mul_f32 v69, v69, v219\n"
    "v_mul_f32 v70, v70, v219\n"
    "v_mul_f32 v71, v71, v219\n"
    "v_mul_f32 v72, v72, v219\n"
    "v_mul_f32 v73, v73, v219\n"
    "v_mul_f32 v74, v74, v219\n"
    "v_mul_f32 v75, v75, v219\n"
    "v_mul_f32 v76, v76, v219\n"
    "v_mul_f32 v77, v77, v219\n"
    "v_mul_f32 v78, v78, v219\n"
    "v_mul_f32 v79, v79, v219\n"
    "v_cvt_pk_bf16_f32 v80, v64, v65\n"
    "v_cvt_pk_bf16_f32 v81, v66, v67\n"
    "v_cvt_pk_bf16_f32 v82, v68, v69\n"
    "v_cvt_pk_bf16_f32 v83, v70, v71\n"
    "v_cvt_pk_bf16_f32 v84, v72, v73\n"
    "v_cvt_pk_bf16_f32 v85, v74, v75\n"
    "v_cvt_pk_bf16_f32 v86, v76, v77\n"
    "v_cvt_pk_bf16_f32 v87, v78, v79\n"
    "buffer_store_dwordx4 v[80:83], v222, s[44:47], s82 offen offset:2048 sc1\n"
    "buffer_store_dwordx4 v[84:87], v222, s[44:47], s82 offen offset:2560 sc1\n"
    "v_accvgpr_read_b32 v96, a96\n"
    "v_accvgpr_read_b32 v97, a97\n"
    "v_accvgpr_read_b32 v98, a98\n"
    "v_accvgpr_read_b32 v99, a99\n"
    "v_accvgpr_read_b32 v100, a100\n"
    "v_accvgpr_read_b32 v101, a101\n"
    "v_accvgpr_read_b32 v102, a102\n"
    "v_accvgpr_read_b32 v103, a103\n"
    "v_accvgpr_read_b32 v104, a104\n"
    "v_accvgpr_read_b32 v105, a105\n"
    "v_accvgpr_read_b32 v106, a106\n"
    "v_accvgpr_read_b32 v107, a107\n"
    "v_accvgpr_read_b32 v108, a108\n"
    "v_accvgpr_read_b32 v109, a109\n"
    "v_accvgpr_read_b32 v110, a110\n"
    "v_accvgpr_read_b32 v111, a111\n"
    "v_mul_f32 v96, v96, v219\n"
    "v_mul_f32 v97, v97, v219\n"
    "v_mul_f32 v98, v98, v219\n"
    "v_mul_f32 v99, v99, v219\n"
    "v_mul_f32 v100, v100, v219\n"
    "v_mul_f32 v101, v101, v219\n"
    "v_mul_f32 v102, v102, v219\n"
    "v_mul_f32 v103, v103, v219\n"
    "v_mul_f32 v104, v104, v219\n"
    "v_mul_f32 v105, v105, v219\n"
    "v_mul_f32 v106, v106, v219\n"
    "v_mul_f32 v107, v107, v219\n"
    "v_mul_f32 v108, v108, v219\n"
    "v_mul_f32 v109, v109, v219\n"
    "v_mul_f32 v110, v110, v219\n"
    "v_mul_f32 v111, v111, v219\n"
    "v_cvt_pk_bf16_f32 v112, v96, v97\n"
    "v_cvt_pk_bf16_f32 v113, v98, v99\n"
    "v_cvt_pk_bf16_f32 v114, v100, v101\n"
    "v_cvt_pk_bf16_f32 v115, v102, v103\n"
    "v_cvt_pk_bf16_f32 v116, v104, v105\n"
    "v_cvt_pk_bf16_f32 v117, v106, v107\n"
    "v_cvt_pk_bf16_f32 v118, v108, v109\n"
    "v_cvt_pk_bf16_f32 v119, v110, v111\n"
    "buffer_store_dwordx4 v[112:115], v222, s[44:47], s82 offen offset:3072 sc1\n"
    "buffer_store_dwordx4 v[116:119], v222, s[44:47], s82 offen offset:3584 sc1\n"
    "s_cmp_eq_u32 s76, 0\n"
    "s_cbranch_scc1 L_drain_%=\n"
    "L_s_%=:\n"
    "s_waitcnt lgkmcnt(4)\n"
    "v_mfma_f32_32x32x16_bf16 v[64:79], a[192:195], a[160:163], v[176:191]\n"
    "ds_read_b128 a[224:227], v204 offset:8192\n"
    "v_exp_f32 v32, v32\n"
    "v_exp_f32 v33, v33\n"
    "v_cvt_pk_bf16_f32 v128, v0, v1\n"
    "v_cvt_pk_bf16_f32 v129, v2, v3\n"
    "v_mfma_f32_32x32x16_bf16 v[64:79], a[196:199], a[164:167], v[64:79]\n"
    "buffer_load_dwordx4 v251, s[52:55], s78 offen offset:2048 sc1 lds\n"
    "ds_read_b128 a[228:231], v204 offset:9216\n"
    "v_add_f32 v198, v198, v32\n"
    "v_exp_f32 v34, v34\n"
    "v_cvt_pk_bf16_f32 v130, v4, v5\n"
    "v_cvt_pk_bf16_f32 v131, v6, v7\n"
    "v_mfma_f32_32x32x16_bf16 v[64:79], a[200:203], a[168:171], v[64:79]\n"
    "ds_read_b128 a[232:235], v204 offset:10240\n"
    "v_add_f32 v199, v199, v33\n"
    "v_add_f32 v198, v198, v34\n"
    "v_exp_f32 v35, v35\n"
    "v_cvt_pk_bf16_f32 v132, v8, v9\n"
    "v_cvt_pk_bf16_f32 v133, v10, v11\n"
    "v_mfma_f32_32x32x16_bf16 v[64:79], a[204:207], a[172:175], v[64:79]\n"
    "ds_read_b128 a[236:239], v204 offset:11264\n"
    "v_add_f32 v199, v199, v35\n"
    "v_exp_f32 v36, v36\n"
    "v_cvt_pk_bf16_f32 v134, v12, v13\n"
    "v_cvt_pk_bf16_f32 v135, v14, v15\n"
    "v_cvt_pk_bf16_f32 v136, v16, v17\n"
    "s_waitcnt lgkmcnt(4)\n"
    "v_mfma_f32_32x32x16_bf16 v[64:79], a[208:211], a[176:179], v[64:79]\n"
    "ds_read_b128 a[240:243], v204 offset:12288\n"
    "v_add_f32 v198, v198, v36\n"
    "v_exp_f32 v37, v37\n"
    "v_cvt_pk_bf16_f32 v137, v18, v19\n"
    "v_cvt_pk_bf16_f32 v138, v20, v21\n"
    "v_cvt_pk_bf16_f32 v139, v22, v23\n"
    "v_mfma_f32_32x32x16_bf16 v[64:79], a[212:215], a[180:183], v[64:79]\n"
    "buffer_load_dwordx4 v251, s[52:55], s78 offen offset:3072 sc1 lds\n"
    "ds_read_b128 a[244:247], v204 offset:13312\n"
    "v_add_f32 v199, v199, v37\n"
    "v_exp_f32 v38, v38\n"
    "v_cvt_pk_bf16_f32 v140, v24, v25\n"
    "v_cvt_pk_bf16_f32 v141, v26, v27\n"
    "v_mfma_f32_32x32x16_bf16 v[64:79], a[216:219], a[184:187], v[64:79]\n"
    "ds_read_b128 a[248:251], v204 offset:14336\n"
    "v_add_f32 v198, v198, v38\n"
    "v_exp_f32 v39, v39\n"
    "v_cvt_pk_bf16_f32 v142, v28, v29\n"
    "v_cvt_pk_bf16_f32 v143, v30, v31\n"
    "v_cvt_pk_bf16_f32 v144, v32, v33\n"
    "v_mfma_f32_32x32x16_bf16 v[64:79], a[220:223], a[188:191], v[64:79]\n"
    "ds_read_b128 a[252:255], v204 offset:15360\n"
    "v_add_f32 v199, v199, v39\n"
    "v_exp_f32 v40, v40\n"
    "v_cvt_pk_bf16_f32 v145, v34, v35\n"
    "v_cvt_pk_bf16_f32 v146, v36, v37\n"
    "v_cvt_pk_bf16_f32 v147, v38, v39\n"
    "s_waitcnt lgkmcnt(4)\n"
    "v_mfma_f32_32x32x16_bf16 v[80:95], a[224:227], a[160:163], v[176:191]\n"
    "ds_read_b128 a[192:195], v204 offset:16384\n"
    "v_add_f32 v198, v198, v40\n"
    "v_exp_f32 v41, v41\n"
    "s_add_u32 m0, s81, 4096\n"
    "v_mfma_f32_32x32x16_bf16 v[80:95], a[228:231], a[164:167], v[80:95]\n"
    "buffer_load_dwordx4 v248, s[52:55], s78 offen sc1 lds\n"
    "ds_read_b128 a[196:199], v204 offset:17408\n"
    "v_add_f32 v199, v199, v41\n"
    "v_exp_f32 v42, v42\n"
    "v_exp_f32 v43, v43\n"
    "v_mfma_f32_32x32x16_bf16 v[80:95], a[232:235], a[168:171], v[80:95]\n"
    "ds_read_b128 a[200:203], v204 offset:18432\n"
    "v_add_f32 v198, v198, v42\n"
    "v_add_f32 v199, v199, v43\n"
    "v_exp_f32 v44, v44\n"
    "v_cvt_pk_bf16_f32 v148, v40, v41\n"
    "v_cvt_pk_bf16_f32 v149, v42, v43\n"
    "v_mfma_f32_32x32x16_bf16 v[80:95], a[236:239], a[172:175], v[80:95]\n"
    "ds_read_b128 a[204:207], v204 offset:19456\n"
    "v_add_f32 v198, v198, v44\n"
    "v_exp_f32 v45, v45\n"
    "s_waitcnt lgkmcnt(4)\n"
    "v_mfma_f32_32x32x16_bf16 v[80:95], a[240:243], a[176:179], v[80:95]\n"
    "ds_read_b128 a[208:211], v204 offset:20480\n"
    "v_add_f32 v199, v199, v45\n"
    "v_exp_f32 v46, v46\n"
    "v_cvt_pk_bf16_f32 v150, v44, v45\n"
    "v_mfma_f32_32x32x16_bf16 v[80:95], a[244:247], a[180:183], v[80:95]\n"
    "buffer_load_dwordx4 v248, s[52:55], s78 offen offset:1024 sc1 lds\n"
    "ds_read_b128 a[212:215], v204 offset:21504\n"
    "v_add_f32 v198, v198, v46\n"
    "v_exp_f32 v47, v47\n"
    "v_mfma_f32_32x32x16_bf16 v[80:95], a[248:251], a[184:187], v[80:95]\n"
    "ds_read_b128 a[216:219], v204 offset:22528\n"
    "v_add_f32 v199, v199, v47\n"
    "v_exp_f32 v48, v48\n"
    "v_cvt_pk_bf16_f32 v151, v46, v47\n"
    "v_mfma_f32_32x32x16_bf16 v[80:95], a[252:255], a[188:191], v[80:95]\n"
    "ds_read_b128 a[220:223], v204 offset:23552\n"
    "v_add_f32 v198, v198, v48\n"
    "v_exp_f32 v49, v49\n"
    "s_waitcnt lgkmcnt(4)\n"
    "v_mfma_f32_32x32x16_bf16 v[96:111], a[192:195], a[160:163], v[176:191]\n"
    "ds_read_b128 a[224:227], v204 offset:24576\n"
    "v_add_f32 v199, v199, v49\n"
    "v_exp_f32 v50, v50\n"
    "v_cvt_pk_bf16_f32 v152, v48, v49\n"
    "v_mfma_f32_32x32x16_bf16 v[96:111], a[196:199], a[164:167], v[96:111]\n"
    "buffer_load_dwordx4 v248, s[52:55], s78 offen offset:2048 sc1 lds\n"
    "ds_read_b128 a[228:231], v204 offset:25600\n"
    "v_add_f32 v198, v198, v50\n"
    "v_exp_f32 v51, v51\n"
    "v_mfma_f32_32x32x16_bf16 v[96:111], a[200:203], a[168:171], v[96:111]\n"
    "ds_read_b128 a[232:235], v204 offset:26624\n"
    "v_add_f32 v199, v199, v51\n"
    "v_exp_f32 v52, v52\n"
    "v_exp_f32 v53, v53\n"
    "v_cvt_pk_bf16_f32 v153, v50, v51\n"
    "v_mfma_f32_32x32x16_bf16 v[96:111], a[204:207], a[172:175], v[96:111]\n"
    "ds_read_b128 a[236:239], v204 offset:27648\n"
    "v_add_f32 v198, v198, v52\n"
    "v_add_f32 v199, v199, v53\n"
    "v_exp_f32 v54, v54\n"
    "v_cvt_pk_bf16_f32 v154, v52, v53\n"
    "s_waitcnt lgkmcnt(4)\n"
    "v_mfma_f32_32x32x16_bf16 v[96:111], a[208:211], a[176:179], v[96:111]\n"
    "ds_read_b128 a[240:243], v204 offset:28672\n"
    "v_exp_f32 v55, v55\n"
    "v_max3_f32 v193, v64, v65, v66\n"
    "v_max3_f32 v193, v193, v67, v68\n"
    "v_max3_f32 v193, v193, v69, v70\n"
    "v_max3_f32 v193, v193, v71, v72\n"
    "v_mfma_f32_32x32x16_bf16 v[96:111], a[212:215], a[180:183], v[96:111]\n"
    "buffer_load_dwordx4 v248, s[52:55], s78 offen offset:3072 sc1 lds\n"
    "ds_read_b128 a[244:247], v204 offset:29696\n"
    "v_exp_f32 v56, v56\n"
    "v_max3_f32 v193, v193, v73, v74\n"
    "v_max3_f32 v193, v193, v75, v76\n"
    "v_max3_f32 v193, v193, v77, v78\n"
    "v_mfma_f32_32x32x16_bf16 v[96:111], a[216:219], a[184:187], v[96:111]\n"
    "ds_read_b128 a[248:251], v204 offset:30720\n"
    "v_exp_f32 v57, v57\n"
    "v_max_f32 v193, v193, v79\n"
    "v_max3_f32 v192, v80, v81, v82\n"
    "v_max3_f32 v192, v192, v83, v84\n"
    "v_max3_f32 v192, v192, v85, v86\n"
    "v_mfma_f32_32x32x16_bf16 v[96:111], a[220:223], a[188:191], v[96:111]\n"
    "ds_read_b128 a[252:255], v204 offset:31744\n"
    "v_exp_f32 v58, v58\n"
    "v_max3_f32 v192, v192, v87, v88\n"
    "v_max3_f32 v192, v192, v89, v90\n"
    "v_max3_f32 v192, v192, v91, v92\n"
    "v_max3_f32 v192, v192, v93, v94\n"
    "s_waitcnt lgkmcnt(4)\n"
    "v_mfma_f32_32x32x16_bf16 v[112:127], a[224:227], a[160:163], v[176:191]\n"
    "ds_read_b128 a[192:195], v205\n"
    "v_add_f32 v198, v198, v54\n"
    "v_add_f32 v199, v199, v55\n"
    "v_exp_f32 v59, v59\n"
    "v_max_f32 v192, v192, v95\n"
    "s_add_u32 m0, s79, 0\n"
    "v_mfma_f32_32x32x16_bf16 v[112:127], a[228:231], a[164:167], v[112:127]\n"
    "buffer_load_dwordx4 v251, s[48:51], s77 offen sc1 lds\n"
    "ds_read_b128 a[196:199], v205 offset:2048\n"
    "v_add_f32 v198, v198, v56\n"
    "v_add_f32 v199, v199, v57\n"
    "v_add_f32 v198, v198, v58\n"
    "v_exp_f32 v60, v60\n"
    "v_mfma_f32_32x32x16_bf16 v[112:127], a[232:235], a[168:171], v[112:127]\n"
    "ds_read_b128 a[200:203], v205 offset:4096\n"
    "v_add_f32 v199, v199, v59\n"
    "v_add_f32 v198, v198, v60\n"
    "v_exp_f32 v61, v61\n"
    "s_add_u32 s78, s78, 32768\n"
    "s_xor_b32 s81, s81, 229376\n"
    "v_mfma_f32_32x32x16_bf16 v[112:127], a[236:239], a[172:175], v[112:127]\n"
    "ds_read_b128 a[204:207], v205 offset:6144\n"
    "v_add_f32 v199, v199, v61\n"
    "v_exp_f32 v62, v62\n"
    "v_exp_f32 v63, v63\n"
    "v_cvt_pk_bf16_f32 v155, v54, v55\n"
    "s_waitcnt lgkmcnt(4)\n"
    "v_mfma_f32_32x32x16_bf16 v[112:127], a[240:243], a[176:179], v[112:127]\n"
    "ds_read_b128 a[208:211], v205 offset:1024\n"
    "v_add_f32 v198, v198, v62\n"
    "v_add_f32 v199, v199, v63\n"
    "v_cvt_pk_bf16_f32 v156, v56, v57\n"
    "v_cvt_pk_bf16_f32 v157, v58, v59\n"
    "v_cvt_pk_bf16_f32 v158, v60, v61\n"
    "v_mfma_f32_32x32x16_bf16 v[112:127], a[244:247], a[180:183], v[112:127]\n"
    "buffer_load_dwordx4 v251, s[48:51], s77 offen offset:1024 sc1 lds\n"
    "ds_read_b128 a[212:215], v205 offset:3072\n"
    "v_cvt_pk_bf16_f32 v159, v62, v63\n"
    "v_mfma_f32_32x32x16_bf16 v[112:127], a[248:251], a[184:187], v[112:127]\n"
    "ds_read_b128 a[216:219], v205 offset:5120\n"
    "v_mfma_f32_32x32x16_bf16 v[112:127], a[252:255], a[188:191], v[112:127]\n"
    "ds_read_b128 a[220:223], v205 offset:7168\n"
    "s_waitcnt lgkmcnt(4)\n"
    "v_mfma_f32_32x32x16_bf16 a[16:31], a[192:195], v[128:131], a[16:31]\n"
    "ds_read_b128 a[224:227], v205 offset:8192\n"
    "v_mfma_f32_32x32x16_bf16 a[48:63], a[196:199], v[128:131], a[48:63]\n"
    "buffer_load_dwordx4 v251, s[48:51], s77 offen offset:2048 sc1 lds\n"
    "ds_read_b128 a[228:231], v205 offset:10240\n"
    "v_mfma_f32_32x32x16_bf16 a[80:95], a[200:203], v[128:131], a[80:95]\n"
    "ds_read_b128 a[232:235], v205 offset:12288\n"
    "v_mfma_f32_32x32x16_bf16 a[112:127], a[204:207], v[128:131], a[112:127]\n"
    "ds_read_b128 a[236:239], v205 offset:14336\n"
    "s_waitcnt lgkmcnt(4)\n"
    "v_mfma_f32_32x32x16_bf16 a[16:31], a[208:211], v[132:135], a[16:31]\n"
    "ds_read_b128 a[240:243], v205 offset:9216\n"
    "v_max3_f32 v193, v193, v96, v97\n"
    "v_max3_f32 v193, v193, v98, v99\n"
    "v_max3_f32 v193, v193, v100, v101\n"
    "v_max3_f32 v193, v193, v102, v103\n"
    "v_max3_f32 v193, v193, v104, v105\n"
    "v_mfma_f32_32x32x16_bf16 a[48:63], a[212:215], v[132:135], a[48:63]\n"
    "buffer_load_dwordx4 v251, s[48:51], s77 offen offset:3072 sc1 lds\n"
    "ds_read_b128 a[244:247], v205 offset:11264\n"
    "v_max3_f32 v193, v193, v106, v107\n"
    "v_max3_f32 v193, v193, v108, v109\n"
    "v_max3_f32 v193, v193, v110, v111\n"
    "v_max3_f32 v192, v192, v112, v113\n"
    "v_mfma_f32_32x32x16_bf16 a[80:95], a[216:219], v[132:135], a[80:95]\n"
    "ds_read_b128 a[248:251], v205 offset:13312\n"
    "v_max3_f32 v192, v192, v114, v115\n"
    "v_max3_f32 v192, v192, v116, v117\n"
    "v_max3_f32 v192, v192, v118, v119\n"
    "v_max3_f32 v192, v192, v120, v121\n"
    "v_max3_f32 v192, v192, v122, v123\n"
    "v_mfma_f32_32x32x16_bf16 a[112:127], a[220:223], v[132:135], a[112:127]\n"
    "ds_read_b128 a[252:255], v205 offset:15360\n"
    "v_max3_f32 v192, v192, v124, v125\n"
    "v_max3_f32 v192, v192, v126, v127\n"
    "v_max_f32 v193, v192, v193\n"
    "v_cmp_lt_f32 vcc, s90, v193\n"
    "s_waitcnt lgkmcnt(4)\n"
    "v_mfma_f32_32x32x16_bf16 a[16:31], a[224:227], v[136:139], a[16:31]\n"
    "ds_read_b128 a[192:195], v205 offset:16384\n"
    "s_cbranch_vccnz L_s1_ra_%=\n"
    "L_s1_ra_%=r:\n"
    "s_add_u32 m0, s79, 4096\n"
    "v_mfma_f32_32x32x16_bf16 a[48:63], a[228:231], v[136:139], a[48:63]\n"
    "buffer_load_dwordx4 v248, s[48:51], s77 offen sc1 lds\n"
    "ds_read_b128 a[196:199], v205 offset:18432\n"
    "v_mfma_f32_32x32x16_bf16 a[80:95], a[232:235], v[136:139], a[80:95]\n"
    "ds_read_b128 a[200:203], v205 offset:20480\n"
    "v_exp_f32 v64, v64\n"
    "v_exp_f32 v65, v65\n"
    "v_mfma_f32_32x32x16_bf16 a[112:127], a[236:239], v[136:139], a[112:127]\n"
    "ds_read_b128 a[204:207], v205 offset:22528\n"
    "v_add_f32 v198, v198, v64\n"
    "v_add_f32 v199, v199, v65\n"
    "v_exp_f32 v66, v66\n"
    "v_exp_f32 v67, v67\n"
    "s_waitcnt lgkmcnt(4)\n"
    "v_mfma_f32_32x32x16_bf16 a[16:31], a[240:243], v[140:143], a[16:31]\n"
    "ds_read_b128 a[208:211], v205 offset:17408\n"
    "v_add_f32 v198, v198, v66\n"
    "v_add_f32 v199, v199, v67\n"
    "v_exp_f32 v68, v68\n"
    "s_add_u32 s79, s80, s94\n"
    "s_add_u32 s80, s80, 32768\n"
    "v_mfma_f32_32x32x16_bf16 a[48:63], a[244:247], v[140:143], a[48:63]\n"
    "buffer_load_dwordx4 v248, s[48:51], s77 offen offset:1024 sc1 lds\n"
    "ds_read_b128 a[212:215], v205 offset:19456\n"
    "v_add_f32 v198, v198, v68\n"
    "v_exp_f32 v69, v69\n"
    "s_cmp_eq_u32 s80, 98304\n"
    "s_cselect_b32 s80, 0, s80\n"
    "v_mfma_f32_32x32x16_bf16 a[80:95], a[248:251], v[140:143], a[80:95]\n"
    "ds_read_b128 a[216:219], v205 offset:21504\n"
    "v_add_f32 v199, v199, v69\n"
    "v_exp_f32 v70, v70\n"
    "v_exp_f32 v71, v71\n"
    "v_add_u32 v204, s80, v251\n"
    "v_mfma_f32_32x32x16_bf16 a[112:127], a[252:255], v[140:143], a[112:127]\n"
    "ds_read_b128 a[220:223], v205 offset:23552\n"
    "v_add_f32 v198, v198, v70\n"
    "v_add_f32 v199, v199, v71\n"
    "v_exp_f32 v72, v72\n"
    "v_exp_f32 v73, v73\n"
    "s_waitcnt lgkmcnt(4)\n"
    "v_mfma_f32_32x32x16_bf16 a[16:31], a[192:195], v[144:147], a[16:31]\n"
    "ds_read_b128 a[224:227], v205 offset:24576\n"
    "v_add_f32 v198, v198, v72\n"
    "v_add_f32 v199, v199, v73\n"
    "v_exp_f32 v74, v74\n"
    "v_exp_f32 v75, v75\n"
    "v_mfma_f32_32x32x16_bf16 a[48:63], a[196:199], v[144:147], a[48:63]\n"
    "buffer_load_dwordx4 v248, s[48:51], s77 offen offset:2048 sc1 lds\n"
    "ds_read_b128 a[228:231], v205 offset:26624\n"
    "v_add_f32 v198, v198, v74\n"
    "v_add_f32 v199, v199, v75\n"
    "v_exp_f32 v76, v76\n"
    "v_mfma_f32_32x32x16_bf16 a[80:95], a[200:203], v[144:147], a[80:95]\n"
    "ds_read_b128 a[232:235], v205 offset:28672\n"
    "v_add_f32 v198, v198, v76\n"
    "v_exp_f32 v77, v77\n"
    "v_exp_f32 v78, v78\n"
    "v_mfma_f32_32x32x16_bf16 a[112:127], a[204:207], v[144:147], a[112:127]\n"
    "ds_read_b128 a[236:239], v205 offset:30720\n"
    "v_add_f32 v199, v199, v77\n"
    "v_add_f32 v198, v198, v78\n"
    "v_exp_f32 v79, v79\n"
    "v_exp_f32 v80, v80\n"
    "s_waitcnt lgkmcnt(4)\n"
    "v_mfma_f32_32x32x16_bf16 a[16:31], a[208:211], v[148:151], a[16:31]\n"
    "ds_read_b128 a[240:243], v205 offset:25600\n"
    "v_add_f32 v199, v199, v79\n"
    "v_add_f32 v198, v198, v80\n"
    "v_exp_f32 v81, v81\n"
    "v_mfma_f32_32x32x16_bf16 a[48:63], a[212:215], v[148:151], a[48:63]\n"
    "buffer_load_dwordx4 v248, s[48:51], s77 offen offset:3072 sc1 lds\n"
    "ds_read_b128 a[244:247], v205 offset:27648\n"
    "v_add_f32 v199, v199, v81\n"
    "v_exp_f32 v82, v82\n"
    "v_exp_f32 v83, v83\n"
    "v_mfma_f32_32x32x16_bf16 a[80:95], a[216:219], v[148:151], a[80:95]\n"
    "ds_read_b128 a[248:251], v205 offset:29696\n"
    "v_add_f32 v198, v198, v82\n"
    "v_add_f32 v199, v199, v83\n"
    "v_exp_f32 v84, v84\n"
    "s_add_u32 s77, s77, 32768\n"
    "v_mfma_f32_32x32x16_bf16 a[112:127], a[220:223], v[148:151], a[112:127]\n"
    "ds_read_b128 a[252:255], v205 offset:31744\n"
    "v_add_f32 v198, v198, v84\n"
    "v_exp_f32 v85, v85\n"
    "v_exp_f32 v86, v86\n"
    "s_waitcnt vmcnt(8) lgkmcnt(0)\n"
    "s_barrier\n"
    "v_mfma_f32_32x32x16_bf16 a[16:31], a[224:227], v[152:155], a[16:31]\n"
    "ds_read_b128 a[192:195], v204\n"
    "v_add_f32 v199, v199, v85\n"
    "v_exp_f32 v87, v87\n"
    "s_add_u32 m0, s81, 0\n"
    "v_mfma_f32_32x32x16_bf16 a[48:63], a[228:231], v[152:155], a[48:63]\n"
    "buffer_load_dwordx4 v251, s[52:55], s78 offen sc1 lds\n"
    "ds_read_b128 a[196:199], v204 offset:1024\n"
    "v_add_f32 v198, v198, v86\n"
    "v_exp_f32 v88, v88\n"
    "v_exp_f32 v89, v89\n"
    "v_mfma_f32_32x32x16_bf16 a[80:95], a[232:235], v[152:155], a[80:95]\n"
    "ds_read_b128 a[200:203], v204 offset:2048\n"
    "v_add_f32 v199, v199, v87\n"
    "v_add_f32 v198, v198, v88\n"
    "v_exp_f32 v90, v90\n"
    "v_exp_f32 v91, v91\n"
    "v_mfma_f32_32x32x16_bf16 a[112:127], a[236:239], v[152:155], a[112:127]\n"
    "ds_read_b128 a[204:207], v204 offset:3072\n"
    "v_add_f32 v199, v199, v89\n"
    "v_add_f32 v198, v198, v90\n"
    "v_exp_f32 v92, v92\n"
    "v_exp_f32 v93, v93\n"
    "v_mfma_f32_32x32x16_bf16 a[16:31], a[240:243], v[156:159], a[16:31]\n"
    "ds_read_b128 a[208:211], v204 offset:4096\n"
    "v_add_f32 v199, v199, v91\n"
    "v_add_f32 v198, v198, v92\n"
    "v_add_f32 v199, v199, v93\n"
    "v_exp_f32 v94, v94\n"
    "v_xor_b32 v205, 229376, v205\n"
    "v_mfma_f32_32x32x16_bf16 a[48:63], a[244:247], v[156:159], a[48:63]\n"
    "buffer_load_dwordx4 v251, s[52:55], s78 offen offset:1024 sc1 lds\n"
    "ds_read_b128 a[212:215], v204 offset:5120\n"
    "v_add_f32 v198, v198, v94\n"
    "v_exp_f32 v95, v95\n"
    "v_mfma_f32_32x32x16_bf16 a[80:95], a[248:251], v[156:159], a[80:95]\n"
    "ds_read_b128 a[216:219], v204 offset:6144\n"
    "v_add_f32 v199, v199, v95\n"
    "v_mfma_f32_32x32x16_bf16 a[112:127], a[252:255], v[156:159], a[112:127]\n"
    "ds_read_b128 a[220:223], v204 offset:7168\n"
    "s_cmp_lg_u32 s91, 0\n"
    "s_cbranch_scc0 L_rbs1_%=\n"
    "s_call_b64 s[98:99], L_rareb_%=\n"
    "L_rbs1_%=:\n"
    "s_sub_u32 s76, s76, 2\n"
    "s_cmp_eq_u32 s76, 0\n"
    "s_cselect_b32 s88, 1, 0\n"
    "s_waitcnt lgkmcnt(4)\n"
    "v_mfma_f32_32x32x16_bf16 v[0:15], a[192:195], a[160:163], v[176:191]\n"
    "ds_read_b128 a[224:227], v204 offset:8192\n"
    "v_exp_f32 v96, v96\n"
    "v_exp_f32 v97, v97\n"
    "v_cvt_pk_bf16_f32 v128, v64, v65\n"
    "v_cvt_pk_bf16_f32 v129, v66, v67\n"
    "v_mfma_f32_32x32x16_bf16 v[0:15], a[196:199], a[164:167], v[0:15]\n"
    "buffer_load_dwordx4 v251, s[52:55], s78 offen offset:2048 sc1 lds\n"
    "ds_read_b128 a[228:231], v204 offset:9216\n"
    "v_add_f32 v198, v198, v96\n"
    "v_exp_f32 v98, v98\n"
    "v_cvt_pk_bf16_f32 v130, v68, v69\n"
    "v_cvt_pk_bf16_f32 v131, v70, v71\n"
    "v_mfma_f32_32x32x16_bf16 v[0:15], a[200:203], a[168:171], v[0:15]\n"
    "ds_read_b128 a[232:235], v204 offset:10240\n"
    "v_add_f32 v199, v199, v97\n"
    "v_add_f32 v198, v198, v98\n"
    "v_exp_f32 v99, v99\n"
    "v_cvt_pk_bf16_f32 v132, v72, v73\n"
    "v_cvt_pk_bf16_f32 v133, v74, v75\n"
    "v_mfma_f32_32x32x16_bf16 v[0:15], a[204:207], a[172:175], v[0:15]\n"
    "ds_read_b128 a[236:239], v204 offset:11264\n"
    "v_add_f32 v199, v199, v99\n"
    "v_exp_f32 v100, v100\n"
    "v_cvt_pk_bf16_f32 v134, v76, v77\n"
    "v_cvt_pk_bf16_f32 v135, v78, v79\n"
    "v_cvt_pk_bf16_f32 v136, v80, v81\n"
    "s_waitcnt lgkmcnt(4)\n"
    "v_mfma_f32_32x32x16_bf16 v[0:15], a[208:211], a[176:179], v[0:15]\n"
    "ds_read_b128 a[240:243], v204 offset:12288\n"
    "v_add_f32 v198, v198, v100\n"
    "v_exp_f32 v101, v101\n"
    "v_cvt_pk_bf16_f32 v137, v82, v83\n"
    "v_cvt_pk_bf16_f32 v138, v84, v85\n"
    "v_cvt_pk_bf16_f32 v139, v86, v87\n"
    "v_mfma_f32_32x32x16_bf16 v[0:15], a[212:215], a[180:183], v[0:15]\n"
    "buffer_load_dwordx4 v251, s[52:55], s78 offen offset:3072 sc1 lds\n"
    "ds_read_b128 a[244:247], v204 offset:13312\n"
    "v_add_f32 v199, v199, v101\n"
    "v_exp_f32 v102, v102\n"
    "v_cvt_pk_bf16_f32 v140, v88, v89\n"
    "v_cvt_pk_bf16_f32 v141, v90, v91\n"
    "v_mfma_f32_32x32x16_bf16 v[0:15], a[216:219], a[184:187], v[0:15]\n"
    "ds_read_b128 a[248:251], v204 offset:14336\n"
    "v_add_f32 v198, v198, v102\n"
    "v_exp_f32 v103, v103\n"
    "v_cvt_pk_bf16_f32 v142, v92, v93\n"
    "v_cvt_pk_bf16_f32 v143, v94, v95\n"
    "v_cvt_pk_bf16_f32 v144, v96, v97\n"
    "v_mfma_f32_32x32x16_bf16 v[0:15], a[220:223], a[188:191], v[0:15]\n"
    "ds_read_b128 a[252:255], v204 offset:15360\n"
    "v_add_f32 v199, v199, v103\n"
    "v_exp_f32 v104, v104\n"
    "v_cvt_pk_bf16_f32 v145, v98, v99\n"
    "v_cvt_pk_bf16_f32 v146, v100, v101\n"
    "v_cvt_pk_bf16_f32 v147, v102, v103\n"
    "s_waitcnt lgkmcnt(4)\n"
    "v_mfma_f32_32x32x16_bf16 v[16:31], a[224:227], a[160:163], v[176:191]\n"
    "ds_read_b128 a[192:195], v204 offset:16384\n"
    "v_add_f32 v198, v198, v104\n"
    "v_exp_f32 v105, v105\n"
    "s_add_u32 m0, s81, 4096\n"
    "v_mfma_f32_32x32x16_bf16 v[16:31], a[228:231], a[164:167], v[16:31]\n"
    "buffer_load_dwordx4 v248, s[52:55], s78 offen sc1 lds\n"
    "ds_read_b128 a[196:199], v204 offset:17408\n"
    "v_add_f32 v199, v199, v105\n"
    "v_exp_f32 v106, v106\n"
    "v_exp_f32 v107, v107\n"
    "v_mfma_f32_32x32x16_bf16 v[16:31], a[232:235], a[168:171], v[16:31]\n"
    "ds_read_b128 a[200:203], v204 offset:18432\n"
    "v_add_f32 v198, v198, v106\n"
    "v_add_f32 v199, v199, v107\n"
    "v_exp_f32 v108, v108\n"
    "v_cvt_pk_bf16_f32 v148, v104, v105\n"
    "v_cvt_pk_bf16_f32 v149, v106, v107\n"
    "v_mfma_f32_32x32x16_bf16 v[16:31], a[236:239], a[172:175], v[16:31]\n"
    "ds_read_b128 a[204:207], v204 offset:19456\n"
    "v_add_f32 v198, v198, v108\n"
    "v_exp_f32 v109, v109\n"
    "s_waitcnt lgkmcnt(4)\n"
    "v_mfma_f32_32x32x16_bf16 v[16:31], a[240:243], a[176:179], v[16:31]\n"
    "ds_read_b128 a[208:211], v204 offset:20480\n"
    "v_add_f32 v199, v199, v109\n"
    "v_exp_f32 v110, v110\n"
    "v_cvt_pk_bf16_f32 v150, v108, v109\n"
    "v_mfma_f32_32x32x16_bf16 v[16:31], a[244:247], a[180:183], v[16:31]\n"
    "buffer_load_dwordx4 v248, s[52:55], s78 offen offset:1024 sc1 lds\n"
    "ds_read_b128 a[212:215], v204 offset:21504\n"
    "v_add_f32 v198, v198, v110\n"
    "v_exp_f32 v111, v111\n"
    "v_mfma_f32_32x32x16_bf16 v[16:31], a[248:251], a[184:187], v[16:31]\n"
    "ds_read_b128 a[216:219], v204 offset:22528\n"
    "v_add_f32 v199, v199, v111\n"
    "v_exp_f32 v112, v112\n"
    "v_cvt_pk_bf16_f32 v151, v110, v111\n"
    "v_mfma_f32_32x32x16_bf16 v[16:31], a[252:255], a[188:191], v[16:31]\n"
    "ds_read_b128 a[220:223], v204 offset:23552\n"
    "v_add_f32 v198, v198, v112\n"
    "v_exp_f32 v113, v113\n"
    "s_waitcnt lgkmcnt(4)\n"
    "v_mfma_f32_32x32x16_bf16 v[32:47], a[192:195], a[160:163], v[176:191]\n"
    "ds_read_b128 a[224:227], v204 offset:24576\n"
    "v_add_f32 v199, v199, v113\n"
    "v_exp_f32 v114, v114\n"
    "v_cvt_pk_bf16_f32 v152, v112, v113\n"
    "v_mfma_f32_32x32x16_bf16 v[32:47], a[196:199], a[164:167], v[32:47]\n"
    "buffer_load_dwordx4 v248, s[52:55], s78 offen offset:2048 sc1 lds\n"
    "ds_read_b128 a[228:231], v204 offset:25600\n"
    "v_add_f32 v198, v198, v114\n"
    "v_exp_f32 v115, v115\n"
    "v_mfma_f32_32x32x16_bf16 v[32:47], a[200:203], a[168:171], v[32:47]\n"
    "ds_read_b128 a[232:235], v204 offset:26624\n"
    "v_add_f32 v199, v199, v115\n"
    "v_exp_f32 v116, v116\n"
    "v_exp_f32 v117, v117\n"
    "v_cvt_pk_bf16_f32 v153, v114, v115\n"
    "v_mfma_f32_32x32x16_bf16 v[32:47], a[204:207], a[172:175], v[32:47]\n"
    "ds_read_b128 a[236:239], v204 offset:27648\n"
    "v_add_f32 v198, v198, v116\n"
    "v_add_f32 v199, v199, v117\n"
    "v_exp_f32 v118, v118\n"
    "v_cvt_pk_bf16_f32 v154, v116, v117\n"
    "s_waitcnt lgkmcnt(4)\n"
    "v_mfma_f32_32x32x16_bf16 v[32:47], a[208:211], a[176:179], v[32:47]\n"
    "ds_read_b128 a[240:243], v204 offset:28672\n"
    "v_add_f32 v198, v198, v118\n"
    "v_exp_f32 v119, v119\n"
    "s_cmp_lg_u32 s88, 0\n"
    "s_cbranch_scc1 L_s0_m0_%=\n"
    "L_s0_m0_%=r:\n"
    "v_mfma_f32_32x32x16_bf16 v[32:47], a[212:215], a[180:183], v[32:47]\n"
    "buffer_load_dwordx4 v248, s[52:55], s78 offen offset:3072 sc1 lds\n"
    "ds_read_b128 a[244:247], v204 offset:29696\n"
    "v_exp_f32 v120, v120\n"
    "v_max3_f32 v193, v0, v1, v2\n"
    "v_max3_f32 v193, v193, v3, v4\n"
    "v_max3_f32 v193, v193, v5, v6\n"
    "v_mfma_f32_32x32x16_bf16 v[32:47], a[216:219], a[184:187], v[32:47]\n"
    "ds_read_b128 a[248:251], v204 offset:30720\n"
    "v_exp_f32 v121, v121\n"
    "v_max3_f32 v193, v193, v7, v8\n"
    "v_max3_f32 v193, v193, v9, v10\n"
    "v_max3_f32 v193, v193, v11, v12\n"
    "v_max3_f32 v193, v193, v13, v14\n"
    "v_mfma_f32_32x32x16_bf16 v[32:47], a[220:223], a[188:191], v[32:47]\n"
    "ds_read_b128 a[252:255], v204 offset:31744\n"
    "v_exp_f32 v122, v122\n"
    "v_max_f32 v193, v193, v15\n"
    "v_max3_f32 v192, v16, v17, v18\n"
    "v_max3_f32 v192, v192, v19, v20\n"
    "v_max3_f32 v192, v192, v21, v22\n"
    "s_waitcnt lgkmcnt(4)\n"
    "v_mfma_f32_32x32x16_bf16 v[48:63], a[224:227], a[160:163], v[176:191]\n"
    "ds_read_b128 a[192:195], v205\n"
    "v_exp_f32 v123, v123\n"
    "v_max3_f32 v192, v192, v23, v24\n"
    "v_max3_f32 v192, v192, v25, v26\n"
    "v_max3_f32 v192, v192, v27, v28\n"
    "s_add_u32 m0, s79, 0\n"
    "v_mfma_f32_32x32x16_bf16 v[48:63], a[228:231], a[164:167], v[48:63]\n"
    "buffer_load_dwordx4 v251, s[48:51], s77 offen sc1 lds\n"
    "ds_read_b128 a[196:199], v205 offset:2048\n"
    "v_add_f32 v199, v199, v119\n"
    "v_exp_f32 v124, v124\n"
    "v_max3_f32 v192, v192, v29, v30\n"
    "v_max_f32 v192, v192, v31\n"
    "v_mfma_f32_32x32x16_bf16 v[48:63], a[232:235], a[168:171], v[48:63]\n"
    "ds_read_b128 a[200:203], v205 offset:4096\n"
    "v_add_f32 v198, v198, v120\n"
    "v_add_f32 v199, v199, v121\n"
    "v_add_f32 v198, v198, v122\n"
    "v_add_f32 v199, v199, v123\n"
    "v_exp_f32 v125, v125\n"
    "v_mfma_f32_32x32x16_bf16 v[48:63], a[236:239], a[172:175], v[48:63]\n"
    "ds_read_b128 a[204:207], v205 offset:6144\n"
    "v_add_f32 v198, v198, v124\n"
    "v_add_f32 v199, v199, v125\n"
    "v_exp_f32 v126, v126\n"
    "v_exp_f32 v127, v127\n"
    "s_waitcnt lgkmcnt(4)\n"
    "v_mfma_f32_32x32x16_bf16 v[48:63], a[240:243], a[176:179], v[48:63]\n"
    "ds_read_b128 a[208:211], v205 offset:1024\n"
    "v_add_f32 v198, v198, v126\n"
    "v_add_f32 v199, v199, v127\n"
    "v_cvt_pk_bf16_f32 v155, v118, v119\n"
    "s_add_u32 s78, s78, 32768\n"
    "s_xor_b32 s81, s81, 229376\n"
    "v_mfma_f32_32x32x16_bf16 v[48:63], a[244:247], a[180:183], v[48:63]\n"
    "buffer_load_dwordx4 v251, s[48:51], s77 offen offset:1024 sc1 lds\n"
    "ds_read_b128 a[212:215], v205 offset:3072\n"
    "v_cvt_pk_bf16_f32 v156, v120, v121\n"
    "v_cvt_pk_bf16_f32 v157, v122, v123\n"
    "v_cvt_pk_bf16_f32 v158, v124, v125\n"
    "v_cvt_pk_bf16_f32 v159, v126, v127\n"
    "v_mfma_f32_32x32x16_bf16 v[48:63], a[248:251], a[184:187], v[48:63]\n"
    "ds_read_b128 a[216:219], v205 offset:5120\n"
    "v_mfma_f32_32x32x16_bf16 v[48:63], a[252:255], a[188:191], v[48:63]\n"
    "ds_read_b128 a[220:223], v205 offset:7168\n"
    "s_waitcnt lgkmcnt(4)\n"
    "v_mfma_f32_32x32x16_bf16 a[16:31], a[192:195], v[128:131], a[16:31]\n"
    "ds_read_b128 a[224:227], v205 offset:8192\n"
    "v_mfma_f32_32x32x16_bf16 a[48:63], a[196:199], v[128:131], a[48:63]\n"
    "buffer_load_dwordx4 v251, s[48:51], s77 offen offset:2048 sc1 lds\n"
    "ds_read_b128 a[228:231], v205 offset:10240\n"
    "v_mfma_f32_32x32x16_bf16 a[80:95], a[200:203], v[128:131], a[80:95]\n"
    "ds_read_b128 a[232:235], v205 offset:12288\n"
    "v_mfma_f32_32x32x16_bf16 a[112:127], a[204:207], v[128:131], a[112:127]\n"
    "ds_read_b128 a[236:239], v205 offset:14336\n"
    "s_waitcnt lgkmcnt(4)\n"
    "v_mfma_f32_32x32x16_bf16 a[16:31], a[208:211], v[132:135], a[16:31]\n"
    "ds_read_b128 a[240:243], v205 offset:9216\n"
    "s_cmp_lg_u32 s88, 0\n"
    "s_cbranch_scc1 L_s0_m1_%=\n"
    "L_s0_m1_%=r:\n"
    "v_mfma_f32_32x32x16_bf16 a[48:63], a[212:215], v[132:135], a[48:63]\n"
    "buffer_load_dwordx4 v251, s[48:51], s77 offen offset:3072 sc1 lds\n"
    "ds_read_b128 a[244:247], v205 offset:11264\n"
    "v_max3_f32 v193, v193, v32, v33\n"
    "v_max3_f32 v193, v193, v34, v35\n"
    "v_max3_f32 v193, v193, v36, v37\n"
    "v_max3_f32 v193, v193, v38, v39\n"
    "v_mfma_f32_32x32x16_bf16 a[80:95], a[216:219], v[132:135], a[80:95]\n"
    "ds_read_b128 a[248:251], v205 offset:13312\n"
    "v_max3_f32 v193, v193, v40, v41\n"
    "v_max3_f32 v193, v193, v42, v43\n"
    "v_max3_f32 v193, v193, v44, v45\n"
    "v_max3_f32 v193, v193, v46, v47\n"
    "v_max3_f32 v192, v192, v48, v49\n"
    "v_mfma_f32_32x32x16_bf16 a[112:127], a[220:223], v[132:135], a[112:127]\n"
    "ds_read_b128 a[252:255], v205 offset:15360\n"
    "v_max3_f32 v192, v192, v50, v51\n"
    "v_max3_f32 v192, v192, v52, v53\n"
    "v_max3_f32 v192, v192, v54, v55\n"
    "v_max3_f32 v192, v192, v56, v57\n"
    "v_max3_f32 v192, v192, v58, v59\n"
    "s_waitcnt lgkmcnt(4)\n"
    "v_mfma_f32_32x32x16_bf16 a[16:31], a[224:227], v[136:139], a[16:31]\n"
    "ds_read_b128 a[192:195], v205 offset:16384\n"
    "v_max3_f32 v192, v192, v60, v61\n"
    "v_max3_f32 v192, v192, v62, v63\n"
    "v_max_f32 v193, v192, v193\n"
    "v_cmp_lt_f32 vcc, s90, v193\n"
    "s_add_u32 m0, s79, 4096\n"
    "v_mfma_f32_32x32x16_bf16 a[48:63], a[228:231], v[136:139], a[48:63]\n"
    "buffer_load_dwordx4 v248, s[48:51], s77 offen sc1 lds\n"
    "ds_read_b128 a[196:199], v205 offset:18432\n"
    "s_cbranch_vccnz L_s0_ra_%=\n"
    "L_s0_ra_%=r:\n"
    "v_mfma_f32_32x32x16_bf16 a[80:95], a[232:235], v[136:139], a[80:95]\n"
    "ds_read_b128 a[200:203], v205 offset:20480\n"
    "v_exp_f32 v0, v0\n"
    "v_exp_f32 v1, v1\n"
    "v_mfma_f32_32x32x16_bf16 a[112:127], a[236:239], v[136:139], a[112:127]\n"
    "ds_read_b128 a[204:207], v205 offset:22528\n"
    "v_add_f32 v198, v198, v0\n"
    "v_add_f32 v199, v199, v1\n"
    "v_exp_f32 v2, v2\n"
    "v_exp_f32 v3, v3\n"
    "s_waitcnt lgkmcnt(4)\n"
    "v_mfma_f32_32x32x16_bf16 a[16:31], a[240:243], v[140:143], a[16:31]\n"
    "ds_read_b128 a[208:211], v205 offset:17408\n"
    "v_add_f32 v198, v198, v2\n"
    "v_add_f32 v199, v199, v3\n"
    "v_exp_f32 v4, v4\n"
    "s_add_u32 s79, s80, s94\n"
    "s_add_u32 s80, s80, 32768\n"
    "v_mfma_f32_32x32x16_bf16 a[48:63], a[244:247], v[140:143], a[48:63]\n"
    "buffer_load_dwordx4 v248, s[48:51], s77 offen offset:1024 sc1 lds\n"
    "ds_read_b128 a[212:215], v205 offset:19456\n"
    "v_add_f32 v198, v198, v4\n"
    "v_exp_f32 v5, v5\n"
    "s_cmp_eq_u32 s80, 98304\n"
    "s_cselect_b32 s80, 0, s80\n"
    "v_mfma_f32_32x32x16_bf16 a[80:95], a[248:251], v[140:143], a[80:95]\n"
    "ds_read_b128 a[216:219], v205 offset:21504\n"
    "v_add_f32 v199, v199, v5\n"
    "v_exp_f32 v6, v6\n"
    "v_exp_f32 v7, v7\n"
    "v_add_u32 v204, s80, v251\n"
    "v_mfma_f32_32x32x16_bf16 a[112:127], a[252:255], v[140:143], a[112:127]\n"
    "ds_read_b128 a[220:223], v205 offset:23552\n"
    "v_add_f32 v198, v198, v6\n"
    "v_add_f32 v199, v199, v7\n"
    "v_exp_f32 v8, v8\n"
    "v_exp_f32 v9, v9\n"
    "s_waitcnt lgkmcnt(4)\n"
    "v_mfma_f32_32x32x16_bf16 a[16:31], a[192:195], v[144:147], a[16:31]\n"
    "ds_read_b128 a[224:227], v205 offset:24576\n"
    "v_add_f32 v198, v198, v8\n"
    "v_add_f32 v199, v199, v9\n"
    "v_exp_f32 v10, v10\n"
    "v_exp_f32 v11, v11\n"
    "v_mfma_f32_32x32x16_bf16 a[48:63], a[196:199], v[144:147], a[48:63]\n"
    "buffer_load_dwordx4 v248, s[48:51], s77 offen offset:2048 sc1 lds\n"
    "ds_read_b128 a[228:231], v205 offset:26624\n"
    "v_add_f32 v198, v198, v10\n"
    "v_add_f32 v199, v199, v11\n"
    "v_exp_f32 v12, v12\n"
    "v_mfma_f32_32x32x16_bf16 a[80:95], a[200:203], v[144:147], a[80:95]\n"
    "ds_read_b128 a[232:235], v205 offset:28672\n"
    "v_add_f32 v198, v198, v12\n"
    "v_exp_f32 v13, v13\n"
    "v_exp_f32 v14, v14\n"
    "v_mfma_f32_32x32x16_bf16 a[112:127], a[204:207], v[144:147], a[112:127]\n"
    "ds_read_b128 a[236:239], v205 offset:30720\n"
    "v_add_f32 v199, v199, v13\n"
    "v_add_f32 v198, v198, v14\n"
    "v_exp_f32 v15, v15\n"
    "v_exp_f32 v16, v16\n"
    "s_waitcnt lgkmcnt(4)\n"
    "v_mfma_f32_32x32x16_bf16 a[16:31], a[208:211], v[148:151], a[16:31]\n"
    "ds_read_b128 a[240:243], v205 offset:25600\n"
    "v_add_f32 v199, v199, v15\n"
    "v_add_f32 v198, v198, v16\n"
    "v_exp_f32 v17, v17\n"
    "v_mfma_f32_32x32x16_bf16 a[48:63], a[212:215], v[148:151], a[48:63]\n"
    "buffer_load_dwordx4 v248, s[48:51], s77 offen offset:3072 sc1 lds\n"
    "ds_read_b128 a[244:247], v205 offset:27648\n"
    "v_add_f32 v199, v199, v17\n"
    "v_exp_f32 v18, v18\n"
    "v_exp_f32 v19, v19\n"
    "v_mfma_f32_32x32x16_bf16 a[80:95], a[216:219], v[148:151], a[80:95]\n"
    "ds_read_b128 a[248:251], v205 offset:29696\n"
    "v_add_f32 v198, v198, v18\n"
    "v_add_f32 v199, v199, v19\n"
    "v_exp_f32 v20, v20\n"
    "s_add_u32 s77, s77, 32768\n"
    "v_mfma_f32_32x32x16_bf16 a[112:127], a[220:223], v[148:151], a[112:127]\n"
    "ds_read_b128 a[252:255], v205 offset:31744\n"
    "v_add_f32 v198, v198, v20\n"
    "v_exp_f32 v21, v21\n"
    "v_exp_f32 v22, v22\n"
    "s_waitcnt vmcnt(8) lgkmcnt(0)\n"
    "s_barrier\n"
    "v_mfma_f32_32x32x16_bf16 a[16:31], a[224:227], v[152:155], a[16:31]\n"
    "ds_read_b128 a[192:195], v204\n"
    "v_add_f32 v199, v199, v21\n"
    "v_exp_f32 v23, v23\n"
    "s_add_u32 m0, s81, 0\n"
    "v_mfma_f32_32x32x16_bf16 a[48:63], a[228:231], v[152:155], a[48:63]\n"
    "buffer_load_dwordx4 v251, s[52:55], s78 offen sc1 lds\n"
    "ds_read_b128 a[196:199], v204 offset:1024\n"
    "v_add_f32 v198, v198, v22\n"
    "v_exp_f32 v24, v24\n"
    "v_exp_f32 v25, v25\n"
    "v_mfma_f32_32x32x16_bf16 a[80:95], a[232:235], v[152:155], a[80:95]\n"
    "ds_read_b128 a[200:203], v204 offset:2048\n"
    "v_add_f32 v199, v199, v23\n"
    "v_add_f32 v198, v198, v24\n"
    "v_exp_f32 v26, v26\n"
    "v_exp_f32 v27, v27\n"
    "v_mfma_f32_32x32x16_bf16 a[112:127], a[236:239], v[152:155], a[112:127]\n"
    "ds_read_b128 a[204:207], v204 offset:3072\n"
    "v_add_f32 v199, v199, v25\n"
    "v_add_f32 v198, v198, v26\n"
    "v_exp_f32 v28, v28\n"
    "v_exp_f32 v29, v29\n"
    "v_mfma_f32_32x32x16_bf16 a[16:31], a[240:243], v[156:159], a[16:31]\n"
    "ds_read_b128 a[208:211], v204 offset:4096\n"
    "v_add_f32 v199, v199, v27\n"
    "v_add_f32 v198, v198, v28\n"
    "v_add_f32 v199, v199, v29\n"
    "v_exp_f32 v30, v30\n"
    "v_xor_b32 v205, 229376, v205\n"
    "v_mfma_f32_32x32x16_bf16 a[48:63], a[244:247], v[156:159], a[48:63]\n"
    "buffer_load_dwordx4 v251, s[52:55], s78 offen offset:1024 sc1 lds\n"
    "ds_read_b128 a[212:215], v204 offset:5120\n"
    "v_add_f32 v198, v198, v30\n"
    "v_exp_f32 v31, v31\n"
    "v_mfma_f32_32x32x16_bf16 a[80:95], a[248:251], v[156:159], a[80:95]\n"
    "ds_read_b128 a[216:219], v204 offset:6144\n"
    "v_add_f32 v199, v199, v31\n"
    "v_mfma_f32_32x32x16_bf16 a[112:127], a[252:255], v[156:159], a[112:127]\n"
    "ds_read_b128 a[220:223], v204 offset:7168\n"
    "s_cmp_lg_u32 s91, 0\n"
    "s_cbranch_scc0 L_rbs0_%=\n"
    "s_call_b64 s[98:99], L_rareb_%=\n"
    "L_rbs0_%=:\n"
    "s_cmp_lg_u32 s76, 0\n"
    "s_cbranch_scc1 L_s_%=\n"
    "L_drain_%=:\n"
    "s_waitcnt lgkmcnt(0)\n"
    "ds_read_b128 a[192:195], v205\n"
    "ds_read_b128 a[196:199], v205 offset:2048\n"
    "ds_read_b128 a[200:203], v205 offset:4096\n"
    "ds_read_b128 a[204:207], v205 offset:6144\n"
    "ds_read_b128 a[208:211], v205 offset:1024\n"
    "ds_read_b128 a[212:215], v205 offset:3072\n"
    "ds_read_b128 a[216:219], v205 offset:5120\n"
    "ds_read_b128 a[220:223], v205 offset:7168\n"
    "v_cvt_pk_bf16_f32 v128, v0, v1\n"
    "v_cvt_pk_bf16_f32 v129, v2, v3\n"
    "v_cvt_pk_bf16_f32 v130, v4, v5\n"
    "v_cvt_pk_bf16_f32 v131, v6, v7\n"
    "v_cvt_pk_bf16_f32 v132, v8, v9\n"
    "v_cvt_pk_bf16_f32 v133, v10, v11\n"
    "v_cvt_pk_bf16_f32 v134, v12, v13\n"
    "v_cvt_pk_bf16_f32 v135, v14, v15\n"
    "s_waitcnt lgkmcnt(5)\n"
    "v_mfma_f32_32x32x16_bf16 a[16:31], a[192:195], v[128:131], a[16:31]\n"
    "ds_read_b128 a[224:227], v205 offset:8192\n"
    "v_cvt_pk_bf16_f32 v136, v16, v17\n"
    "v_cvt_pk_bf16_f32 v137, v18, v19\n"
    "v_cvt_pk_bf16_f32 v138, v20, v21\n"
    "v_cvt_pk_bf16_f32 v139, v22, v23\n"
    "v_cvt_pk_bf16_f32 v140, v24, v25\n"
    "v_mfma_f32_32x32x16_bf16 a[48:63], a[196:199], v[128:131], a[48:63]\n"
    "ds_read_b128 a[228:231], v205 offset:10240\n"
    "v_cvt_pk_bf16_f32 v141, v26, v27\n"
    "v_cvt_pk_bf16_f32 v142, v28, v29\n"
    "v_cvt_pk_bf16_f32 v143, v30, v31\n"
    "v_exp_f32 v32, v32\n"
    "v_mfma_f32_32x32x16_bf16 a[80:95], a[200:203], v[128:131], a[80:95]\n"
    "ds_read_b128 a[232:235], v205 offset:12288\n"
    "v_exp_f32 v33, v33\n"
    "v_exp_f32 v34, v34\n"
    "v_exp_f32 v35, v35\n"
    "s_waitcnt lgkmcnt(5)\n"
    "v_mfma_f32_32x32x16_bf16 a[112:127], a[204:207], v[128:131], a[112:127]\n"
    "ds_read_b128 a[236:239], v205 offset:14336\n"
    "v_exp_f32 v36, v36\n"
    "v_exp_f32 v37, v37\n"
    "v_exp_f32 v38, v38\n"
    "v_mfma_f32_32x32x16_bf16 a[16:31], a[208:211], v[132:135], a[16:31]\n"
    "ds_read_b128 a[240:243], v205 offset:9216\n"
    "v_exp_f32 v39, v39\n"
    "v_exp_f32 v40, v40\n"
    "v_exp_f32 v41, v41\n"
    "v_mfma_f32_32x32x16_bf16 a[48:63], a[212:215], v[132:135], a[48:63]\n"
    "ds_read_b128 a[244:247], v205 offset:11264\n"
    "v_exp_f32 v42, v42\n"
    "v_exp_f32 v43, v43\n"
    "v_exp_f32 v44, v44\n"
    "s_waitcnt lgkmcnt(5)\n"
    "v_mfma_f32_32x32x16_bf16 a[80:95], a[216:219], v[132:135], a[80:95]\n"
    "ds_read_b128 a[248:251], v205 offset:13312\n"
    "v_exp_f32 v45, v45\n"
    "v_exp_f32 v46, v46\n"
    "v_exp_f32 v47, v47\n"
    "v_mfma_f32_32x32x16_bf16 a[112:127], a[220:223], v[132:135], a[112:127]\n"
    "ds_read_b128 a[252:255], v205 offset:15360\n"
    "v_exp_f32 v48, v48\n"
    "v_cvt_pk_bf16_f32 v144, v32, v33\n"
    "v_cvt_pk_bf16_f32 v145, v34, v35\n"
    "v_cvt_pk_bf16_f32 v146, v36, v37\n"
    "v_cvt_pk_bf16_f32 v147, v38, v39\n"
    "v_mfma_f32_32x32x16_bf16 a[16:31], a[224:227], v[136:139], a[16:31]\n"
    "ds_read_b128 a[192:195], v205 offset:16384\n"
    "v_exp_f32 v49, v49\n"
    "v_exp_f32 v50, v50\n"
    "v_exp_f32 v51, v51\n"
    "s_waitcnt lgkmcnt(5)\n"
    "v_mfma_f32_32x32x16_bf16 a[48:63], a[228:231], v[136:139], a[48:63]\n"
    "ds_read_b128 a[196:199], v205 offset:18432\n"
    "v_exp_f32 v52, v52\n"
    "v_exp_f32 v53, v53\n"
    "v_exp_f32 v54, v54\n"
    "v_mfma_f32_32x32x16_bf16 a[80:95], a[232:235], v[136:139], a[80:95]\n"
    "ds_read_b128 a[200:203], v205 offset:20480\n"
    "v_exp_f32 v55, v55\n"
    "v_cvt_pk_bf16_f32 v148, v40, v41\n"
    "v_cvt_pk_bf16_f32 v149, v42, v43\n"
    "v_cvt_pk_bf16_f32 v150, v44, v45\n"
    "v_cvt_pk_bf16_f32 v151, v46, v47\n"
    "v_mfma_f32_32x32x16_bf16 a[112:127], a[236:239], v[136:139], a[112:127]\n"
    "ds_read_b128 a[204:207], v205 offset:22528\n"
    "v_exp_f32 v56, v56\n"
    "v_exp_f32 v57, v57\n"
    "v_exp_f32 v58, v58\n"
    "s_waitcnt lgkmcnt(5)\n"
    "v_mfma_f32_32x32x16_bf16 a[16:31], a[240:243], v[140:143], a[16:31]\n"
    "ds_read_b128 a[208:211], v205 offset:17408\n"
    "v_exp_f32 v59, v59\n"
    "v_exp_f32 v60, v60\n"
    "v_exp_f32 v61, v61\n"
    "v_mfma_f32_32x32x16_bf16 a[48:63], a[244:247], v[140:143], a[48:63]\n"
    "ds_read_b128 a[212:215], v205 offset:19456\n"
    "v_exp_f32 v62, v62\n"
    "v_exp_f32 v63, v63\n"
    "v_cvt_pk_bf16_f32 v152, v48, v49\n"
    "v_cvt_pk_bf16_f32 v153, v50, v51\n"
    "v_mfma_f32_32x32x16_bf16 a[80:95], a[248:251], v[140:143], a[80:95]\n"
    "ds_read_b128 a[216:219], v205 offset:21504\n"
    "v_cvt_pk_bf16_f32 v154, v52, v53\n"
    "v_cvt_pk_bf16_f32 v155, v54, v55\n"
    "v_cvt_pk_bf16_f32 v156, v56, v57\n"
    "v_cvt_pk_bf16_f32 v157, v58, v59\n"
    "v_cvt_pk_bf16_f32 v158, v60, v61\n"
    "s_waitcnt lgkmcnt(5)\n"
    "v_mfma_f32_32x32x16_bf16 a[112:127], a[252:255], v[140:143], a[112:127]\n"
    "ds_read_b128 a[220:223], v205 offset:23552\n"
    "v_add_f32 v198, v198, v32\n"
    "v_add_f32 v199, v199, v33\n"
    "v_add_f32 v198, v198, v34\n"
    "v_add_f32 v199, v199, v35\n"
    "v_cvt_pk_bf16_f32 v159, v62, v63\n"
    "v_mfma_f32_32x32x16_bf16 a[16:31], a[192:195], v[144:147], a[16:31]\n"
    "ds_read_b128 a[224:227], v205 offset:24576\n"
    "v_add_f32 v198, v198, v36\n"
    "v_add_f32 v199, v199, v37\n"
    "v_add_f32 v198, v198, v38\n"
    "v_add_f32 v199, v199, v39\n"
    "v_add_f32 v198, v198, v40\n"
    "v_mfma_f32_32x32x16_bf16 a[48:63], a[196:199], v[144:147], a[48:63]\n"
    "ds_read_b128 a[228:231], v205 offset:26624\n"
    "v_add_f32 v199, v199, v41\n"
    "v_add_f32 v198, v198, v42\n"
    "v_add_f32 v199, v199, v43\n"
    "v_add_f32 v198, v198, v44\n"
    "v_add_f32 v199, v199, v45\n"
    "s_waitcnt lgkmcnt(5)\n"
    "v_mfma_f32_32x32x16_bf16 a[80:95], a[200:203], v[144:147], a[80:95]\n"
    "ds_read_b128 a[232:235], v205 offset:28672\n"
    "v_add_f32 v198, v198, v46\n"
    "v_add_f32 v199, v199, v47\n"
    "v_add_f32 v198, v198, v48\n"
    "v_add_f32 v199, v199, v49\n"
    "v_add_f32 v198, v198, v50\n"
    "v_mfma_f32_32x32x16_bf16 a[112:127], a[204:207], v[144:147], a[112:127]\n"
    "ds_read_b128 a[236:239], v205 offset:30720\n"
    "v_add_f32 v199, v199, v51\n"
    "v_add_f32 v198, v198, v52\n"
    "v_add_f32 v199, v199, v53\n"
    "v_add_f32 v198, v198, v54\n"
    "v_add_f32 v199, v199, v55\n"
    "v_mfma_f32_32x32x16_bf16 a[16:31], a[208:211], v[148:151], a[16:31]\n"
    "ds_read_b128 a[240:243], v205 offset:25600\n"
    "v_add_f32 v198, v198, v56\n"
    "v_add_f32 v199, v199, v57\n"
    "v_add_f32 v198, v198, v58\n"
    "v_add_f32 v199, v199, v59\n"
    "v_add_f32 v198, v198, v60\n"
    "s_waitcnt lgkmcnt(5)\n"
    "v_mfma_f32_32x32x16_bf16 a[48:63], a[212:215], v[148:151], a[48:63]\n"
    "ds_read_b128 a[244:247], v205 offset:27648\n"
    "v_add_f32 v199, v199, v61\n"
    "v_add_f32 v198, v198, v62\n"
    "v_add_f32 v199, v199, v63\n"
    "v_mfma_f32_32x32x16_bf16 a[80:95], a[216:219], v[148:151], a[80:95]\n"
    "ds_read_b128 a[248:251], v205 offset:29696\n"
    "v_mfma_f32_32x32x16_bf16 a[112:127], a[220:223], v[148:151], a[112:127]\n"
    "ds_read_b128 a[252:255], v205 offset:31744\n"
    "s_waitcnt lgkmcnt(5)\n"
    "v_mfma_f32_32x32x16_bf16 a[16:31], a[224:227], v[152:155], a[16:31]\n"
    "v_mfma_f32_32x32x16_bf16 a[48:63], a[228:231], v[152:155], a[48:63]\n"
    "v_mfma_f32_32x32x16_bf16 a[80:95], a[232:235], v[152:155], a[80:95]\n"
    "s_waitcnt lgkmcnt(2)\n"
    "v_mfma_f32_32x32x16_bf16 a[112:127], a[236:239], v[152:155], a[112:127]\n"
    "v_mfma_f32_32x32x16_bf16 a[16:31], a[240:243], v[156:159], a[16:31]\n"
    "v_mfma_f32_32x32x16_bf16 a[48:63], a[244:247], v[156:159], a[48:63]\n"
    "s_waitcnt lgkmcnt(0)\n"
    "v_mfma_f32_32x32x16_bf16 a[80:95], a[248:251], v[156:159], a[80:95]\n"
    "v_mfma_f32_32x32x16_bf16 a[112:127], a[252:255], v[156:159], a[112:127]\n"
    "s_nop 15\n"
    "s_sub_u32 s84, 15, s62\n"
    "v_add_f32 v198, v198, v199\n"
    "v_mov_b32 v211, v198\n"
    "s_nop 1\n"
    "v_permlane32_swap_b32 v211, v198\n"
    "s_nop 0\n"
    "v_add_f32 v211, v211, v198\n"
    "v_rcp_f32 v219, v211\n"
    "s_lshl_b32 s82, s84, 2\n"
    "s_add_u32 s82, s82, s60\n"
    "s_lshl_b32 s82, s82, 18\n"
    "s_lshl_b32 s83, s61, 12\n"
    "s_add_u32 s82, s82, s83\n"
    "v_bfe_u32 v222, v250, 4, 1\n"
    "v_lshlrev_b32 v222, 17, v222\n"
    "v_lshrrev_b32 v223, 5, v250\n"
    "v_lshlrev_b32 v223, 8, v223\n"
    "v_add_u32 v222, v222, v223\n"
    "v_and_b32 v223, 15, v250\n"
    "v_lshlrev_b32 v223, 4, v223\n"
    "v_add_u32 v222, v222, v223\n"
    "v_accvgpr_read_b32 v64, a16\n"
    "v_accvgpr_read_b32 v65, a17\n"
    "v_accvgpr_read_b32 v66, a18\n"
    "v_accvgpr_read_b32 v67, a19\n"
    "v_accvgpr_read_b32 v68, a20\n"
    "v_accvgpr_read_b32 v69, a21\n"
    "v_accvgpr_read_b32 v70, a22\n"
    "v_accvgpr_read_b32 v71, a23\n"
    "v_accvgpr_read_b32 v72, a24\n"
    "v_accvgpr_read_b32 v73, a25\n"
    "v_accvgpr_read_b32 v74, a26\n"
    "v_accvgpr_read_b32 v75, a27\n"
    "v_accvgpr_read_b32 v76, a28\n"
    "v_accvgpr_read_b32 v77, a29\n"
    "v_accvgpr_read_b32 v78, a30\n"
    "v_accvgpr_read_b32 v79, a31\n"
    "v_mul_f32 v64, v64, v219\n"
    "v_mul_f32 v65, v65, v219\n"
    "v_mul_f32 v66, v66, v219\n"
    "v_mul_f32 v67, v67, v219\n"
    "v_mul_f32 v68, v68, v219\n"
    "v_mul_f32 v69, v69, v219\n"
    "v_mul_f32 v70, v70, v219\n"
    "v_mul_f32 v71, v71, v219\n"
    "v_mul_f32 v72, v72, v219\n"
    "v_mul_f32 v73, v73, v219\n"
    "v_mul_f32 v74, v74, v219\n"
    "v_mul_f32 v75, v75, v219\n"
    "v_mul_f32 v76, v76, v219\n"
    "v_mul_f32 v77, v77, v219\n"
    "v_mul_f32 v78, v78, v219\n"
    "v_mul_f32 v79, v79, v219\n"
    "v_cvt_pk_bf16_f32 v80, v64, v65\n"
    "v_cvt_pk_bf16_f32 v81, v66, v67\n"
    "v_cvt_pk_bf16_f32 v82, v68, v69\n"
    "v_cvt_pk_bf16_f32 v83, v70, v71\n"
    "v_cvt_pk_bf16_f32 v84, v72, v73\n"
    "v_cvt_pk_bf16_f32 v85, v74, v75\n"
    "v_cvt_pk_bf16_f32 v86, v76, v77\n"
    "v_cvt_pk_bf16_f32 v87, v78, v79\n"
    "buffer_store_dwordx4 v[80:83], v222, s[44:47], s82 offen sc1\n"
    "buffer_store_dwordx4 v[84:87], v222, s[44:47], s82 offen offset:512 sc1\n"
    "v_accvgpr_read_b32 v96, a48\n"
    "v_accvgpr_read_b32 v97, a49\n"
    "v_accvgpr_read_b32 v98, a50\n"
    "v_accvgpr_read_b32 v99, a51\n"
    "v_accvgpr_read_b32 v100, a52\n"
    "v_accvgpr_read_b32 v101, a53\n"
    "v_accvgpr_read_b32 v102, a54\n"
    "v_accvgpr_read_b32 v103, a55\n"
    "v_accvgpr_read_b32 v104, a56\n"
    "v_accvgpr_read_b32 v105, a57\n"
    "v_accvgpr_read_b32 v106, a58\n"
    "v_accvgpr_read_b32 v107, a59\n"
    "v_accvgpr_read_b32 v108, a60\n"
    "v_accvgpr_read_b32 v109, a61\n"
    "v_accvgpr_read_b32 v110, a62\n"
    "v_accvgpr_read_b32 v111, a63\n"
    "v_mul_f32 v96, v96, v219\n"
    "v_mul_f32 v97, v97, v219\n"
    "v_mul_f32 v98, v98, v219\n"
    "v_mul_f32 v99, v99, v219\n"
    "v_mul_f32 v100, v100, v219\n"
    "v_mul_f32 v101, v101, v219\n"
    "v_mul_f32 v102, v102, v219\n"
    "v_mul_f32 v103, v103, v219\n"
    "v_mul_f32 v104, v104, v219\n"
    "v_mul_f32 v105, v105, v219\n"
    "v_mul_f32 v106, v106, v219\n"
    "v_mul_f32 v107, v107, v219\n"
    "v_mul_f32 v108, v108, v219\n"
    "v_mul_f32 v109, v109, v219\n"
    "v_mul_f32 v110, v110, v219\n"
    "v_mul_f32 v111, v111, v219\n"
    "v_cvt_pk_bf16_f32 v112, v96, v97\n"
    "v_cvt_pk_bf16_f32 v113, v98, v99\n"
    "v_cvt_pk_bf16_f32 v114, v100, v101\n"
    "v_cvt_pk_bf16_f32 v115, v102, v103\n"
    "v_cvt_pk_bf16_f32 v116, v104, v105\n"
    "v_cvt_pk_bf16_f32 v117, v106, v107\n"
    "v_cvt_pk_bf16_f32 v118, v108, v109\n"
    "v_cvt_pk_bf16_f32 v119, v110, v111\n"
    "buffer_store_dwordx4 v[112:115], v222, s[44:47], s82 offen offset:1024 sc1\n"
    "buffer_store_dwordx4 v[116:119], v222, s[44:47], s82 offen offset:1536 sc1\n"
    "v_accvgpr_read_b32 v64, a80\n"
    "v_accvgpr_read_b32 v65, a81\n"
    "v_accvgpr_read_b32 v66, a82\n"
    "v_accvgpr_read_b32 v67, a83\n"
    "v_accvgpr_read_b32 v68, a84\n"
    "v_accvgpr_read_b32 v69, a85\n"
    "v_accvgpr_read_b32 v70, a86\n"
    "v_accvgpr_read_b32 v71, a87\n"
    "v_accvgpr_read_b32 v72, a88\n"
    "v_accvgpr_read_b32 v73, a89\n"
    "v_accvgpr_read_b32 v74, a90\n"
    "v_accvgpr_read_b32 v75, a91\n"
    "v_accvgpr_read_b32 v76, a92\n"
    "v_accvgpr_read_b32 v77, a93\n"
    "v_accvgpr_read_b32 v78, a94\n"
    "v_accvgpr_read_b32 v79, a95\n"
    "v_mul_f32 v64, v64, v219\n"
    "v_mul_f32 v65, v65, v219\n"
    "v_mul_f32 v66, v66, v219\n"
    "v_mul_f32 v67, v67, v219\n"
    "v_mul_f32 v68, v68, v219\n"
    "v_mul_f32 v69, v69, v219\n"
    "v_mul_f32 v70, v70, v219\n"
    "v_mul_f32 v71, v71, v219\n"
    "v_mul_f32 v72, v72, v219\n"
    "v_mul_f32 v73, v73, v219\n"
    "v_mul_f32 v74, v74, v219\n"
    "v_mul_f32 v75, v75, v219\n"
    "v_mul_f32 v76, v76, v219\n"
    "v_mul_f32 v77, v77, v219\n"
    "v_mul_f32 v78, v78, v219\n"
    "v_mul_f32 v79, v79, v219\n"
    "v_cvt_pk_bf16_f32 v80, v64, v65\n"
    "v_cvt_pk_bf16_f32 v81, v66, v67\n"
    "v_cvt_pk_bf16_f32 v82, v68, v69\n"
    "v_cvt_pk_bf16_f32 v83, v70, v71\n"
    "v_cvt_pk_bf16_f32 v84, v72, v73\n"
    "v_cvt_pk_bf16_f32 v85, v74, v75\n"
    "v_cvt_pk_bf16_f32 v86, v76, v77\n"
    "v_cvt_pk_bf16_f32 v87, v78, v79\n"
    "buffer_store_dwordx4 v[80:83], v222, s[44:47], s82 offen offset:2048 sc1\n"
    "buffer_store_dwordx4 v[84:87], v222, s[44:47], s82 offen offset:2560 sc1\n"
    "v_accvgpr_read_b32 v96, a112\n"
    "v_accvgpr_read_b32 v97, a113\n"
    "v_accvgpr_read_b32 v98, a114\n"
    "v_accvgpr_read_b32 v99, a115\n"
    "v_accvgpr_read_b32 v100, a116\n"
    "v_accvgpr_read_b32 v101, a117\n"
    "v_accvgpr_read_b32 v102, a118\n"
    "v_accvgpr_read_b32 v103, a119\n"
    "v_accvgpr_read_b32 v104, a120\n"
    "v_accvgpr_read_b32 v105, a121\n"
    "v_accvgpr_read_b32 v106, a122\n"
    "v_accvgpr_read_b32 v107, a123\n"
    "v_accvgpr_read_b32 v108, a124\n"
    "v_accvgpr_read_b32 v109, a125\n"
    "v_accvgpr_read_b32 v110, a126\n"
    "v_accvgpr_read_b32 v111, a127\n"
    "v_mul_f32 v96, v96, v219\n"
    "v_mul_f32 v97, v97, v219\n"
    "v_mul_f32 v98, v98, v219\n"
    "v_mul_f32 v99, v99, v219\n"
    "v_mul_f32 v100, v100, v219\n"
    "v_mul_f32 v101, v101, v219\n"
    "v_mul_f32 v102, v102, v219\n"
    "v_mul_f32 v103, v103, v219\n"
    "v_mul_f32 v104, v104, v219\n"
    "v_mul_f32 v105, v105, v219\n"
    "v_mul_f32 v106, v106, v219\n"
    "v_mul_f32 v107, v107, v219\n"
    "v_mul_f32 v108, v108, v219\n"
    "v_mul_f32 v109, v109, v219\n"
    "v_mul_f32 v110, v110, v219\n"
    "v_mul_f32 v111, v111, v219\n"
    "v_cvt_pk_bf16_f32 v112, v96, v97\n"
    "v_cvt_pk_bf16_f32 v113, v98, v99\n"
    "v_cvt_pk_bf16_f32 v114, v100, v101\n"
    "v_cvt_pk_bf16_f32 v115, v102, v103\n"
    "v_cvt_pk_bf16_f32 v116, v104, v105\n"
    "v_cvt_pk_bf16_f32 v117, v106, v107\n"
    "v_cvt_pk_bf16_f32 v118, v108, v109\n"
    "v_cvt_pk_bf16_f32 v119, v110, v111\n"
    "buffer_store_dwordx4 v[112:115], v222, s[44:47], s82 offen offset:3072 sc1\n"
    "buffer_store_dwordx4 v[116:119], v222, s[44:47], s82 offen offset:3584 sc1\n"
    "s_branch L_atend_%=\n"
    "L_a_m0_%=:\n"
    "v_mov_b32 v210, 4286578688\n"
    "v_cmp_gt_i32 s[82:83], 0, v206\n"
    "v_cmp_gt_i32 s[84:85], 1, v206\n"
    "v_cmp_gt_i32 s[86:87], 2, v206\n"
    "v_cndmask_b32 v0, v0, v210, s[82:83]\n"
    "v_cmp_gt_i32 s[82:83], 3, v206\n"
    "v_cndmask_b32 v1, v1, v210, s[84:85]\n"
    "v_cmp_gt_i32 s[84:85], 8, v206\n"
    "v_cndmask_b32 v2, v2, v210, s[86:87]\n"
    "v_cmp_gt_i32 s[86:87], 9, v206\n"
    "v_cndmask_b32 v3, v3, v210, s[82:83]\n"
    "v_cmp_gt_i32 s[82:83], 10, v206\n"
    "v_cndmask_b32 v4, v4, v210, s[84:85]\n"
    "v_cmp_gt_i32 s[84:85], 11, v206\n"
    "v_cndmask_b32 v5, v5, v210, s[86:87]\n"
    "v_cmp_gt_i32 s[86:87], 16, v206\n"
    "v_cndmask_b32 v6, v6, v210, s[82:83]\n"
    "v_cmp_gt_i32 s[82:83], 17, v206\n"
    "v_cndmask_b32 v7, v7, v210, s[84:85]\n"
    "v_cmp_gt_i32 s[84:85], 18, v206\n"
    "v_cndmask_b32 v8, v8, v210, s[86:87]\n"
    "v_cmp_gt_i32 s[86:87], 19, v206\n"
    "v_cndmask_b32 v9, v9, v210, s[82:83]\n"
    "v_cmp_gt_i32 s[82:83], 24, v206\n"
    "v_cndmask_b32 v10, v10, v210, s[84:85]\n"
    "v_cmp_gt_i32 s[84:85], 25, v206\n"
    "v_cndmask_b32 v11, v11, v210, s[86:87]\n"
    "v_cmp_gt_i32 s[86:87], 26, v206\n"
    "v_cndmask_b32 v12, v12, v210, s[82:83]\n"
    "v_cmp_gt_i32 s[82:83], 27, v206\n"
    "v_cndmask_b32 v13, v13, v210, s[84:85]\n"
    "s_nop 0\n"
    "v_cndmask_b32 v14, v14, v210, s[86:87]\n"
    "s_nop 0\n"
    "v_cndmask_b32 v15, v15, v210, s[82:83]\n"
    "s_branch L_a_m0_%=r\n"
    "L_a_m1_%=:\n"
    "v_mov_b32 v210, 4286578688\n"
    "v_cmp_gt_i32 s[82:83], 32, v206\n"
    "v_cmp_gt_i32 s[84:85], 33, v206\n"
    "v_cmp_gt_i32 s[86:87], 34, v206\n"
    "v_cndmask_b32 v32, v32, v210, s[82:83]\n"
    "v_cmp_gt_i32 s[82:83], 35, v206\n"
    "v_cndmask_b32 v33, v33, v210, s[84:85]\n"
    "v_cmp_gt_i32 s[84:85], 40, v206\n"
    "v_cndmask_b32 v34, v34, v210, s[86:87]\n"
    "v_cmp_gt_i32 s[86:87], 41, v206\n"
    "v_cndmask_b32 v35, v35, v210, s[82:83]\n"
    "v_cmp_gt_i32 s[82:83], 42, v206\n"
    "v_cndmask_b32 v36, v36, v210, s[84:85]\n"
    "v_cmp_gt_i32 s[84:85], 43, v206\n"
    "v_cndmask_b32 v37, v37, v210, s[86:87]\n"
    "v_cmp_gt_i32 s[86:87], 48, v206\n"
    "v_cndmask_b32 v38, v38, v210, s[82:83]\n"
    "v_cmp_gt_i32 s[82:83], 49, v206\n"
    "v_cndmask_b32 v39, v39, v210, s[84:85]\n"
    "v_cmp_gt_i32 s[84:85], 50, v206\n"
    "v_cndmask_b32 v40, v40, v210, s[86:87]\n"
    "v_cmp_gt_i32 s[86:87], 51, v206\n"
    "v_cndmask_b32 v41, v41, v210, s[82:83]\n"
    "v_cmp_gt_i32 s[82:83], 56, v206\n"
    "v_cndmask_b32 v42, v42, v210, s[84:85]\n"
    "v_cmp_gt_i32 s[84:85], 57, v206\n"
    "v_cndmask_b32 v43, v43, v210, s[86:87]\n"
    "v_cmp_gt_i32 s[86:87], 58, v206\n"
    "v_cndmask_b32 v44, v44, v210, s[82:83]\n"
    "v_cmp_gt_i32 s[82:83], 59, v206\n"
    "v_cndmask_b32 v45, v45, v210, s[84:85]\n"
    "s_nop 0\n"
    "v_cndmask_b32 v46, v46, v210, s[86:87]\n"
    "s_nop 0\n"
    "v_cndmask_b32 v47, v47, v210, s[82:83]\n"
    "s_branch L_a_m1_%=r\n"
    "L_a_ra_%=:\n"
    "v_mov_b32 v211, v192\n"
    "s_nop 1\n"
    "v_permlane32_swap_b32 v211, v192\n"
    "s_nop 0\n"
    "v_max_f32 v212, v211, v192\n"
    "v_max_f32 v212, 0, v212\n"
    "v_add_f32 v200, v200, v212\n"
    "v_sub_f32 v202, 0, v212\n"
    "v_exp_f32 v202, v202\n"
    "v_sub_f32 v0, v0, v212\n"
    "v_sub_f32 v1, v1, v212\n"
    "v_sub_f32 v2, v2, v212\n"
    "v_sub_f32 v3, v3, v212\n"
    "v_sub_f32 v4, v4, v212\n"
    "v_sub_f32 v5, v5, v212\n"
    "v_sub_f32 v6, v6, v212\n"
    "v_sub_f32 v7, v7, v212\n"
    "v_sub_f32 v8, v8, v212\n"
    "v_sub_f32 v9, v9, v212\n"
    "v_sub_f32 v10, v10, v212\n"
    "v_sub_f32 v11, v11, v212\n"
    "v_sub_f32 v12, v12, v212\n"
    "v_sub_f32 v13, v13, v212\n"
    "v_sub_f32 v14, v14, v212\n"
    "v_sub_f32 v15, v15, v212\n"
    "v_sub_f32 v32, v32, v212\n"
    "v_sub_f32 v33, v33, v212\n"
    "v_sub_f32 v34, v34, v212\n"
    "v_sub_f32 v35, v35, v212\n"
    "v_sub_f32 v36, v36, v212\n"
    "v_sub_f32 v37, v37, v212\n"
    "v_sub_f32 v38, v38, v212\n"
    "v_sub_f32 v39, v39, v212\n"
    "v_sub_f32 v40, v40, v212\n"
    "v_sub_f32 v41, v41, v212\n"
    "v_sub_f32 v42, v42, v212\n"
    "v_sub_f32 v43, v43, v212\n"
    "v_sub_f32 v44, v44, v212\n"
    "v_sub_f32 v45, v45, v212\n"
    "v_sub_f32 v46, v46, v212\n"
    "v_sub_f32 v47, v47, v212\n"
    "v_sub_f32 v160, 0, v200\n"
    "v_sub_f32 v161, 0, v200\n"
    "v_sub_f32 v162, 0, v200\n"
    "v_sub_f32 v163, 0, v200\n"
    "v_sub_f32 v164, 0, v200\n"
    "v_sub_f32 v165, 0, v200\n"
    "v_sub_f32 v166, 0, v200\n"
    "v_sub_f32 v167, 0, v200\n"
    "v_sub_f32 v168, 0, v200\n"
    "v_sub_f32 v169, 0, v200\n"
    "v_sub_f32 v170, 0, v200\n"
    "v_sub_f32 v171, 0, v200\n"
    "v_sub_f32 v172, 0, v200\n"
    "v_sub_f32 v173, 0, v200\n"
    "v_sub_f32 v174, 0, v200\n"
    "v_sub_f32 v175, 0, v200\n"
    "v_mul_f32 v196, v196, v202\n"
    "v_mul_f32 v197, v197, v202\n"
    "v_mov_b32 v211, v193\n"
    "s_nop 1\n"
    "v_permlane32_swap_b32 v211, v193\n"
    "s_nop 0\n"
    "v_max_f32 v213, v211, v193\n"
    "v_max_f32 v213, 0, v213\n"
    "v_add_f32 v201, v201, v213\n"
    "v_sub_f32 v203, 0, v213\n"
    "v_exp_f32 v203, v203\n"
    "v_sub_f32 v16, v16, v213\n"
    "v_sub_f32 v17, v17, v213\n"
    "v_sub_f32 v18, v18, v213\n"
    "v_sub_f32 v19, v19, v213\n"
    "v_sub_f32 v20, v20, v213\n"
    "v_sub_f32 v21, v21, v213\n"
    "v_sub_f32 v22, v22, v213\n"
    "v_sub_f32 v23, v23, v213\n"
    "v_sub_f32 v24, v24, v213\n"
    "v_sub_f32 v25, v25, v213\n"
    "v_sub_f32 v26, v26, v213\n"
    "v_sub_f32 v27, v27, v213\n"
    "v_sub_f32 v28, v28, v213\n"
    "v_sub_f32 v29, v29, v213\n"
    "v_sub_f32 v30, v30, v213\n"
    "v_sub_f32 v31, v31, v213\n"
    "v_sub_f32 v48, v48, v213\n"
    "v_sub_f32 v49, v49, v213\n"
    "v_sub_f32 v50, v50, v213\n"
    "v_sub_f32 v51, v51, v213\n"
    "v_sub_f32 v52, v52, v213\n"
    "v_sub_f32 v53, v53, v213\n"
    "v_sub_f32 v54, v54, v213\n"
    "v_sub_f32 v55, v55, v213\n"
    "v_sub_f32 v56, v56, v213\n"
    "v_sub_f32 v57, v57, v213\n"
    "v_sub_f32 v58, v58, v213\n"
    "v_sub_f32 v59, v59, v213\n"
    "v_sub_f32 v60, v60, v213\n"
    "v_sub_f32 v61, v61, v213\n"
    "v_sub_f32 v62, v62, v213\n"
    "v_sub_f32 v63, v63, v213\n"
    "v_sub_f32 v176, 0, v201\n"
    "v_sub_f32 v177, 0, v201\n"
    "v_sub_f32 v178, 0, v201\n"
    "v_sub_f32 v179, 0, v201\n"
    "v_sub_f32 v180, 0, v201\n"
    "v_sub_f32 v181, 0, v201\n"
    "v_sub_f32 v182, 0, v201\n"
    "v_sub_f32 v183, 0, v201\n"
    "v_sub_f32 v184, 0, v201\n"
    "v_sub_f32 v185, 0, v201\n"
    "v_sub_f32 v186, 0, v201\n"
    "v_sub_f32 v187, 0, v201\n"
    "v_sub_f32 v188, 0, v201\n"
    "v_sub_f32 v189, 0, v201\n"
    "v_sub_f32 v190, 0, v201\n"
    "v_sub_f32 v191, 0, v201\n"
    "v_mul_f32 v198, v198, v203\n"
    "v_mul_f32 v199, v199, v203\n"
    "s_mov_b32 s91, 1\n"
    "s_nop 1\n"
    "s_branch L_a_ra_%=r\n"
    "L_b_m0_%=:\n"
    "v_mov_b32 v210, 4286578688\n"
    "v_cmp_gt_i32 s[82:83], 0, v207\n"
    "v_cmp_gt_i32 s[84:85], 1, v207\n"
    "v_cmp_gt_i32 s[86:87], 2, v207\n"
    "v_cndmask_b32 v64, v64, v210, s[82:83]\n"
    "v_cmp_gt_i32 s[82:83], 3, v207\n"
    "v_cndmask_b32 v65, v65, v210, s[84:85]\n"
    "v_cmp_gt_i32 s[84:85], 8, v207\n"
    "v_cndmask_b32 v66, v66, v210, s[86:87]\n"
    "v_cmp_gt_i32 s[86:87], 9, v207\n"
    "v_cndmask_b32 v67, v67, v210, s[82:83]\n"
    "v_cmp_gt_i32 s[82:83], 10, v207\n"
    "v_cndmask_b32 v68, v68, v210, s[84:85]\n"
    "v_cmp_gt_i32 s[84:85], 11, v207\n"
    "v_cndmask_b32 v69, v69, v210, s[86:87]\n"
    "v_cmp_gt_i32 s[86:87], 16, v207\n"
    "v_cndmask_b32 v70, v70, v210, s[82:83]\n"
    "v_cmp_gt_i32 s[82:83], 17, v207\n"
    "v_cndmask_b32 v71, v71, v210, s[84:85]\n"
    "v_cmp_gt_i32 s[84:85], 18, v207\n"
    "v_cndmask_b32 v72, v72, v210, s[86:87]\n"
    "v_cmp_gt_i32 s[86:87], 19, v207\n"
    "v_cndmask_b32 v73, v73, v210, s[82:83]\n"
    "v_cmp_gt_i32 s[82:83], 24, v207\n"
    "v_cndmask_b32 v74, v74, v210, s[84:85]\n"
    "v_cmp_gt_i32 s[84:85], 25, v207\n"
    "v_cndmask_b32 v75, v75, v210, s[86:87]\n"
    "v_cmp_gt_i32 s[86:87], 26, v207\n"
    "v_cndmask_b32 v76, v76, v210, s[82:83]\n"
    "v_cmp_gt_i32 s[82:83], 27, v207\n"
    "v_cndmask_b32 v77, v77, v210, s[84:85]\n"
    "s_nop 0\n"
    "v_cndmask_b32 v78, v78, v210, s[86:87]\n"
    "s_nop 0\n"
    "v_cndmask_b32 v79, v79, v210, s[82:83]\n"
    "s_branch L_b_m0_%=r\n"
    "L_b_m1_%=:\n"
    "v_mov_b32 v210, 4286578688\n"
    "v_cmp_gt_i32 s[82:83], 32, v207\n"
    "v_cmp_gt_i32 s[84:85], 33, v207\n"
    "v_cmp_gt_i32 s[86:87], 34, v207\n"
    "v_cndmask_b32 v96, v96, v210, s[82:83]\n"
    "v_cmp_gt_i32 s[82:83], 35, v207\n"
    "v_cndmask_b32 v97, v97, v210, s[84:85]\n"
    "v_cmp_gt_i32 s[84:85], 40, v207\n"
    "v_cndmask_b32 v98, v98, v210, s[86:87]\n"
    "v_cmp_gt_i32 s[86:87], 41, v207\n"
    "v_cndmask_b32 v99, v99, v210, s[82:83]\n"
    "v_cmp_gt_i32 s[82:83], 42, v207\n"
    "v_cndmask_b32 v100, v100, v210, s[84:85]\n"
    "v_cmp_gt_i32 s[84:85], 43, v207\n"
    "v_cndmask_b32 v101, v101, v210, s[86:87]\n"
    "v_cmp_gt_i32 s[86:87], 48, v207\n"
    "v_cndmask_b32 v102, v102, v210, s[82:83]\n"
    "v_cmp_gt_i32 s[82:83], 49, v207\n"
    "v_cndmask_b32 v103, v103, v210, s[84:85]\n"
    "v_cmp_gt_i32 s[84:85], 50, v207\n"
    "v_cndmask_b32 v104, v104, v210, s[86:87]\n"
    "v_cmp_gt_i32 s[86:87], 51, v207\n"
    "v_cndmask_b32 v105, v105, v210, s[82:83]\n"
    "v_cmp_gt_i32 s[82:83], 56, v207\n"
    "v_cndmask_b32 v106, v106, v210, s[84:85]\n"
    "v_cmp_gt_i32 s[84:85], 57, v207\n"
    "v_cndmask_b32 v107, v107, v210, s[86:87]\n"
    "v_cmp_gt_i32 s[86:87], 58, v207\n"
    "v_cndmask_b32 v108, v108, v210, s[82:83]\n"
    "v_cmp_gt_i32 s[82:83], 59, v207\n"
    "v_cndmask_b32 v109, v109, v210, s[84:85]\n"
    "s_nop 0\n"
    "v_cndmask_b32 v110, v110, v210, s[86:87]\n"
    "s_nop 0\n"
    "v_cndmask_b32 v111, v111, v210, s[82:83]\n"
    "s_branch L_b_m1_%=r\n"
    "L_b_ra_%=:\n"
    "v_mov_b32 v211, v192\n"
    "s_nop 1\n"
    "v_permlane32_swap_b32 v211, v192\n"
    "s_nop 0\n"
    "v_max_f32 v212, v211, v192\n"
    "v_max_f32 v212, 0, v212\n"
    "v_add_f32 v200, v200, v212\n"
    "v_sub_f32 v202, 0, v212\n"
    "v_exp_f32 v202, v202\n"
    "v_sub_f32 v64, v64, v212\n"
    "v_sub_f32 v65, v65, v212\n"
    "v_sub_f32 v66, v66, v212\n"
    "v_sub_f32 v67, v67, v212\n"
    "v_sub_f32 v68, v68, v212\n"
    "v_sub_f32 v69, v69, v212\n"
    "v_sub_f32 v70, v70, v212\n"
    "v_sub_f32 v71, v71, v212\n"
    "v_sub_f32 v72, v72, v212\n"
    "v_sub_f32 v73, v73, v212\n"
    "v_sub_f32 v74, v74, v212\n"
    "v_sub_f32 v75, v75, v212\n"
    "v_sub_f32 v76, v76, v212\n"
    "v_sub_f32 v77, v77, v212\n"
    "v_sub_f32 v78, v78, v212\n"
    "v_sub_f32 v79, v79, v212\n"
    "v_sub_f32 v96, v96, v212\n"
    "v_sub_f32 v97, v97, v212\n"
    "v_sub_f32 v98, v98, v212\n"
    "v_sub_f32 v99, v99, v212\n"
    "v_sub_f32 v100, v100, v212\n"
    "v_sub_f32 v101, v101, v212\n"
    "v_sub_f32 v102, v102, v212\n"
    "v_sub_f32 v103, v103, v212\n"
    "v_sub_f32 v104, v104, v212\n"
    "v_sub_f32 v105, v105, v212\n"
    "v_sub_f32 v106, v106, v212\n"
    "v_sub_f32 v107, v107, v212\n"
    "v_sub_f32 v108, v108, v212\n"
    "v_sub_f32 v109, v109, v212\n"
    "v_sub_f32 v110, v110, v212\n"
    "v_sub_f32 v111, v111, v212\n"
    "v_sub_f32 v160, 0, v200\n"
    "v_sub_f32 v161, 0, v200\n"
    "v_sub_f32 v162, 0, v200\n"
    "v_sub_f32 v163, 0, v200\n"
    "v_sub_f32 v164, 0, v200\n"
    "v_sub_f32 v165, 0, v200\n"
    "v_sub_f32 v166, 0, v200\n"
    "v_sub_f32 v167, 0, v200\n"
    "v_sub_f32 v168, 0, v200\n"
    "v_sub_f32 v169, 0, v200\n"
    "v_sub_f32 v170, 0, v200\n"
    "v_sub_f32 v171, 0, v200\n"
    "v_sub_f32 v172, 0, v200\n"
    "v_sub_f32 v173, 0, v200\n"
    "v_sub_f32 v174, 0, v200\n"
    "v_sub_f32 v175, 0, v200\n"
    "v_mul_f32 v196, v196, v202\n"
    "v_mul_f32 v197, v197, v202\n"
    "v_mov_b32 v211, v193\n"
    "s_nop 1\n"
    "v_permlane32_swap_b32 v211, v193\n"
    "s_nop 0\n"
    "v_max_f32 v213, v211, v193\n"
    "v_max_f32 v213, 0, v213\n"
    "v_add_f32 v201, v201, v213\n"
    "v_sub_f32 v203, 0, v213\n"
    "v_exp_f32 v203, v203\n"
    "v_sub_f32 v80, v80, v213\n"
    "v_sub_f32 v81, v81, v213\n"
    "v_sub_f32 v82, v82, v213\n"
    "v_sub_f32 v83, v83, v213\n"
    "v_sub_f32 v84, v84, v213\n"
    "v_sub_f32 v85, v85, v213\n"
    "v_sub_f32 v86, v86, v213\n"
    "v_sub_f32 v87, v87, v213\n"
    "v_sub_f32 v88, v88, v213\n"
    "v_sub_f32 v89, v89, v213\n"
    "v_sub_f32 v90, v90, v213\n"
    "v_sub_f32 v91, v91, v213\n"
    "v_sub_f32 v92, v92, v213\n"
    "v_sub_f32 v93, v93, v213\n"
    "v_sub_f32 v94, v94, v213\n"
    "v_sub_f32 v95, v95, v213\n"
    "v_sub_f32 v112, v112, v213\n"
    "v_sub_f32 v113, v113, v213\n"
    "v_sub_f32 v114, v114, v213\n"
    "v_sub_f32 v115, v115, v213\n"
    "v_sub_f32 v116, v116, v213\n"
    "v_sub_f32 v117, v117, v213\n"
    "v_sub_f32 v118, v118, v213\n"
    "v_sub_f32 v119, v119, v213\n"
    "v_sub_f32 v120, v120, v213\n"
    "v_sub_f32 v121, v121, v213\n"
    "v_sub_f32 v122, v122, v213\n"
    "v_sub_f32 v123, v123, v213\n"
    "v_sub_f32 v124, v124, v213\n"
    "v_sub_f32 v125, v125, v213\n"
    "v_sub_f32 v126, v126, v213\n"
    "v_sub_f32 v127, v127, v213\n"
    "v_sub_f32 v176, 0, v201\n"
    "v_sub_f32 v177, 0, v201\n"
    "v_sub_f32 v178, 0, v201\n"
    "v_sub_f32 v179, 0, v201\n"
    "v_sub_f32 v180, 0, v201\n"
    "v_sub_f32 v181, 0, v201\n"
    "v_sub_f32 v182, 0, v201\n"
    "v_sub_f32 v183, 0, v201\n"
    "v_sub_f32 v184, 0, v201\n"
    "v_sub_f32 v185, 0, v201\n"
    "v_sub_f32 v186, 0, v201\n"
    "v_sub_f32 v187, 0, v201\n"
    "v_sub_f32 v188, 0, v201\n"
    "v_sub_f32 v189, 0, v201\n"
    "v_sub_f32 v190, 0, v201\n"
    "v_sub_f32 v191, 0, v201\n"
    "v_mul_f32 v198, v198, v203\n"
    "v_mul_f32 v199, v199, v203\n"
    "s_mov_b32 s91, 1\n"
    "s_nop 1\n"
    "s_branch L_b_ra_%=r\n"
    "L_t_m0_%=:\n"
    "v_mov_b32 v210, 4286578688\n"
    "v_cmp_gt_i32 s[82:83], 0, v206\n"
    "v_cmp_gt_i32 s[84:85], 1, v206\n"
    "v_cmp_gt_i32 s[86:87], 2, v206\n"
    "v_cndmask_b32 v0, v0, v210, s[82:83]\n"
    "v_cmp_gt_i32 s[82:83], 3, v206\n"
    "v_cndmask_b32 v1, v1, v210, s[84:85]\n"
    "v_cmp_gt_i32 s[84:85], 8, v206\n"
    "v_cndmask_b32 v2, v2, v210, s[86:87]\n"
    "v_cmp_gt_i32 s[86:87], 9, v206\n"
    "v_cndmask_b32 v3, v3, v210, s[82:83]\n"
    "v_cmp_gt_i32 s[82:83], 10, v206\n"
    "v_cndmask_b32 v4, v4, v210, s[84:85]\n"
    "v_cmp_gt_i32 s[84:85], 11, v206\n"
    "v_cndmask_b32 v5, v5, v210, s[86:87]\n"
    "v_cmp_gt_i32 s[86:87], 16, v206\n"
    "v_cndmask_b32 v6, v6, v210, s[82:83]\n"
    "v_cmp_gt_i32 s[82:83], 17, v206\n"
    "v_cndmask_b32 v7, v7, v210, s[84:85]\n"
    "v_cmp_gt_i32 s[84:85], 18, v206\n"
    "v_cndmask_b32 v8, v8, v210, s[86:87]\n"
    "v_cmp_gt_i32 s[86:87], 19, v206\n"
    "v_cndmask_b32 v9, v9, v210, s[82:83]\n"
    "v_cmp_gt_i32 s[82:83], 24, v206\n"
    "v_cndmask_b32 v10, v10, v210, s[84:85]\n"
    "v_cmp_gt_i32 s[84:85], 25, v206\n"
    "v_cndmask_b32 v11, v11, v210, s[86:87]\n"
    "v_cmp_gt_i32 s[86:87], 26, v206\n"
    "v_cndmask_b32 v12, v12, v210, s[82:83]\n"
    "v_cmp_gt_i32 s[82:83], 27, v206\n"
    "v_cndmask_b32 v13, v13, v210, s[84:85]\n"
    "v_cmp_gt_i32 s[84:85], 32, v206\n"
    "v_cndmask_b32 v14, v14, v210, s[86:87]\n"
    "v_cmp_gt_i32 s[86:87], 33, v206\n"
    "v_cndmask_b32 v15, v15, v210, s[82:83]\n"
    "v_cmp_gt_i32 s[82:83], 34, v206\n"
    "v_cndmask_b32 v16, v16, v210, s[84:85]\n"
    "v_cmp_gt_i32 s[84:85], 35, v206\n"
    "v_cndmask_b32 v17, v17, v210, s[86:87]\n"
    "v_cmp_gt_i32 s[86:87], 40, v206\n"
    "v_cndmask_b32 v18, v18, v210, s[82:83]\n"
    "v_cmp_gt_i32 s[82:83], 41, v206\n"
    "v_cndmask_b32 v19, v19, v210, s[84:85]\n"
    "v_cmp_gt_i32 s[84:85], 42, v206\n"
    "v_cndmask_b32 v20, v20, v210, s[86:87]\n"
    "v_cmp_gt_i32 s[86:87], 43, v206\n"
    "v_cndmask_b32 v21, v21, v210, s[82:83]\n"
    "v_cmp_gt_i32 s[82:83], 48, v206\n"
    "v_cndmask_b32 v22, v22, v210, s[84:85]\n"
    "v_cmp_gt_i32 s[84:85], 49, v206\n"
    "v_cndmask_b32 v23, v23, v210, s[86:87]\n"
    "v_cmp_gt_i32 s[86:87], 50, v206\n"
    "v_cndmask_b32 v24, v24, v210, s[82:83]\n"
    "v_cmp_gt_i32 s[82:83], 51, v206\n"
    "v_cndmask_b32 v25, v25, v210, s[84:85]\n"
    "v_cmp_gt_i32 s[84:85], 56, v206\n"
    "v_cndmask_b32 v26, v26, v210, s[86:87]\n"
    "v_cmp_gt_i32 s[86:87], 57, v206\n"
    "v_cndmask_b32 v27, v27, v210, s[82:83]\n"
    "v_cmp_gt_i32 s[82:83], 58, v206\n"
    "v_cndmask_b32 v28, v28, v210, s[84:85]\n"
    "v_cmp_gt_i32 s[84:85], 59, v206\n"
    "v_cndmask_b32 v29, v29, v210, s[86:87]\n"
    "s_nop 0\n"
    "v_cndmask_b32 v30, v30, v210, s[82:83]\n"
    "s_nop 0\n"
    "v_cndmask_b32 v31, v31, v210, s[84:85]\n"
    "s_branch L_t_m0_%=r\n"
    "L_t_m1_%=:\n"
    "v_mov_b32 v210, 4286578688\n"
    "v_cmp_gt_i32 s[82:83], 0, v207\n"
    "v_cmp_gt_i32 s[84:85], 1, v207\n"
    "v_cmp_gt_i32 s[86:87], 2, v207\n"
    "v_cndmask_b32 v32, v32, v210, s[82:83]\n"
    "v_cmp_gt_i32 s[82:83], 3, v207\n"
    "v_cndmask_b32 v33, v33, v210, s[84:85]\n"
    "v_cmp_gt_i32 s[84:85], 8, v207\n"
    "v_cndmask_b32 v34, v34, v210, s[86:87]\n"
    "v_cmp_gt_i32 s[86:87], 9, v207\n"
    "v_cndmask_b32 v35, v35, v210, s[82:83]\n"
    "v_cmp_gt_i32 s[82:83], 10, v207\n"
    "v_cndmask_b32 v36, v36, v210, s[84:85]\n"
    "v_cmp_gt_i32 s[84:85], 11, v207\n"
    "v_cndmask_b32 v37, v37, v210, s[86:87]\n"
    "v_cmp_gt_i32 s[86:87], 16, v207\n"
    "v_cndmask_b32 v38, v38, v210, s[82:83]\n"
    "v_cmp_gt_i32 s[82:83], 17, v207\n"
    "v_cndmask_b32 v39, v39, v210, s[84:85]\n"
    "v_cmp_gt_i32 s[84:85], 18, v207\n"
    "v_cndmask_b32 v40, v40, v210, s[86:87]\n"
    "v_cmp_gt_i32 s[86:87], 19, v207\n"
    "v_cndmask_b32 v41, v41, v210, s[82:83]\n"
    "v_cmp_gt_i32 s[82:83], 24, v207\n"
    "v_cndmask_b32 v42, v42, v210, s[84:85]\n"
    "v_cmp_gt_i32 s[84:85], 25, v207\n"
    "v_cndmask_b32 v43, v43, v210, s[86:87]\n"
    "v_cmp_gt_i32 s[86:87], 26, v207\n"
    "v_cndmask_b32 v44, v44, v210, s[82:83]\n"
    "v_cmp_gt_i32 s[82:83], 27, v207\n"
    "v_cndmask_b32 v45, v45, v210, s[84:85]\n"
    "v_cmp_gt_i32 s[84:85], 32, v207\n"
    "v_cndmask_b32 v46, v46, v210, s[86:87]\n"
    "v_cmp_gt_i32 s[86:87], 33, v207\n"
    "v_cndmask_b32 v47, v47, v210, s[82:83]\n"
    "v_cmp_gt_i32 s[82:83], 34, v207\n"
    "v_cndmask_b32 v48, v48, v210, s[84:85]\n"
    "v_cmp_gt_i32 s[84:85], 35, v207\n"
    "v_cndmask_b32 v49, v49, v210, s[86:87]\n"
    "v_cmp_gt_i32 s[86:87], 40, v207\n"
    "v_cndmask_b32 v50, v50, v210, s[82:83]\n"
    "v_cmp_gt_i32 s[82:83], 41, v207\n"
    "v_cndmask_b32 v51, v51, v210, s[84:85]\n"
    "v_cmp_gt_i32 s[84:85], 42, v207\n"
    "v_cndmask_b32 v52, v52, v210, s[86:87]\n"
    "v_cmp_gt_i32 s[86:87], 43, v207\n"
    "v_cndmask_b32 v53, v53, v210, s[82:83]\n"
    "v_cmp_gt_i32 s[82:83], 48, v207\n"
    "v_cndmask_b32 v54, v54, v210, s[84:85]\n"
    "v_cmp_gt_i32 s[84:85], 49, v207\n"
    "v_cndmask_b32 v55, v55, v210, s[86:87]\n"
    "v_cmp_gt_i32 s[86:87], 50, v207\n"
    "v_cndmask_b32 v56, v56, v210, s[82:83]\n"
    "v_cmp_gt_i32 s[82:83], 51, v207\n"
    "v_cndmask_b32 v57, v57, v210, s[84:85]\n"
    "v_cmp_gt_i32 s[84:85], 56, v207\n"
    "v_cndmask_b32 v58, v58, v210, s[86:87]\n"
    "v_cmp_gt_i32 s[86:87], 57, v207\n"
    "v_cndmask_b32 v59, v59, v210, s[82:83]\n"
    "v_cmp_gt_i32 s[82:83], 58, v207\n"
    "v_cndmask_b32 v60, v60, v210, s[84:85]\n"
    "v_cmp_gt_i32 s[84:85], 59, v207\n"
    "v_cndmask_b32 v61, v61, v210, s[86:87]\n"
    "s_nop 0\n"
    "v_cndmask_b32 v62, v62, v210, s[82:83]\n"
    "s_nop 0\n"
    "v_cndmask_b32 v63, v63, v210, s[84:85]\n"
    "s_branch L_t_m1_%=r\n"
    "L_t_ra_%=:\n"
    "v_mov_b32 v211, v193\n"
    "s_nop 1\n"
    "v_permlane32_swap_b32 v211, v193\n"
    "s_nop 0\n"
    "v_max_f32 v213, v211, v193\n"
    "v_max_f32 v213, 0, v213\n"
    "v_add_f32 v201, v201, v213\n"
    "v_sub_f32 v203, 0, v213\n"
    "v_exp_f32 v203, v203\n"
    "v_sub_f32 v0, v0, v213\n"
    "v_sub_f32 v1, v1, v213\n"
    "v_sub_f32 v2, v2, v213\n"
    "v_sub_f32 v3, v3, v213\n"
    "v_sub_f32 v4, v4, v213\n"
    "v_sub_f32 v5, v5, v213\n"
    "v_sub_f32 v6, v6, v213\n"
    "v_sub_f32 v7, v7, v213\n"
    "v_sub_f32 v8, v8, v213\n"
    "v_sub_f32 v9, v9, v213\n"
    "v_sub_f32 v10, v10, v213\n"
    "v_sub_f32 v11, v11, v213\n"
    "v_sub_f32 v12, v12, v213\n"
    "v_sub_f32 v13, v13, v213\n"
    "v_sub_f32 v14, v14, v213\n"
    "v_sub_f32 v15, v15, v213\n"
    "v_sub_f32 v16, v16, v213\n"
    "v_sub_f32 v17, v17, v213\n"
    "v_sub_f32 v18, v18, v213\n"
    "v_sub_f32 v19, v19, v213\n"
    "v_sub_f32 v20, v20, v213\n"
    "v_sub_f32 v21, v21, v213\n"
    "v_sub_f32 v22, v22, v213\n"
    "v_sub_f32 v23, v23, v213\n"
    "v_sub_f32 v24, v24, v213\n"
    "v_sub_f32 v25, v25, v213\n"
    "v_sub_f32 v26, v26, v213\n"
    "v_sub_f32 v27, v27, v213\n"
    "v_sub_f32 v28, v28, v213\n"
    "v_sub_f32 v29, v29, v213\n"
    "v_sub_f32 v30, v30, v213\n"
    "v_sub_f32 v31, v31, v213\n"
    "v_sub_f32 v32, v32, v213\n"
    "v_sub_f32 v33, v33, v213\n"
    "v_sub_f32 v34, v34, v213\n"
    "v_sub_f32 v35, v35, v213\n"
    "v_sub_f32 v36, v36, v213\n"
    "v_sub_f32 v37, v37, v213\n"
    "v_sub_f32 v38, v38, v213\n"
    "v_sub_f32 v39, v39, v213\n"
    "v_sub_f32 v40, v40, v213\n"
    "v_sub_f32 v41, v41, v213\n"
    "v_sub_f32 v42, v42, v213\n"
    "v_sub_f32 v43, v43, v213\n"
    "v_sub_f32 v44, v44, v213\n"
    "v_sub_f32 v45, v45, v213\n"
    "v_sub_f32 v46, v46, v213\n"
    "v_sub_f32 v47, v47, v213\n"
    "v_sub_f32 v48, v48, v213\n"
    "v_sub_f32 v49, v49, v213\n"
    "v_sub_f32 v50, v50, v213\n"
    "v_sub_f32 v51, v51, v213\n"
    "v_sub_f32 v52, v52, v213\n"
    "v_sub_f32 v53, v53, v213\n"
    "v_sub_f32 v54, v54, v213\n"
    "v_sub_f32 v55, v55, v213\n"
    "v_sub_f32 v56, v56, v213\n"
    "v_sub_f32 v57, v57, v213\n"
    "v_sub_f32 v58, v58, v213\n"
    "v_sub_f32 v59, v59, v213\n"
    "v_sub_f32 v60, v60, v213\n"
    "v_sub_f32 v61, v61, v213\n"
    "v_sub_f32 v62, v62, v213\n"
    "v_sub_f32 v63, v63, v213\n"
    "v_sub_f32 v176, 0, v201\n"
    "v_sub_f32 v177, 0, v201\n"
    "v_sub_f32 v178, 0, v201\n"
    "v_sub_f32 v179, 0, v201\n"
    "v_sub_f32 v180, 0, v201\n"
    "v_sub_f32 v181, 0, v201\n"
    "v_sub_f32 v182, 0, v201\n"
    "v_sub_f32 v183, 0, v201\n"
    "v_sub_f32 v184, 0, v201\n"
    "v_sub_f32 v185, 0, v201\n"
    "v_sub_f32 v186, 0, v201\n"
    "v_sub_f32 v187, 0, v201\n"
    "v_sub_f32 v188, 0, v201\n"
    "v_sub_f32 v189, 0, v201\n"
    "v_sub_f32 v190, 0, v201\n"
    "v_sub_f32 v191, 0, v201\n"
    "v_mul_f32 v198, v198, v203\n"
    "v_mul_f32 v199, v199, v203\n"
    "v_mov_b32 v202, 0x3f800000\n"
    "s_mov_b32 s91, 1\n"
    "s_nop 1\n"
    "s_branch L_t_ra_%=r\n"
    "L_s1_ra_%=:\n"
    "v_mov_b32 v211, v193\n"
    "s_nop 1\n"
    "v_permlane32_swap_b32 v211, v193\n"
    "s_nop 0\n"
    "v_max_f32 v213, v211, v193\n"
    "v_max_f32 v213, 0, v213\n"
    "v_add_f32 v201, v201, v213\n"
    "v_sub_f32 v203, 0, v213\n"
    "v_exp_f32 v203, v203\n"
    "v_sub_f32 v64, v64, v213\n"
    "v_sub_f32 v65, v65, v213\n"
    "v_sub_f32 v66, v66, v213\n"
    "v_sub_f32 v67, v67, v213\n"
    "v_sub_f32 v68, v68, v213\n"
    "v_sub_f32 v69, v69, v213\n"
    "v_sub_f32 v70, v70, v213\n"
    "v_sub_f32 v71, v71, v213\n"
    "v_sub_f32 v72, v72, v213\n"
    "v_sub_f32 v73, v73, v213\n"
    "v_sub_f32 v74, v74, v213\n"
    "v_sub_f32 v75, v75, v213\n"
    "v_sub_f32 v76, v76, v213\n"
    "v_sub_f32 v77, v77, v213\n"
    "v_sub_f32 v78, v78, v213\n"
    "v_sub_f32 v79, v79, v213\n"
    "v_sub_f32 v80, v80, v213\n"
    "v_sub_f32 v81, v81, v213\n"
    "v_sub_f32 v82, v82, v213\n"
    "v_sub_f32 v83, v83, v213\n"
    "v_sub_f32 v84, v84, v213\n"
    "v_sub_f32 v85, v85, v213\n"
    "v_sub_f32 v86, v86, v213\n"
    "v_sub_f32 v87, v87, v213\n"
    "v_sub_f32 v88, v88, v213\n"
    "v_sub_f32 v89, v89, v213\n"
    "v_sub_f32 v90, v90, v213\n"
    "v_sub_f32 v91, v91, v213\n"
    "v_sub_f32 v92, v92, v213\n"
    "v_sub_f32 v93, v93, v213\n"
    "v_sub_f32 v94, v94, v213\n"
    "v_sub_f32 v95, v95, v213\n"
    "v_sub_f32 v96, v96, v213\n"
    "v_sub_f32 v97, v97, v213\n"
    "v_sub_f32 v98, v98, v213\n"
    "v_sub_f32 v99, v99, v213\n"
    "v_sub_f32 v100, v100, v213\n"
    "v_sub_f32 v101, v101, v213\n"
    "v_sub_f32 v102, v102, v213\n"
    "v_sub_f32 v103, v103, v213\n"
    "v_sub_f32 v104, v104, v213\n"
    "v_sub_f32 v105, v105, v213\n"
    "v_sub_f32 v106, v106, v213\n"
    "v_sub_f32 v107, v107, v213\n"
    "v_sub_f32 v108, v108, v213\n"
    "v_sub_f32 v109, v109, v213\n"
    "v_sub_f32 v110, v110, v213\n"
    "v_sub_f32 v111, v111, v213\n"
    "v_sub_f32 v112, v112, v213\n"
    "v_sub_f32 v113, v113, v213\n"
    "v_sub_f32 v114, v114, v213\n"
    "v_sub_f32 v115, v115, v213\n"
    "v_sub_f32 v116, v116, v213\n"
    "v_sub_f32 v117, v117, v213\n"
    "v_sub_f32 v118, v118, v213\n"
    "v_sub_f32 v119, v119, v213\n"
    "v_sub_f32 v120, v120, v213\n"
    "v_sub_f32 v121, v121, v213\n"
    "v_sub_f32 v122, v122, v213\n"
    "v_sub_f32 v123, v123, v213\n"
    "v_sub_f32 v124, v124, v213\n"
    "v_sub_f32 v125, v125, v213\n"
    "v_sub_f32 v126, v126, v213\n"
    "v_sub_f32 v127, v127, v213\n"
    "v_sub_f32 v176, 0, v201\n"
    "v_sub_f32 v177, 0, v201\n"
    "v_sub_f32 v178, 0, v201\n"
    "v_sub_f32 v179, 0, v201\n"
    "v_sub_f32 v180, 0, v201\n"
    "v_sub_f32 v181, 0, v201\n"
    "v_sub_f32 v182, 0, v201\n"
    "v_sub_f32 v183, 0, v201\n"
    "v_sub_f32 v184, 0, v201\n"
    "v_sub_f32 v185, 0, v201\n"
    "v_sub_f32 v186, 0, v201\n"
    "v_sub_f32 v187, 0, v201\n"
    "v_sub_f32 v188, 0, v201\n"
    "v_sub_f32 v189, 0, v201\n"
    "v_sub_f32 v190, 0, v201\n"
    "v_sub_f32 v191, 0, v201\n"
    "v_mul_f32 v198, v198, v203\n"
    "v_mul_f32 v199, v199, v203\n"
    "v_mov_b32 v202, 0x3f800000\n"
    "s_mov_b32 s91, 1\n"
    "s_nop 1\n"
    "s_branch L_s1_ra_%=r\n"
    "L_s0_m0_%=:\n"
    "v_mov_b32 v210, 4286578688\n"
    "v_cmp_gt_i32 s[82:83], 0, v206\n"
    "v_cmp_gt_i32 s[84:85], 1, v206\n"
    "v_cmp_gt_i32 s[86:87], 2, v206\n"
    "v_cndmask_b32 v0, v0, v210, s[82:83]\n"
    "v_cmp_gt_i32 s[82:83], 3, v206\n"
    "v_cndmask_b32 v1, v1, v210, s[84:85]\n"
    "v_cmp_gt_i32 s[84:85], 8, v206\n"
    "v_cndmask_b32 v2, v2, v210, s[86:87]\n"
    "v_cmp_gt_i32 s[86:87], 9, v206\n"
    "v_cndmask_b32 v3, v3, v210, s[82:83]\n"
    "v_cmp_gt_i32 s[82:83], 10, v206\n"
    "v_cndmask_b32 v4, v4, v210, s[84:85]\n"
    "v_cmp_gt_i32 s[84:85], 11, v206\n"
    "v_cndmask_b32 v5, v5, v210, s[86:87]\n"
    "v_cmp_gt_i32 s[86:87], 16, v206\n"
    "v_cndmask_b32 v6, v6, v210, s[82:83]\n"
    "v_cmp_gt_i32 s[82:83], 17, v206\n"
    "v_cndmask_b32 v7, v7, v210, s[84:85]\n"
    "v_cmp_gt_i32 s[84:85], 18, v206\n"
    "v_cndmask_b32 v8, v8, v210, s[86:87]\n"
    "v_cmp_gt_i32 s[86:87], 19, v206\n"
    "v_cndmask_b32 v9, v9, v210, s[82:83]\n"
    "v_cmp_gt_i32 s[82:83], 24, v206\n"
    "v_cndmask_b32 v10, v10, v210, s[84:85]\n"
    "v_cmp_gt_i32 s[84:85], 25, v206\n"
    "v_cndmask_b32 v11, v11, v210, s[86:87]\n"
    "v_cmp_gt_i32 s[86:87], 26, v206\n"
    "v_cndmask_b32 v12, v12, v210, s[82:83]\n"
    "v_cmp_gt_i32 s[82:83], 27, v206\n"
    "v_cndmask_b32 v13, v13, v210, s[84:85]\n"
    "v_cmp_gt_i32 s[84:85], 32, v206\n"
    "v_cndmask_b32 v14, v14, v210, s[86:87]\n"
    "v_cmp_gt_i32 s[86:87], 33, v206\n"
    "v_cndmask_b32 v15, v15, v210, s[82:83]\n"
    "v_cmp_gt_i32 s[82:83], 34, v206\n"
    "v_cndmask_b32 v16, v16, v210, s[84:85]\n"
    "v_cmp_gt_i32 s[84:85], 35, v206\n"
    "v_cndmask_b32 v17, v17, v210, s[86:87]\n"
    "v_cmp_gt_i32 s[86:87], 40, v206\n"
    "v_cndmask_b32 v18, v18, v210, s[82:83]\n"
    "v_cmp_gt_i32 s[82:83], 41, v206\n"
    "v_cndmask_b32 v19, v19, v210, s[84:85]\n"
    "v_cmp_gt_i32 s[84:85], 42, v206\n"
    "v_cndmask_b32 v20, v20, v210, s[86:87]\n"
    "v_cmp_gt_i32 s[86:87], 43, v206\n"
    "v_cndmask_b32 v21, v21, v210, s[82:83]\n"
    "v_cmp_gt_i32 s[82:83], 48, v206\n"
    "v_cndmask_b32 v22, v22, v210, s[84:85]\n"
    "v_cmp_gt_i32 s[84:85], 49, v206\n"
    "v_cndmask_b32 v23, v23, v210, s[86:87]\n"
    "v_cmp_gt_i32 s[86:87], 50, v206\n"
    "v_cndmask_b32 v24, v24, v210, s[82:83]\n"
    "v_cmp_gt_i32 s[82:83], 51, v206\n"
    "v_cndmask_b32 v25, v25, v210, s[84:85]\n"
    "v_cmp_gt_i32 s[84:85], 56, v206\n"
    "v_cndmask_b32 v26, v26, v210, s[86:87]\n"
    "v_cmp_gt_i32 s[86:87], 57, v206\n"
    "v_cndmask_b32 v27, v27, v210, s[82:83]\n"
    "v_cmp_gt_i32 s[82:83], 58, v206\n"
    "v_cndmask_b32 v28, v28, v210, s[84:85]\n"
    "v_cmp_gt_i32 s[84:85], 59, v206\n"
    "v_cndmask_b32 v29, v29, v210, s[86:87]\n"
    "s_nop 0\n"
    "v_cndmask_b32 v30, v30, v210, s[82:83]\n"
    "s_nop 0\n"
    "v_cndmask_b32 v31, v31, v210, s[84:85]\n"
    "s_branch L_s0_m0_%=r\n"
    "L_s0_m1_%=:\n"
    "v_mov_b32 v210, 4286578688\n"
    "v_cmp_gt_i32 s[82:83], 0, v207\n"
    "v_cmp_gt_i32 s[84:85], 1, v207\n"
    "v_cmp_gt_i32 s[86:87], 2, v207\n"
    "v_cndmask_b32 v32, v32, v210, s[82:83]\n"
    "v_cmp_gt_i32 s[82:83], 3, v207\n"
    "v_cndmask_b32 v33, v33, v210, s[84:85]\n"
    "v_cmp_gt_i32 s[84:85], 8, v207\n"
    "v_cndmask_b32 v34, v34, v210, s[86:87]\n"
    "v_cmp_gt_i32 s[86:87], 9, v207\n"
    "v_cndmask_b32 v35, v35, v210, s[82:83]\n"
    "v_cmp_gt_i32 s[82:83], 10, v207\n"
    "v_cndmask_b32 v36, v36, v210, s[84:85]\n"
    "v_cmp_gt_i32 s[84:85], 11, v207\n"
    "v_cndmask_b32 v37, v37, v210, s[86:87]\n"
    "v_cmp_gt_i32 s[86:87], 16, v207\n"
    "v_cndmask_b32 v38, v38, v210, s[82:83]\n"
    "v_cmp_gt_i32 s[82:83], 17, v207\n"
    "v_cndmask_b32 v39, v39, v210, s[84:85]\n"
    "v_cmp_gt_i32 s[84:85], 18, v207\n"
    "v_cndmask_b32 v40, v40, v210, s[86:87]\n"
    "v_cmp_gt_i32 s[86:87], 19, v207\n"
    "v_cndmask_b32 v41, v41, v210, s[82:83]\n"
    "v_cmp_gt_i32 s[82:83], 24, v207\n"
    "v_cndmask_b32 v42, v42, v210, s[84:85]\n"
    "v_cmp_gt_i32 s[84:85], 25, v207\n"
    "v_cndmask_b32 v43, v43, v210, s[86:87]\n"
    "v_cmp_gt_i32 s[86:87], 26, v207\n"
    "v_cndmask_b32 v44, v44, v210, s[82:83]\n"
    "v_cmp_gt_i32 s[82:83], 27, v207\n"
    "v_cndmask_b32 v45, v45, v210, s[84:85]\n"
    "v_cmp_gt_i32 s[84:85], 32, v207\n"
    "v_cndmask_b32 v46, v46, v210, s[86:87]\n"
    "v_cmp_gt_i32 s[86:87], 33, v207\n"
    "v_cndmask_b32 v47, v47, v210, s[82:83]\n"
    "v_cmp_gt_i32 s[82:83], 34, v207\n"
    "v_cndmask_b32 v48, v48, v210, s[84:85]\n"
    "v_cmp_gt_i32 s[84:85], 35, v207\n"
    "v_cndmask_b32 v49, v49, v210, s[86:87]\n"
    "v_cmp_gt_i32 s[86:87], 40, v207\n"
    "v_cndmask_b32 v50, v50, v210, s[82:83]\n"
    "v_cmp_gt_i32 s[82:83], 41, v207\n"
    "v_cndmask_b32 v51, v51, v210, s[84:85]\n"
    "v_cmp_gt_i32 s[84:85], 42, v207\n"
    "v_cndmask_b32 v52, v52, v210, s[86:87]\n"
    "v_cmp_gt_i32 s[86:87], 43, v207\n"
    "v_cndmask_b32 v53, v53, v210, s[82:83]\n"
    "v_cmp_gt_i32 s[82:83], 48, v207\n"
    "v_cndmask_b32 v54, v54, v210, s[84:85]\n"
    "v_cmp_gt_i32 s[84:85], 49, v207\n"
    "v_cndmask_b32 v55, v55, v210, s[86:87]\n"
    "v_cmp_gt_i32 s[86:87], 50, v207\n"
    "v_cndmask_b32 v56, v56, v210, s[82:83]\n"
    "v_cmp_gt_i32 s[82:83], 51, v207\n"
    "v_cndmask_b32 v57, v57, v210, s[84:85]\n"
    "v_cmp_gt_i32 s[84:85], 56, v207\n"
    "v_cndmask_b32 v58, v58, v210, s[86:87]\n"
    "v_cmp_gt_i32 s[86:87], 57, v207\n"
    "v_cndmask_b32 v59, v59, v210, s[82:83]\n"
    "v_cmp_gt_i32 s[82:83], 58, v207\n"
    "v_cndmask_b32 v60, v60, v210, s[84:85]\n"
    "v_cmp_gt_i32 s[84:85], 59, v207\n"
    "v_cndmask_b32 v61, v61, v210, s[86:87]\n"
    "s_nop 0\n"
    "v_cndmask_b32 v62, v62, v210, s[82:83]\n"
    "s_nop 0\n"
    "v_cndmask_b32 v63, v63, v210, s[84:85]\n"
    "s_branch L_s0_m1_%=r\n"
    "L_s0_ra_%=:\n"
    "v_mov_b32 v211, v193\n"
    "s_nop 1\n"
    "v_permlane32_swap_b32 v211, v193\n"
    "s_nop 0\n"
    "v_max_f32 v213, v211, v193\n"
    "v_max_f32 v213, 0, v213\n"
    "v_add_f32 v201, v201, v213\n"
    "v_sub_f32 v203, 0, v213\n"
    "v_exp_f32 v203, v203\n"
    "v_sub_f32 v0, v0, v213\n"
    "v_sub_f32 v1, v1, v213\n"
    "v_sub_f32 v2, v2, v213\n"
    "v_sub_f32 v3, v3, v213\n"
    "v_sub_f32 v4, v4, v213\n"
    "v_sub_f32 v5, v5, v213\n"
    "v_sub_f32 v6, v6, v213\n"
    "v_sub_f32 v7, v7, v213\n"
    "v_sub_f32 v8, v8, v213\n"
    "v_sub_f32 v9, v9, v213\n"
    "v_sub_f32 v10, v10, v213\n"
    "v_sub_f32 v11, v11, v213\n"
    "v_sub_f32 v12, v12, v213\n"
    "v_sub_f32 v13, v13, v213\n"
    "v_sub_f32 v14, v14, v213\n"
    "v_sub_f32 v15, v15, v213\n"
    "v_sub_f32 v16, v16, v213\n"
    "v_sub_f32 v17, v17, v213\n"
    "v_sub_f32 v18, v18, v213\n"
    "v_sub_f32 v19, v19, v213\n"
    "v_sub_f32 v20, v20, v213\n"
    "v_sub_f32 v21, v21, v213\n"
    "v_sub_f32 v22, v22, v213\n"
    "v_sub_f32 v23, v23, v213\n"
    "v_sub_f32 v24, v24, v213\n"
    "v_sub_f32 v25, v25, v213\n"
    "v_sub_f32 v26, v26, v213\n"
    "v_sub_f32 v27, v27, v213\n"
    "v_sub_f32 v28, v28, v213\n"
    "v_sub_f32 v29, v29, v213\n"
    "v_sub_f32 v30, v30, v213\n"
    "v_sub_f32 v31, v31, v213\n"
    "v_sub_f32 v32, v32, v213\n"
    "v_sub_f32 v33, v33, v213\n"
    "v_sub_f32 v34, v34, v213\n"
    "v_sub_f32 v35, v35, v213\n"
    "v_sub_f32 v36, v36, v213\n"
    "v_sub_f32 v37, v37, v213\n"
    "v_sub_f32 v38, v38, v213\n"
    "v_sub_f32 v39, v39, v213\n"
    "v_sub_f32 v40, v40, v213\n"
    "v_sub_f32 v41, v41, v213\n"
    "v_sub_f32 v42, v42, v213\n"
    "v_sub_f32 v43, v43, v213\n"
    "v_sub_f32 v44, v44, v213\n"
    "v_sub_f32 v45, v45, v213\n"
    "v_sub_f32 v46, v46, v213\n"
    "v_sub_f32 v47, v47, v213\n"
    "v_sub_f32 v48, v48, v213\n"
    "v_sub_f32 v49, v49, v213\n"
    "v_sub_f32 v50, v50, v213\n"
    "v_sub_f32 v51, v51, v213\n"
    "v_sub_f32 v52, v52, v213\n"
    "v_sub_f32 v53, v53, v213\n"
    "v_sub_f32 v54, v54, v213\n"
    "v_sub_f32 v55, v55, v213\n"
    "v_sub_f32 v56, v56, v213\n"
    "v_sub_f32 v57, v57, v213\n"
    "v_sub_f32 v58, v58, v213\n"
    "v_sub_f32 v59, v59, v213\n"
    "v_sub_f32 v60, v60, v213\n"
    "v_sub_f32 v61, v61, v213\n"
    "v_sub_f32 v62, v62, v213\n"
    "v_sub_f32 v63, v63, v213\n"
    "v_sub_f32 v176, 0, v201\n"
    "v_sub_f32 v177, 0, v201\n"
    "v_sub_f32 v178, 0, v201\n"
    "v_sub_f32 v179, 0, v201\n"
    "v_sub_f32 v180, 0, v201\n"
    "v_sub_f32 v181, 0, v201\n"
    "v_sub_f32 v182, 0, v201\n"
    "v_sub_f32 v183, 0, v201\n"
    "v_sub_f32 v184, 0, v201\n"
    "v_sub_f32 v185, 0, v201\n"
    "v_sub_f32 v186, 0, v201\n"
    "v_sub_f32 v187, 0, v201\n"
    "v_sub_f32 v188, 0, v201\n"
    "v_sub_f32 v189, 0, v201\n"
    "v_sub_f32 v190, 0, v201\n"
    "v_sub_f32 v191, 0, v201\n"
    "v_mul_f32 v198, v198, v203\n"
    "v_mul_f32 v199, v199, v203\n"
    "v_mov_b32 v202, 0x3f800000\n"
    "s_mov_b32 s91, 1\n"
    "s_nop 1\n"
    "s_branch L_s0_ra_%=r\n"
    "L_rareb_%=:\n"
    "s_nop 15\n"
    "v_accvgpr_read_b32 v214, a0\n"
    "v_accvgpr_read_b32 v215, a1\n"
    "v_accvgpr_read_b32 v216, a2\n"
    "v_accvgpr_read_b32 v217, a3\n"
    "v_mul_f32 v214, v214, v202\n"
    "v_mul_f32 v215, v215, v202\n"
    "v_mul_f32 v216, v216, v202\n"
    "v_mul_f32 v217, v217, v202\n"
    "v_accvgpr_write_b32 a0, v214\n"
    "v_accvgpr_write_b32 a1, v215\n"
    "v_accvgpr_write_b32 a2, v216\n"
    "v_accvgpr_write_b32 a3, v217\n"
    "v_accvgpr_read_b32 v214, a4\n"
    "v_accvgpr_read_b32 v215, a5\n"
    "v_accvgpr_read_b32 v216, a6\n"
    "v_accvgpr_read_b32 v217, a7\n"
    "v_mul_f32 v214, v214, v202\n"
    "v_mul_f32 v215, v215, v202\n"
    "v_mul_f32 v216, v216, v202\n"
    "v_mul_f32 v217, v217, v202\n"
    "v_accvgpr_write_b32 a4, v214\n"
    "v_accvgpr_write_b32 a5, v215\n"
    "v_accvgpr_write_b32 a6, v216\n"
    "v_accvgpr_write_b32 a7, v217\n"
    "v_accvgpr_read_b32 v214, a8\n"
    "v_accvgpr_read_b32 v215, a9\n"
    "v_accvgpr_read_b32 v216, a10\n"
    "v_accvgpr_read_b32 v217, a11\n"
    "v_mul_f32 v214, v214, v202\n"
    "v_mul_f32 v215, v215, v202\n"
    "v_mul_f32 v216, v216, v202\n"
    "v_mul_f32 v217, v217, v202\n"
    "v_accvgpr_write_b32 a8, v214\n"
    "v_accvgpr_write_b32 a9, v215\n"
    "v_accvgpr_write_b32 a10, v216\n"
    "v_accvgpr_write_b32 a11, v217\n"
    "v_accvgpr_read_b32 v214, a12\n"
    "v_accvgpr_read_b32 v215, a13\n"
    "v_accvgpr_read_b32 v216, a14\n"
    "v_accvgpr_read_b32 v217, a15\n"
    "v_mul_f32 v214, v214, v202\n"
    "v_mul_f32 v215, v215, v202\n"
    "v_mul_f32 v216, v216, v202\n"
    "v_mul_f32 v217, v217, v202\n"
    "v_accvgpr_write_b32 a12, v214\n"
    "v_accvgpr_write_b32 a13, v215\n"
    "v_accvgpr_write_b32 a14, v216\n"
    "v_accvgpr_write_b32 a15, v217\n"
    "v_accvgpr_read_b32 v214, a16\n"
    "v_accvgpr_read_b32 v215, a17\n"
    "v_accvgpr_read_b32 v216, a18\n"
    "v_accvgpr_read_b32 v217, a19\n"
    "v_mul_f32 v214, v214, v203\n"
    "v_mul_f32 v215, v215, v203\n"
    "v_mul_f32 v216, v216, v203\n"
    "v_mul_f32 v217, v217, v203\n"
    "v_accvgpr_write_b32 a16, v214\n"
    "v_accvgpr_write_b32 a17, v215\n"
    "v_accvgpr_write_b32 a18, v216\n"
    "v_accvgpr_write_b32 a19, v217\n"
    "v_accvgpr_read_b32 v214, a20\n"
    "v_accvgpr_read_b32 v215, a21\n"
    "v_accvgpr_read_b32 v216, a22\n"
    "v_accvgpr_read_b32 v217, a23\n"
    "v_mul_f32 v214, v214, v203\n"
    "v_mul_f32 v215, v215, v203\n"
    "v_mul_f32 v216, v216, v203\n"
    "v_mul_f32 v217, v217, v203\n"
    "v_accvgpr_write_b32 a20, v214\n"
    "v_accvgpr_write_b32 a21, v215\n"
    "v_accvgpr_write_b32 a22, v216\n"
    "v_accvgpr_write_b32 a23, v217\n"
    "v_accvgpr_read_b32 v214, a24\n"
    "v_accvgpr_read_b32 v215, a25\n"
    "v_accvgpr_read_b32 v216, a26\n"
    "v_accvgpr_read_b32 v217, a27\n"
    "v_mul_f32 v214, v214, v203\n"
    "v_mul_f32 v215, v215, v203\n"
    "v_mul_f32 v216, v216, v203\n"
    "v_mul_f32 v217, v217, v203\n"
    "v_accvgpr_write_b32 a24, v214\n"
    "v_accvgpr_write_b32 a25, v215\n"
    "v_accvgpr_write_b32 a26, v216\n"
    "v_accvgpr_write_b32 a27, v217\n"
    "v_accvgpr_read_b32 v214, a28\n"
    "v_accvgpr_read_b32 v215, a29\n"
    "v_accvgpr_read_b32 v216, a30\n"
    "v_accvgpr_read_b32 v217, a31\n"
    "v_mul_f32 v214, v214, v203\n"
    "v_mul_f32 v215, v215, v203\n"
    "v_mul_f32 v216, v216, v203\n"
    "v_mul_f32 v217, v217, v203\n"
    "v_accvgpr_write_b32 a28, v214\n"
    "v_accvgpr_write_b32 a29, v215\n"
    "v_accvgpr_write_b32 a30, v216\n"
    "v_accvgpr_write_b32 a31, v217\n"
    "v_accvgpr_read_b32 v214, a32\n"
    "v_accvgpr_read_b32 v215, a33\n"
    "v_accvgpr_read_b32 v216, a34\n"
    "v_accvgpr_read_b32 v217, a35\n"
    "v_mul_f32 v214, v214, v202\n"
    "v_mul_f32 v215, v215, v202\n"
    "v_mul_f32 v216, v216, v202\n"
    "v_mul_f32 v217, v217, v202\n"
    "v_accvgpr_write_b32 a32, v214\n"
    "v_accvgpr_write_b32 a33, v215\n"
    "v_accvgpr_write_b32 a34, v216\n"
    "v_accvgpr_write_b32 a35, v217\n"
    "v_accvgpr_read_b32 v214, a36\n"
    "v_accvgpr_read_b32 v215, a37\n"
    "v_accvgpr_read_b32 v216, a38\n"
    "v_accvgpr_read_b32 v217, a39\n"
    "v_mul_f32 v214, v214, v202\n"
    "v_mul_f32 v215, v215, v202\n"
    "v_mul_f32 v216, v216, v202\n"
    "v_mul_f32 v217, v217, v202\n"
    "v_accvgpr_write_b32 a36, v214\n"
    "v_accvgpr_write_b32 a37, v215\n"
    "v_accvgpr_write_b32 a38, v216\n"
    "v_accvgpr_write_b32 a39, v217\n"
    "v_accvgpr_read_b32 v214, a40\n"
    "v_accvgpr_read_b32 v215, a41\n"
    "v_accvgpr_read_b32 v216, a42\n"
    "v_accvgpr_read_b32 v217, a43\n"
    "v_mul_f32 v214, v214, v202\n"
    "v_mul_f32 v215, v215, v202\n"
    "v_mul_f32 v216, v216, v202\n"
    "v_mul_f32 v217, v217, v202\n"
    "v_accvgpr_write_b32 a40, v214\n"
    "v_accvgpr_write_b32 a41, v215\n"
    "v_accvgpr_write_b32 a42, v216\n"
    "v_accvgpr_write_b32 a43, v217\n"
    "v_accvgpr_read_b32 v214, a44\n"
    "v_accvgpr_read_b32 v215, a45\n"
    "v_accvgpr_read_b32 v216, a46\n"
    "v_accvgpr_read_b32 v217, a47\n"
    "v_mul_f32 v214, v214, v202\n"
    "v_mul_f32 v215, v215, v202\n"
    "v_mul_f32 v216, v216, v202\n"
    "v_mul_f32 v217, v217, v202\n"
    "v_accvgpr_write_b32 a44, v214\n"
    "v_accvgpr_write_b32 a45, v215\n"
    "v_accvgpr_write_b32 a46, v216\n"
    "v_accvgpr_write_b32 a47, v217\n"
    "v_accvgpr_read_b32 v214, a48\n"
    "v_accvgpr_read_b32 v215, a49\n"
    "v_accvgpr_read_b32 v216, a50\n"
    "v_accvgpr_read_b32 v217, a51\n"
    "v_mul_f32 v214, v214, v203\n"
    "v_mul_f32 v215, v215, v203\n"
    "v_mul_f32 v216, v216, v203\n"
    "v_mul_f32 v217, v217, v203\n"
    "v_accvgpr_write_b32 a48, v214\n"
    "v_accvgpr_write_b32 a49, v215\n"
    "v_accvgpr_write_b32 a50, v216\n"
    "v_accvgpr_write_b32 a51, v217\n"
    "v_accvgpr_read_b32 v214, a52\n"
    "v_accvgpr_read_b32 v215, a53\n"
    "v_accvgpr_read_b32 v216, a54\n"
    "v_accvgpr_read_b32 v217, a55\n"
    "v_mul_f32 v214, v214, v203\n"
    "v_mul_f32 v215, v215, v203\n"
    "v_mul_f32 v216, v216, v203\n"
    "v_mul_f32 v217, v217, v203\n"
    "v_accvgpr_write_b32 a52, v214\n"
    "v_accvgpr_write_b32 a53, v215\n"
    "v_accvgpr_write_b32 a54, v216\n"
    "v_accvgpr_write_b32 a55, v217\n"
    "v_accvgpr_read_b32 v214, a56\n"
    "v_accvgpr_read_b32 v215, a57\n"
    "v_accvgpr_read_b32 v216, a58\n"
    "v_accvgpr_read_b32 v217, a59\n"
    "v_mul_f32 v214, v214, v203\n"
    "v_mul_f32 v215, v215, v203\n"
    "v_mul_f32 v216, v216, v203\n"
    "v_mul_f32 v217, v217, v203\n"
    "v_accvgpr_write_b32 a56, v214\n"
    "v_accvgpr_write_b32 a57, v215\n"
    "v_accvgpr_write_b32 a58, v216\n"
    "v_accvgpr_write_b32 a59, v217\n"
    "v_accvgpr_read_b32 v214, a60\n"
    "v_accvgpr_read_b32 v215, a61\n"
    "v_accvgpr_read_b32 v216, a62\n"
    "v_accvgpr_read_b32 v217, a63\n"
    "v_mul_f32 v214, v214, v203\n"
    "v_mul_f32 v215, v215, v203\n"
    "v_mul_f32 v216, v216, v203\n"
    "v_mul_f32 v217, v217, v203\n"
    "v_accvgpr_write_b32 a60, v214\n"
    "v_accvgpr_write_b32 a61, v215\n"
    "v_accvgpr_write_b32 a62, v216\n"
    "v_accvgpr_write_b32 a63, v217\n"
    "v_accvgpr_read_b32 v214, a64\n"
    "v_accvgpr_read_b32 v215, a65\n"
    "v_accvgpr_read_b32 v216, a66\n"
    "v_accvgpr_read_b32 v217, a67\n"
    "v_mul_f32 v214, v214, v202\n"
    "v_mul_f32 v215, v215, v202\n"
    "v_mul_f32 v216, v216, v202\n"
    "v_mul_f32 v217, v217, v202\n"
    "v_accvgpr_write_b32 a64, v214\n"
    "v_accvgpr_write_b32 a65, v215\n"
    "v_accvgpr_write_b32 a66, v216\n"
    "v_accvgpr_write_b32 a67, v217\n"
    "v_accvgpr_read_b32 v214, a68\n"
    "v_accvgpr_read_b32 v215, a69\n"
    "v_accvgpr_read_b32 v216, a70\n"
    "v_accvgpr_read_b32 v217, a71\n"
    "v_mul_f32 v214, v214, v202\n"
    "v_mul_f32 v215, v215, v202\n"
    "v_mul_f32 v216, v216, v202\n"
    "v_mul_f32 v217, v217, v202\n"
    "v_accvgpr_write_b32 a68, v214\n"
    "v_accvgpr_write_b32 a69, v215\n"
    "v_accvgpr_write_b32 a70, v216\n"
    "v_accvgpr_write_b32 a71, v217\n"
    "v_accvgpr_read_b32 v214, a72\n"
    "v_accvgpr_read_b32 v215, a73\n"
    "v_accvgpr_read_b32 v216, a74\n"
    "v_accvgpr_read_b32 v217, a75\n"
    "v_mul_f32 v214, v214, v202\n"
    "v_mul_f32 v215, v215, v202\n"
    "v_mul_f32 v216, v216, v202\n"
    "v_mul_f32 v217, v217, v202\n"
    "v_accvgpr_write_b32 a72, v214\n"
    "v_accvgpr_write_b32 a73, v215\n"
    "v_accvgpr_write_b32 a74, v216\n"
    "v_accvgpr_write_b32 a75, v217\n"
    "v_accvgpr_read_b32 v214, a76\n"
    "v_accvgpr_read_b32 v215, a77\n"
    "v_accvgpr_read_b32 v216, a78\n"
    "v_accvgpr_read_b32 v217, a79\n"
    "v_mul_f32 v214, v214, v202\n"
    "v_mul_f32 v215, v215, v202\n"
    "v_mul_f32 v216, v216, v202\n"
    "v_mul_f32 v217, v217, v202\n"
    "v_accvgpr_write_b32 a76, v214\n"
    "v_accvgpr_write_b32 a77, v215\n"
    "v_accvgpr_write_b32 a78, v216\n"
    "v_accvgpr_write_b32 a79, v217\n"
    "v_accvgpr_read_b32 v214, a80\n"
    "v_accvgpr_read_b32 v215, a81\n"
    "v_accvgpr_read_b32 v216, a82\n"
    "v_accvgpr_read_b32 v217, a83\n"
    "v_mul_f32 v214, v214, v203\n"
    "v_mul_f32 v215, v215, v203\n"
    "v_mul_f32 v216, v216, v203\n"
    "v_mul_f32 v217, v217, v203\n"
    "v_accvgpr_write_b32 a80, v214\n"
    "v_accvgpr_write_b32 a81, v215\n"
    "v_accvgpr_write_b32 a82, v216\n"
    "v_accvgpr_write_b32 a83, v217\n"
    "v_accvgpr_read_b32 v214, a84\n"
    "v_accvgpr_read_b32 v215, a85\n"
    "v_accvgpr_read_b32 v216, a86\n"
    "v_accvgpr_read_b32 v217, a87\n"
    "v_mul_f32 v214, v214, v203\n"
    "v_mul_f32 v215, v215, v203\n"
    "v_mul_f32 v216, v216, v203\n"
    "v_mul_f32 v217, v217, v203\n"
    "v_accvgpr_write_b32 a84, v214\n"
    "v_accvgpr_write_b32 a85, v215\n"
    "v_accvgpr_write_b32 a86, v216\n"
    "v_accvgpr_write_b32 a87, v217\n"
    "v_accvgpr_read_b32 v214, a88\n"
    "v_accvgpr_read_b32 v215, a89\n"
    "v_accvgpr_read_b32 v216, a90\n"
    "v_accvgpr_read_b32 v217, a91\n"
    "v_mul_f32 v214, v214, v203\n"
    "v_mul_f32 v215, v215, v203\n"
    "v_mul_f32 v216, v216, v203\n"
    "v_mul_f32 v217, v217, v203\n"
    "v_accvgpr_write_b32 a88, v214\n"
    "v_accvgpr_write_b32 a89, v215\n"
    "v_accvgpr_write_b32 a90, v216\n"
    "v_accvgpr_write_b32 a91, v217\n"
    "v_accvgpr_read_b32 v214, a92\n"
    "v_accvgpr_read_b32 v215, a93\n"
    "v_accvgpr_read_b32 v216, a94\n"
    "v_accvgpr_read_b32 v217, a95\n"
    "v_mul_f32 v214, v214, v203\n"
    "v_mul_f32 v215, v215, v203\n"
    "v_mul_f32 v216, v216, v203\n"
    "v_mul_f32 v217, v217, v203\n"
    "v_accvgpr_write_b32 a92, v214\n"
    "v_accvgpr_write_b32 a93, v215\n"
    "v_accvgpr_write_b32 a94, v216\n"
    "v_accvgpr_write_b32 a95, v217\n"
    "v_accvgpr_read_b32 v214, a96\n"
    "v_accvgpr_read_b32 v215, a97\n"
    "v_accvgpr_read_b32 v216, a98\n"
    "v_accvgpr_read_b32 v217, a99\n"
    "v_mul_f32 v214, v214, v202\n"
    "v_mul_f32 v215, v215, v202\n"
    "v_mul_f32 v216, v216, v202\n"
    "v_mul_f32 v217, v217, v202\n"
    "v_accvgpr_write_b32 a96, v214\n"
    "v_accvgpr_write_b32 a97, v215\n"
    "v_accvgpr_write_b32 a98, v216\n"
    "v_accvgpr_write_b32 a99, v217\n"
    "v_accvgpr_read_b32 v214, a100\n"
    "v_accvgpr_read_b32 v215, a101\n"
    "v_accvgpr_read_b32 v216, a102\n"
    "v_accvgpr_read_b32 v217, a103\n"
    "v_mul_f32 v214, v214, v202\n"
    "v_mul_f32 v215, v215, v202\n"
    "v_mul_f32 v216, v216, v202\n"
    "v_mul_f32 v217, v217, v202\n"
    "v_accvgpr_write_b32 a100, v214\n"
    "v_accvgpr_write_b32 a101, v215\n"
    "v_accvgpr_write_b32 a102, v216\n"
    "v_accvgpr_write_b32 a103, v217\n"
    "v_accvgpr_read_b32 v214, a104\n"
    "v_accvgpr_read_b32 v215, a105\n"
    "v_accvgpr_read_b32 v216, a106\n"
    "v_accvgpr_read_b32 v217, a107\n"
    "v_mul_f32 v214, v214, v202\n"
    "v_mul_f32 v215, v215, v202\n"
    "v_mul_f32 v216, v216, v202\n"
    "v_mul_f32 v217, v217, v202\n"
    "v_accvgpr_write_b32 a104, v214\n"
    "v_accvgpr_write_b32 a105, v215\n"
    "v_accvgpr_write_b32 a106, v216\n"
    "v_accvgpr_write_b32 a107, v217\n"
    "v_accvgpr_read_b32 v214, a108\n"
    "v_accvgpr_read_b32 v215, a109\n"
    "v_accvgpr_read_b32 v216, a110\n"
    "v_accvgpr_read_b32 v217, a111\n"
    "v_mul_f32 v214, v214, v202\n"
    "v_mul_f32 v215, v215, v202\n"
    "v_mul_f32 v216, v216, v202\n"
    "v_mul_f32 v217, v217, v202\n"
    "v_accvgpr_write_b32 a108, v214\n"
    "v_accvgpr_write_b32 a109, v215\n"
    "v_accvgpr_write_b32 a110, v216\n"
    "v_accvgpr_write_b32 a111, v217\n"
    "v_accvgpr_read_b32 v214, a112\n"
    "v_accvgpr_read_b32 v215, a113\n"
    "v_accvgpr_read_b32 v216, a114\n"
    "v_accvgpr_read_b32 v217, a115\n"
    "v_mul_f32 v214, v214, v203\n"
    "v_mul_f32 v215, v215, v203\n"
    "v_mul_f32 v216, v216, v203\n"
    "v_mul_f32 v217, v217, v203\n"
    "v_accvgpr_write_b32 a112, v214\n"
    "v_accvgpr_write_b32 a113, v215\n"
    "v_accvgpr_write_b32 a114, v216\n"
    "v_accvgpr_write_b32 a115, v217\n"
    "v_accvgpr_read_b32 v214, a116\n"
    "v_accvgpr_read_b32 v215, a117\n"
    "v_accvgpr_read_b32 v216, a118\n"
    "v_accvgpr_read_b32 v217, a119\n"
    "v_mul_f32 v214, v214, v203\n"
    "v_mul_f32 v215, v215, v203\n"
    "v_mul_f32 v216, v216, v203\n"
    "v_mul_f32 v217, v217, v203\n"
    "v_accvgpr_write_b32 a116, v214\n"
    "v_accvgpr_write_b32 a117, v215\n"
    "v_accvgpr_write_b32 a118, v216\n"
    "v_accvgpr_write_b32 a119, v217\n"
    "v_accvgpr_read_b32 v214, a120\n"
    "v_accvgpr_read_b32 v215, a121\n"
    "v_accvgpr_read_b32 v216, a122\n"
    "v_accvgpr_read_b32 v217, a123\n"
    "v_mul_f32 v214, v214, v203\n"
    "v_mul_f32 v215, v215, v203\n"
    "v_mul_f32 v216, v216, v203\n"
    "v_mul_f32 v217, v217, v203\n"
    "v_accvgpr_write_b32 a120, v214\n"
    "v_accvgpr_write_b32 a121, v215\n"
    "v_accvgpr_write_b32 a122, v216\n"
    "v_accvgpr_write_b32 a123, v217\n"
    "v_accvgpr_read_b32 v214, a124\n"
    "v_accvgpr_read_b32 v215, a125\n"
    "v_accvgpr_read_b32 v216, a126\n"
    "v_accvgpr_read_b32 v217, a127\n"
    "v_mul_f32 v214, v214, v203\n"
    "v_mul_f32 v215, v215, v203\n"
    "v_mul_f32 v216, v216, v203\n"
    "v_mul_f32 v217, v217, v203\n"
    "v_accvgpr_write_b32 a124, v214\n"
    "v_accvgpr_write_b32 a125, v215\n"
    "v_accvgpr_write_b32 a126, v216\n"
    "v_accvgpr_write_b32 a127, v217\n"
    "s_mov_b32 s91, 0\n"
    "s_nop 1\n"
    "s_setpc_b64 s[98:99]\n"
    "L_atend_%=:\n"
    "s_waitcnt vmcnt(0) lgkmcnt(0)\n"
    "L_codeend_%=:\n"
    :: "s"((unsigned)px), "s"((unsigned)(px >> 32)), "s"((unsigned)pc), "s"((unsigned)(pc >> 32)), "s"((unsigned)pq), "s"((unsigned)(pq >> 32)), "s"((unsigned)pk), "s"((unsigned)(pk >> 32)), "s"((unsigned)pv), "s"((unsigned)(pv >> 32)), "s"((unsigned)pw), "s"((unsigned)(pw >> 32)), "s"(wid), "s"(h), "s"(j), "s"(b), "s"((unsigned)po), "s"((unsigned)(po >> 32)), "s"((unsigned)p1), "s"((unsigned)(p1 >> 32)), "s"((unsigned)p2), "s"((unsigned)(p2 >> 32)), "s"((unsigned)p3), "s"((unsigned)(p3 >> 32)), "s"((unsigned)p4), "s"((unsigned)(p4 >> 32))
    : "v0", "v1", "v2", "v3", "v4", "v5", "v6", "v7", "v8", "v9", "v10", "v11", "v12", "v13", "v14", "v15", "v16", "v17", "v18", "v19", "v20", "v21", "v22", "v23", "v24", "v25", "v26", "v27", "v28", "v29", "v30", "v31", "v32", "v33", "v34", "v35", "v36", "v37", "v38", "v39", "v40", "v41", "v42", "v43", "v44", "v45", "v46", "v47", "v48", "v49", "v50", "v51", "v52", "v53", "v54", "v55", "v56", "v57", "v58", "v59", "v60", "v61", "v62", "v63", "v64", "v65", "v66", "v67", "v68", "v69", "v70", "v71", "v72", "v73", "v74", "v75", "v76", "v77", "v78", "v79", "v80", "v81", "v82", "v83", "v84", "v85", "v86", "v87", "v88", "v89", "v90", "v91", "v92", "v93", "v94", "v95", "v96", "v97", "v98", "v99", "v100", "v101", "v102", "v103", "v104", "v105", "v106", "v107", "v108", "v109", "v110", "v111", "v112", "v113", "v114", "v115", "v116", "v117", "v118", "v119", "v120", "v121", "v122", "v123", "v124", "v125", "v126", "v127", "v128", "v129", "v130", "v131", "v132", "v133", "v134", "v135", "v136", "v137", "v138", "v139", "v140", "v141", "v142", "v143", "v144", "v145", "v146", "v147", "v148", "v149", "v150", "v151", "v152", "v153", "v154", "v155", "v156", "v157", "v158", "v159", "v160", "v161", "v162", "v163", "v164", "v165", "v166", "v167", "v168", "v169", "v170", "v171", "v172", "v173", "v174", "v175", "v176", "v177", "v178", "v179", "v180", "v181", "v182", "v183", "v184", "v185", "v186", "v187", "v188", "v189", "v190", "v191", "v192", "v193", "v194", "v195", "v196", "v197", "v198", "v199", "v200", "v201", "v202", "v203", "v204", "v205", "v206", "v207", "v208", "v209", "v210", "v211", "v212", "v213", "v214", "v215", "v216", "v217", "v218", "v219", "v220", "v221", "v222", "v223", "v224", "v225", "v226", "v227", "v228", "v229", "v230", "v231", "v232", "v233", "v234", "v235", "v236", "v237", "v238", "v239", "v240", "v241", "v242", "v243", "v244", "v245", "v246", "v247", "v248", "v249", "v250", "v251", "v252", "v253", "v254", "v255", "a0", "a1", "a2", "a3", "a4", "a5", "a6", "a7", "a8", "a9", "a10", "a11", "a12", "a13", "a14", "a15", "a16", "a17", "a18", "a19", "a20", "a21", "a22", "a23", "a24", "a25", "a26", "a27", "a28", "a29", "a30", "a31", "a32", "a33", "a34", "a35", "a36", "a37", "a38", "a39", "a40", "a41", "a42", "a43", "a44", "a45", "a46", "a47", "a48", "a49", "a50", "a51", "a52", "a53", "a54", "a55", "a56", "a57", "a58", "a59", "a60", "a61", "a62", "a63", "a64", "a65", "a66", "a67", "a68", "a69", "a70", "a71", "a72", "a73", "a74", "a75", "a76", "a77", "a78", "a79", "a80", "a81", "a82", "a83", "a84", "a85", "a86", "a87", "a88", "a89", "a90", "a91", "a92", "a93", "a94", "a95", "a96", "a97", "a98", "a99", "a100", "a101", "a102", "a103", "a104", "a105", "a106", "a107", "a108", "a109", "a110", "a111", "a112", "a113", "a114", "a115", "a116", "a117", "a118", "a119", "a120", "a121", "a122", "a123", "a124", "a125", "a126", "a127", "a128", "a129", "a130", "a131", "a132", "a133", "a134", "a135", "a136", "a137", "a138", "a139", "a140", "a141", "a142", "a143", "a144", "a145", "a146", "a147", "a148", "a149", "a150", "a151", "a152", "a153", "a154", "a155", "a156", "a157", "a158", "a159", "a160", "a161", "a162", "a163", "a164", "a165", "a166", "a167", "a168", "a169", "a170", "a171", "a172", "a173", "a174", "a175", "a176", "a177", "a178", "a179", "a180", "a181", "a182", "a183", "a184", "a185", "a186", "a187", "a188", "a189", "a190", "a191", "a192", "a193", "a194", "a195", "a196", "a197", "a198", "a199", "a200", "a201", "a202", "a203", "a204", "a205", "a206", "a207", "a208", "a209", "a210", "a211", "a212", "a213", "a214", "a215", "a216", "a217", "a218", "a219", "a220", "a221", "a222", "a223", "a224", "a225", "a226", "a227", "a228", "a229", "a230", "a231", "a232", "a233", "a234", "a235", "a236", "a237", "a238", "a239", "a240", "a241", "a242", "a243", "a244", "a245", "a246", "a247", "a248", "a249", "a250", "a251", "a252", "a253", "a254", "a255", "s36", "s37", "s38", "s39", "s40", "s41", "s42", "s43", "s44", "s45", "s46", "s47", "s48", "s49", "s50", "s51", "s52", "s53", "s54", "s55", "s56", "s57", "s58", "s59", "s60", "s61", "s62", "s63", "s64", "s65", "s66", "s67", "s68", "s69", "s70", "s71", "s72", "s73", "s74", "s75", "s76", "s77", "s78", "s79", "s80", "s81", "s82", "s83", "s84", "s85", "s86", "s87", "s88", "s89", "s90", "s91", "s92", "s93", "s94", "s95", "s96", "s97", "s98", "s99", "vcc", "scc", "memory");
}

#define OUT_RED_BYTES (4 * 64 * 84 * 4)
#define OUT_X_OFF (OUT_RED_BYTES + 4 * CONV_LDS_W + 16)
#define OUT_LDS (OUT_X_OFF)
__global__ __launch_bounds__(768, 3) void k_out(const u16* __restrict__ x, const u16* __restrict__ ssk, const u16* __restrict__ ssd, char* __restrict__ ws, u16* __restrict__ out) {
  extern __shared__ __attribute__((aligned(16))) char smem[];
  const unsigned b = blockIdx.x, xcd = b & 7, idx = b >> 3;
  const unsigned rb = xcd * 4 + (idx & 3), cbk = idx >> 2;
  const unsigned w = __builtin_amdgcn_readfirstlane(threadIdx.x >> 6);
  if (threadIdx.x == 512) { *(unsigned*)(smem + OUT_RED_BYTES + 4 * CONV_LDS_W) = 0u; *(unsigned*)(smem + OUT_RED_BYTES + 4 * CONV_LDS_W + 4) = 0u; }
  __syncthreads();
  u32x4_t xv[3];
  if (w < 8) {
    const unsigned long long pw = (unsigned long long)ws;
    asm volatile(
    "s_add_u32 s40, %0, %5\n"
    "s_addc_u32 s41, %1, 0\n"
    "s_and_b32 s41, s41, 0xffff\n"
    "s_mov_b32 s42, 0x1000000\n"
    "s_mov_b32 s43, 0x00020000\n"
    "s_add_u32 s44, %0, %6\n"
    "s_addc_u32 s45, %1, 0\n"
    "s_and_b32 s45, s45, 0xffff\n"
    "s_mov_b32 s46, 0x500000\n"
    "s_mov_b32 s47, 0x00020000\n"
    "s_setprio 2\n"
    "v_mbcnt_lo_u32_b32 v158, -1, 0\n"
    "v_mbcnt_hi_u32_b32 v158, -1, v158\n"
    "v_lshlrev_b32 v157, 4, v158\n"
    "v_add_u32 v152, 0, v157\n"
    "v_add_u32 v153, 131072, v157\n"
    "v_add_u32 v154, 262144, v157\n"
    "v_add_u32 v155, 393216, v157\n"
    "v_add_u32 v156, 524288, v157\n"
    "s_lshl_b32 s53, %3, 19\n"
    "s_lshl_b32 s54, %2, 10\n"
    "s_add_u32 s50, s53, s54\n"
    "s_mul_i32 s53, %4, 655360\n"
    "s_add_u32 s51, s53, s54\n"
    "buffer_load_dwordx4 v[80:83], v152, s[40:43], s50 offen\n"
    "buffer_load_dwordx4 v[84:87], v153, s[40:43], s50 offen\n"
    "buffer_load_dwordx4 v[88:91], v154, s[40:43], s50 offen\n"
    "buffer_load_dwordx4 v[92:95], v155, s[40:43], s50 offen\n"
    "buffer_load_dwordx4 v[96:99], v152, s[44:47], s51 offen\n"
    "buffer_load_dwordx4 v[100:103], v153, s[44:47], s51 offen\n"
    "buffer_load_dwordx4 v[104:107], v154, s[44:47], s51 offen\n"
    "buffer_load_dwordx4 v[108:111], v155, s[44:47], s51 offen\n"
    "buffer_load_dwordx4 v[112:115], v156, s[44:47], s51 offen\n"
    "s_add_u32 s50, s50, 8192\n"
    "s_add_u32 s51, s51, 8192\n"
    "v_mov_b32 v0, 0\n"
    "v_mov_b32 v1, 0\n"
    "v_mov_b32 v2, 0\n"
    "v_mov_b32 v3, 0\n"
    "v_mov_b32 v4, 0\n"
    "v_mov_b32 v5, 0\n"
    "v_mov_b32 v6, 0\n"
    "v_mov_b32 v7, 0\n"
    "v_mov_b32 v8, 0\n"
    "v_mov_b32 v9, 0\n"
    "v_mov_b32 v10, 0\n"
    "v_mov_b32 v11, 0\n"
    "v_mov_b32 v12, 0\n"
    "v_mov_b32 v13, 0\n"
    "v_mov_b32 v14, 0\n"
    "v_mov_b32 v15, 0\n"
    "v_mov_b32 v16, 0\n"
    "v_mov_b32 v17, 0\n"
    "v_mov_b32 v18, 0\n"
    "v_mov_b32 v19, 0\n"
    "v_mov_b32 v20, 0\n"
    "v_mov_b32 v21, 0\n"
    "v_mov_b32 v22, 0\n"
    "v_mov_b32 v23, 0\n"
    "v_mov_b32 v24, 0\n"
    "v_mov_b32 v25, 0\n"
    "v_mov_b32 v26, 0\n"
    "v_mov_b32 v27, 0\n"
    "v_mov_b32 v28, 0\n"
    "v_mov_b32 v29, 0\n"
    "v_mov_b32 v30, 0\n"
    "v_mov_b32 v31, 0\n"
    "v_mov_b32 v32, 0\n"
    "v_mov_b32 v33, 0\n"
    "v_mov_b32 v34, 0\n"
    "v_mov_b32 v35, 0\n"
    "v_mov_b32 v36, 0\n"
    "v_mov_b32 v37, 0\n"
    "v_mov_b32 v38, 0\n"
    "v_mov_b32 v39, 0\n"
    "v_mov_b32 v40, 0\n"
    "v_mov_b32 v41, 0\n"
    "v_mov_b32 v42, 0\n"
    "v_mov_b32 v43, 0\n"
    "v_mov_b32 v44, 0\n"
    "v_mov_b32 v45, 0\n"
    "v_mov_b32 v46, 0\n"
    "v_mov_b32 v47, 0\n"
    "v_mov_b32 v48, 0\n"
    "v_mov_b32 v49, 0\n"
    "v_mov_b32 v50, 0\n"
    "v_mov_b32 v51, 0\n"
    "v_mov_b32 v52, 0\n"
    "v_mov_b32 v53, 0\n"
    "v_mov_b32 v54, 0\n"
    "v_mov_b32 v55, 0\n"
    "v_mov_b32 v56, 0\n"
    "v_mov_b32 v57, 0\n"
    "v_mov_b32 v58, 0\n"
    "v_mov_b32 v59, 0\n"
    "v_mov_b32 v60, 0\n"
    "v_mov_b32 v61, 0\n"
    "v_mov_b32 v62, 0\n"
    "v_mov_b32 v63, 0\n"
    "v_mov_b32 v64, 0\n"
    "v_mov_b32 v65, 0\n"
    "v_mov_b32 v66, 0\n"
    "v_mov_b32 v67, 0\n"
    "v_mov_b32 v68, 0\n"
    "v_mov_b32 v69, 0\n"
    "v_mov_b32 v70, 0\n"
    "v_mov_b32 v71, 0\n"
    "v_mov_b32 v72, 0\n"
    "v_mov_b32 v73, 0\n"
    "v_mov_b32 v74, 0\n"
    "v_mov_b32 v75, 0\n"
    "v_mov_b32 v76, 0\n"
    "v_mov_b32 v77, 0\n"
    "v_mov_b32 v78, 0\n"
    "v_mov_b32 v79, 0\n"
    "s_mov_b32 s52, 0\n"
    "L_k3_%=:\n"
    "buffer_load_dwordx4 v[116:119], v152, s[40:43], s50 offen\n"
    "buffer_load_dwordx4 v[120:123], v153, s[40:43], s50 offen\n"
    "buffer_load_dwordx4 v[124:127], v154, s[40:43], s50 offen\n"
    "buffer_load_dwordx4 v[128:131], v155, s[40:43], s50 offen\n"
    "buffer_load_dwordx4 v[132:135], v152, s[44:47], s51 offen\n"
    "buffer_load_dwordx4 v[136:139], v153, s[44:47], s51 offen\n"
    "buffer_load_dwordx4 v[140:143], v154, s[44:47], s51 offen\n"
    "buffer_load_dwordx4 v[144:147], v155, s[44:47], s51 offen\n"
    "buffer_load_dwordx4 v[148:151], v156, s[44:47], s51 offen\n"
    "s_add_u32 s50, s50, 8192\n"
    "s_add_u32 s51, s51, 8192\n"
    "s_waitcnt vmcnt(9)\n"
    "v_mfma_f32_16x16x32_bf16 v[0:3], v[96:99], v[80:83], v[0:3]\n"
    "v_mfma_f32_16x16x32_bf16 v[4:7], v[96:99], v[84:87], v[4:7]\n"
    "v_mfma_f32_16x16x32_bf16 v[8:11], v[96:99], v[88:91], v[8:11]\n"
    "v_mfma_f32_16x16x32_bf16 v[12:15], v[96:99], v[92:95], v[12:15]\n"
    "v_mfma_f32_16x16x32_bf16 v[16:19], v[100:103], v[80:83], v[16:19]\n"
    "v_mfma_f32_16x16x32_bf16 v[20:23], v[100:103], v[84:87], v[20:23]\n"
    "v_mfma_f32_16x16x32_bf16 v[24:27], v[100:103], v[88:91], v[24:27]\n"
    "v_mfma_f32_16x16x32_bf16 v[28:31], v[100:103], v[92:95], v[28:31]\n"
    "v_mfma_f32_16x16x32_bf16 v[32:35], v[104:107], v[80:83], v[32:35]\n"
    "v_mfma_f32_16x16x32_bf16 v[36:39], v[104:107], v[84:87], v[36:39]\n"
    "v_mfma_f32_16x16x32_bf16 v[40:43], v[104:107], v[88:91], v[40:43]\n"
    "v_mfma_f32_16x16x32_bf16 v[44:47], v[104:107], v[92:95], v[44:47]\n"
    "v_mfma_f32_16x16x32_bf16 v[48:51], v[108:111], v[80:83], v[48:51]\n"
    "v_mfma_f32_16x16x32_bf16 v[52:55], v[108:111], v[84:87], v[52:55]\n"
    "v_mfma_f32_16x16x32_bf16 v[56:59], v[108:111], v[88:91], v[56:59]\n"
    "v_mfma_f32_16x16x32_bf16 v[60:63], v[108:111], v[92:95], v[60:63]\n"
    "v_mfma_f32_16x16x32_bf16 v[64:67], v[112:115], v[80:83], v[64:67]\n"
    "v_mfma_f32_16x16x32_bf16 v[68:71], v[112:115], v[84:87], v[68:71]\n"
    "v_mfma_f32_16x16x32_bf16 v[72:75], v[112:115], v[88:91], v[72:75]\n"
    "v_mfma_f32_16x16x32_bf16 v[76:79], v[112:115], v[92:95], v[76:79]\n"
    "buffer_load_dwordx4 v[80:83], v152, s[40:43], s50 offen\n"
    "buffer_load_dwordx4 v[84:87], v153, s[40:43], s50 offen\n"
    "buffer_load_dwordx4 v[88:91], v154, s[40:43], s50 offen\n"
    "buffer_load_dwordx4 v[92:95], v155, s[40:43], s50 offen\n"
    "buffer_load_dwordx4 v[96:99], v152, s[44:47], s51 offen\n"
    "buffer_load_dwordx4 v[100:103], v153, s[44:47], s51 offen\n"
    "buffer_load_dwordx4 v[104:107], v154, s[44:47], s51 offen\n"
    "buffer_load_dwordx4 v[108:111], v155, s[44:47], s51 offen\n"
    "buffer_load_dwordx4 v[112:115], v156, s[44:47], s51 offen\n"
    "s_add_u32 s50, s50, 8192\n"
    "s_add_u32 s51, s51, 8192\n"
    "s_waitcnt vmcnt(9)\n"
    "v_mfma_f32_16x16x32_bf16 v[0:3], v[132:135], v[116:119], v[0:3]\n"
    "v_mfma_f32_16x16x32_bf16 v[4:7], v[132:135], v[120:123], v[4:7]\n"
    "v_mfma_f32_16x16x32_bf16 v[8:11], v[132:135], v[124:127], v[8:11]\n"
    "v_mfma_f32_16x16x32_bf16 v[12:15], v[132:135], v[128:131], v[12:15]\n"
    "v_mfma_f32_16x16x32_bf16 v[16:19], v[136:139], v[116:119], v[16:19]\n"
    "v_mfma_f32_16x16x32_bf16 v[20:23], v[136:139], v[120:123], v[20:23]\n"
    "v_mfma_f32_16x16x32_bf16 v[24:27], v[136:139], v[124:127], v[24:27]\n"
    "v_mfma_f32_16x16x32_bf16 v[28:31], v[136:139], v[128:131], v[28:31]\n"
    "v_mfma_f32_16x16x32_bf16 v[32:35], v[140:143], v[116:119], v[32:35]\n"
    "v_mfma_f32_16x16x32_bf16 v[36:39], v[140:143], v[120:123], v[36:39]\n"
    "v_mfma_f32_16x16x32_bf16 v[40:43], v[140:143], v[124:127], v[40:43]\n"
    "v_mfma_f32_16x16x32_bf16 v[44:47], v[140:143], v[128:131], v[44:47]\n"
    "v_mfma_f32_16x16x32_bf16 v[48:51], v[144:147], v[116:119], v[48:51]\n"
    "v_mfma_f32_16x16x32_bf16 v[52:55], v[144:147], v[120:123], v[52:55]\n"
    "v_mfma_f32_16x16x32_bf16 v[56:59], v[144:147], v[124:127], v[56:59]\n"
    "v_mfma_f32_16x16x32_bf16 v[60:63], v[144:147], v[128:131], v[60:63]\n"
    "v_mfma_f32_16x16x32_bf16 v[64:67], v[148:151], v[116:119], v[64:67]\n"
    "v_mfma_f32_16x16x32_bf16 v[68:71], v[148:151], v[120:123], v[68:71]\n"
    "v_mfma_f32_16x16x32_bf16 v[72:75], v[148:151], v[124:127], v[72:75]\n"
    "v_mfma_f32_16x16x32_bf16 v[76:79], v[148:151], v[128:131], v[76:79]\n"
    "s_add_u32 s52, s52, 2\n"
    "s_cmp_lt_u32 s52, 16\n"
    "s_cbranch_scc1 L_k3_%=\n"
    "v_and_b32 v152, 15, v158\n"
    "v_mul_u32_u24 v152, 336, v152\n"
    "v_lshrrev_b32 v153, 4, v158\n"
    "v_lshlrev_b32 v153, 4, v153\n"
    "v_add_u32 v152, v152, v153\n"
    "s_and_b32 s53, %2, 3\n"
    "s_mul_i32 s53, s53, 21504\n"
    "v_add_u32 v152, s53, v152\n"
    "s_waitcnt vmcnt(0)\n"
    "s_nop 7\n"
    "s_cmp_lt_u32 %2, 4\n"
    "s_cbranch_scc1 L_k3lo_%=\n"
    "ds_write_b128 v152, v[0:3]\n"
    "ds_write_b128 v152, v[4:7] offset:5376\n"
    "ds_write_b128 v152, v[8:11] offset:10752\n"
    "ds_write_b128 v152, v[12:15] offset:16128\n"
    "ds_write_b128 v152, v[16:19] offset:64\n"
    "ds_write_b128 v152, v[20:23] offset:5440\n"
    "ds_write_b128 v152, v[24:27] offset:10816\n"
    "ds_write_b128 v152, v[28:31] offset:16192\n"
    "ds_write_b128 v152, v[32:35] offset:128\n"
    "ds_write_b128 v152, v[36:39] offset:5504\n"
    "ds_write_b128 v152, v[40:43] offset:10880\n"
    "ds_write_b128 v152, v[44:47] offset:16256\n"
    "ds_write_b128 v152, v[48:51] offset:192\n"
    "ds_write_b128 v152, v[52:55] offset:5568\n"
    "ds_write_b128 v152, v[56:59] offset:10944\n"
    "ds_write_b128 v152, v[60:63] offset:16320\n"
    "ds_write_b128 v152, v[64:67] offset:256\n"
    "ds_write_b128 v152, v[68:71] offset:5632\n"
    "ds_write_b128 v152, v[72:75] offset:11008\n"
    "ds_write_b128 v152, v[76:79] offset:16384\n"
    "s_waitcnt lgkmcnt(0)\n"
    "s_barrier\n"
    "s_branch L_k3end_%=\n"
    "L_k3lo_%=:\n"
    "s_barrier\n"
    "ds_read_b128 v[80:83], v152\n"
    "ds_read_b128 v[84:87], v152 offset:5376\n"
    "ds_read_b128 v[88:91], v152 offset:10752\n"
    "ds_read_b128 v[92:95], v152 offset:16128\n"
    "ds_read_b128 v[96:99], v152 offset:64\n"
    "ds_read_b128 v[100:103], v152 offset:5440\n"
    "ds_read_b128 v[104:107], v152 offset:10816\n"
    "ds_read_b128 v[108:111], v152 offset:16192\n"
    "ds_read_b128 v[112:115], v152 offset:128\n"
    "ds_read_b128 v[116:119], v152 offset:5504\n"
    "s_waitcnt lgkmcnt(0)\n"
    "v_add_f32 v0, v0, v80\n"
    "v_add_f32 v1, v1, v81\n"
    "v_add_f32 v2, v2, v82\n"
    "v_add_f32 v3, v3, v83\n"
    "v_add_f32 v4, v4, v84\n"
    "v_add_f32 v5, v5, v85\n"
    "v_add_f32 v6, v6, v86\n"
    "v_add_f32 v7, v7, v87\n"
    "v_add_f32 v8, v8, v88\n"
    "v_add_f32 v9, v9, v89\n"
    "v_add_f32 v10, v10, v90\n"
    "v_add_f32 v11, v11, v91\n"
    "v_add_f32 v12, v12, v92\n"
    "v_add_f32 v13, v13, v93\n"
    "v_add_f32 v14, v14, v94\n"
    "v_add_f32 v15, v15, v95\n"
    "v_add_f32 v16, v16, v96\n"
    "v_add_f32 v17, v17, v97\n"
    "v_add_f32 v18, v18, v98\n"
    "v_add_f32 v19, v19, v99\n"
    "v_add_f32 v20, v20, v100\n"
    "v_add_f32 v21, v21, v101\n"
    "v_add_f32 v22, v22, v102\n"
    "v_add_f32 v23, v23, v103\n"
    "v_add_f32 v24, v24, v104\n"
    "v_add_f32 v25, v25, v105\n"
    "v_add_f32 v26, v26, v106\n"
    "v_add_f32 v27, v27, v107\n"
    "v_add_f32 v28, v28, v108\n"
    "v_add_f32 v29, v29, v109\n"
    "v_add_f32 v30, v30, v110\n"
    "v_add_f32 v31, v31, v111\n"
    "v_add_f32 v32, v32, v112\n"
    "v_add_f32 v33, v33, v113\n"
    "v_add_f32 v34, v34, v114\n"
    "v_add_f32 v35, v35, v115\n"
    "v_add_f32 v36, v36, v116\n"
    "v_add_f32 v37, v37, v117\n"
    "v_add_f32 v38, v38, v118\n"
    "v_add_f32 v39, v39, v119\n"
    "ds_write_b128 v152, v[0:3]\n"
    "ds_write_b128 v152, v[4:7] offset:5376\n"
    "ds_write_b128 v152, v[8:11] offset:10752\n"
    "ds_write_b128 v152, v[12:15] offset:16128\n"
    "ds_write_b128 v152, v[16:19] offset:64\n"
    "ds_write_b128 v152, v[20:23] offset:5440\n"
    "ds_write_b128 v152, v[24:27] offset:10816\n"
    "ds_write_b128 v152, v[28:31] offset:16192\n"
    "ds_write_b128 v152, v[32:35] offset:128\n"
    "ds_write_b128 v152, v[36:39] offset:5504\n"
    "ds_read_b128 v[80:83], v152 offset:10880\n"
    "ds_read_b128 v[84:87], v152 offset:16256\n"
    "ds_read_b128 v[88:91], v152 offset:192\n"
    "ds_read_b128 v[92:95], v152 offset:5568\n"
    "ds_read_b128 v[96:99], v152 offset:10944\n"
    "ds_read_b128 v[100:103], v152 offset:16320\n"
    "ds_read_b128 v[104:107], v152 offset:256\n"
    "ds_read_b128 v[108:111], v152 offset:5632\n"
    "ds_read_b128 v[112:115], v152 offset:11008\n"
    "ds_read_b128 v[116:119], v152 offset:16384\n"
    "s_waitcnt lgkmcnt(0)\n"
    "v_add_f32 v40, v40, v80\n"
    "v_add_f32 v41, v41, v81\n"
    "v_add_f32 v42, v42, v82\n"
    "v_add_f32 v43, v43, v83\n"
    "v_add_f32 v44, v44, v84\n"
    "v_add_f32 v45, v45, v85\n"
    "v_add_f32 v46, v46, v86\n"
    "v_add_f32 v47, v47, v87\n"
    "v_add_f32 v48, v48, v88\n"
    "v_add_f32 v49, v49, v89\n"
    "v_add_f32 v50, v50, v90\n"
    "v_add_f32 v51, v51, v91\n"
    "v_add_f32 v52, v52, v92\n"
    "v_add_f32 v53, v53, v93\n"
    "v_add_f32 v54, v54, v94\n"
    "v_add_f32 v55, v55, v95\n"
    "v_add_f32 v56, v56, v96\n"
    "v_add_f32 v57, v57, v97\n"
    "v_add_f32 v58, v58, v98\n"
    "v_add_f32 v59, v59, v99\n"
    "v_add_f32 v60, v60, v100\n"
    "v_add_f32 v61, v61, v101\n"
    "v_add_f32 v62, v62, v102\n"
    "v_add_f32 v63, v63, v103\n"
    "v_add_f32 v64, v64, v104\n"
    "v_add_f32 v65, v65, v105\n"
    "v_add_f32 v66, v66, v106\n"
    "v_add_f32 v67, v67, v107\n"
    "v_add_f32 v68, v68, v108\n"
    "v_add_f32 v69, v69, v109\n"
    "v_add_f32 v70, v70, v110\n"
    "v_add_f32 v71, v71, v111\n"
    "v_add_f32 v72, v72, v112\n"
    "v_add_f32 v73, v73, v113\n"
    "v_add_f32 v74, v74, v114\n"
    "v_add_f32 v75, v75, v115\n"
    "v_add_f32 v76, v76, v116\n"
    "v_add_f32 v77, v77, v117\n"
    "v_add_f32 v78, v78, v118\n"
    "v_add_f32 v79, v79, v119\n"
    "ds_write_b128 v152, v[40:43] offset:10880\n"
    "ds_write_b128 v152, v[44:47] offset:16256\n"
    "ds_write_b128 v152, v[48:51] offset:192\n"
    "ds_write_b128 v152, v[52:55] offset:5568\n"
    "ds_write_b128 v152, v[56:59] offset:10944\n"
    "ds_write_b128 v152, v[60:63] offset:16320\n"
    "ds_write_b128 v152, v[64:67] offset:256\n"
    "ds_write_b128 v152, v[68:71] offset:5632\n"
    "ds_write_b128 v152, v[72:75] offset:11008\n"
    "ds_write_b128 v152, v[76:79] offset:16384\n"
    "s_waitcnt lgkmcnt(0)\n"
    "L_k3end_%=:\n"
    "s_setprio 0\n"
      :: "s"((unsigned)pw), "s"((unsigned)(pw >> 32)), "s"(w), "s"(rb), "s"(cbk), "s"(WS_CF), "s"(WS_WOF)
      : "v0", "v1", "v2", "v3", "v4", "v5", "v6", "v7", "v8", "v9", "v10", "v11", "v12", "v13", "v14", "v15", "v16", "v17", "v18", "v19", "v20", "v21", "v22", "v23", "v24", "v25", "v26", "v27", "v28", "v29", "v30", "v31", "v32", "v33", "v34", "v35", "v36", "v37", "v38", "v39", "v40", "v41", "v42", "v43", "v44", "v45", "v46", "v47", "v48", "v49", "v50", "v51", "v52", "v53", "v54", "v55", "v56", "v57", "v58", "v59", "v60", "v61", "v62", "v63", "v64", "v65", "v66", "v67", "v68", "v69", "v70", "v71", "v72", "v73", "v74", "v75", "v76", "v77", "v78", "v79", "v80", "v81", "v82", "v83", "v84", "v85", "v86", "v87", "v88", "v89", "v90", "v91", "v92", "v93", "v94", "v95", "v96", "v97", "v98", "v99", "v100", "v101", "v102", "v103", "v104", "v105", "v106", "v107", "v108", "v109", "v110", "v111", "v112", "v113", "v114", "v115", "v116", "v117", "v118", "v119", "v120", "v121", "v122", "v123", "v124", "v125", "v126", "v127", "v128", "v129", "v130", "v131", "v132", "v133", "v134", "v135", "v136", "v137", "v138", "v139", "v140", "v141", "v142", "v143", "v144", "v145", "v146", "v147", "v148", "v149", "v150", "v151", "v152", "v153", "v154", "v155", "v156", "v157", "v158", "v159", "s40", "s41", "s42", "s43", "s44", "s45", "s46", "s47", "s48", "s49", "s50", "s51", "s52", "s53", "s54", "s55", "vcc", "scc", "memory");
  } else {
    unsigned ln; asm volatile("v_mbcnt_lo_u32_b32 %0, -1, 0\n\tv_mbcnt_hi_u32_b32 %0, -1, %0" : "=v"(ln));
    char* reg = smem + OUT_RED_BYTES + (w - 8) * CONV_LDS_W;
    __builtin_amdgcn_s_setprio(3);
    if (b < 160) conv_channel(b * 4 + (w - 8), ln, ssk, ssd, ws, reg, (unsigned*)(smem + OUT_RED_BYTES + 4 * CONV_LDS_W));
    const float gq = ((const float*)(ws + WS_G))[rb * 64 + ln];
    for (int k = 0; k < 3; ++k) { const int c = (w - 8) + 4 * k; if (c < 10) xv[k] = *(const u32x4_t*)(x + (size_t)(rb * 64 + ln) * 640 + cbk * 80 + c * 8); }
    if (w == 8) {
      gu32* cnt = (gu32*)(ws + WS_FLAG) + cbk * 32;
      const unsigned long long t0 = __builtin_amdgcn_s_memrealtime();
      if (b >= 160) __builtin_amdgcn_s_sleep(127);
      while ((unsigned)__builtin_amdgcn_readfirstlane(__hip_atomic_load(cnt, __ATOMIC_RELAXED, __HIP_MEMORY_SCOPE_AGENT)) < 20u) {
        if (__builtin_amdgcn_s_memrealtime() - t0 > 2000000ull) break;
        __builtin_amdgcn_s_sleep(24); }
      __builtin_amdgcn_fence(__ATOMIC_ACQUIRE, "wavefront");
      __amdgpu_buffer_rsrc_t zr = __builtin_amdgcn_make_buffer_rsrc((void*)(ws + WS_ZT), 0, 640 * 4096, 0x00020000);
      u16* zs = (u16*)reg;
      u32x4_t zv[10];
      for (int k2 = 0; k2 < 10; ++k2) { const int i = ln + 64 * k2, d = i >> 3, c = i & 7; zv[k2] = __builtin_amdgcn_raw_buffer_load_b128(zr, ((cbk * 80 + d) * 2048 + rb * 64 + c * 8) * 2, 0, 16); }
      for (int k2 = 0; k2 < 10; ++k2) { const int i = ln + 64 * k2, d = i >> 3, c = i & 7; *(u32x4_t*)(zs + d * 72 + c * 8) = zv[k2]; }
    }
    __builtin_amdgcn_s_setprio(0);
    __syncthreads();
    { const u16* zs = (const u16*)(smem + OUT_RED_BYTES); char* obp = smem + OUT_RED_BYTES + CONV_LDS_W; const float omg = rbf(1.f - gq);
      for (int k = 0; k < 3; ++k) { const int c = (w - 8) + 4 * k; if (c < 10) { float o[8];
        for (int e = 0; e < 8; ++e) { const float xf = (e & 1) ? hi16(xv[k][e >> 1]) : lo16(xv[k][e >> 1]); o[e] = omg * rbf(xf * bf2f(zs[(c * 8 + e) * 72 + ln])); }
        *(u32x4_t*)(obp + (c * 64 + ln) * 16) = (u32x4_t){cvtpk(o[0], o[1]), cvtpk(o[2], o[3]), cvtpk(o[4], o[5]), cvtpk(o[6], o[7])}; } }
      if (w == 8) *(float*)(obp + 10240 + ln * 4) = gq; }
  }
  __syncthreads();
  if (w < 10) {
    unsigned ln; asm volatile("v_mbcnt_lo_u32_b32 %0, -1, 0\n\tv_mbcnt_hi_u32_b32 %0, -1, %0" : "=v"(ln));
    const float* red = (const float*)smem + ln * 84 + w * 8; const char* obp = smem + OUT_RED_BYTES + CONV_LDS_W;
    f32x4_t a0 = {}, a1 = {};
    for (int ww = 0; ww < 4; ++ww) { a0 += *(const f32x4_t*)(red + ww * 64 * 84); a1 += *(const f32x4_t*)(red + ww * 64 * 84 + 4); }
    const u32x4_t ob = *(const u32x4_t*)(obp + (w * 64 + ln) * 16); const float g = *(const float*)(obp + 10240 + ln * 4);
    float r8[8];
    for (int e = 0; e < 8; ++e) { const float av = rbf(e < 4 ? a0[e] : a1[e - 4]); r8[e] = rbf(g * av) + ((e & 1) ? hi16(ob[e >> 1]) : lo16(ob[e >> 1])); }
    *(u32x4_t*)(out + (size_t)(rb * 64 + ln) * 640 + cbk * 80 + w * 8) = (u32x4_t){cvtpk(r8[0], r8[1]), cvtpk(r8[2], r8[3]), cvtpk(r8[4], r8[5]), cvtpk(r8[6], r8[7])};
  }
}

extern "C" void kernel_launch(void* const* d_in, const int* in_sizes, int n_in, void* d_out, int out_size, void* d_ws, size_t ws_size, hipStream_t stream) {
  (void)hipFuncSetAttribute((const void*)k_main, hipFuncAttributeMaxDynamicSharedMemorySize, 163840);
  (void)hipFuncSetAttribute((const void*)k_out, hipFuncAttributeMaxDynamicSharedMemorySize, OUT_LDS);
  hipLaunchKernelGGL(k_main, dim3(256), dim3(256), 163840, stream, d_in[0], d_in[1], d_in[2], d_in[3], d_in[4], d_ws, d_in[5],
                     d_in[7], d_in[8], d_in[9], d_in[12]);
  hipLaunchKernelGGL(k_out, dim3(256), dim3(768), OUT_LDS, stream, (const u16*)d_in[0], (const u16*)d_in[10], (const u16*)d_in[11], (char*)d_ws, (u16*)d_out);
}
```
